```python
import torch

def _softmax(x, axis=-1):
    return torch.softmax(x, dim=axis)
def _gelu(x):
    return torch.nn.functional.gelu(x, approximate="tanh")
def _silu(x):
    return torch.nn.functional.silu(x)
def _sigmoid(x):
    return torch.sigmoid(x)
def _layernorm(x, axis=-1, eps=1e-5):
    m = x.mean(dim=axis, keepdim=True)
    v = x.var(dim=axis, keepdim=True, unbiased=False)
    return (x - m) / torch.sqrt(v + eps)
def _scatter_add(tgt, idx, src, axis=0):
    return tgt.clone().index_add_(axis, idx, src)
def _segment_sum(src, seg, n):
    out = torch.zeros((n,) + src.shape[1:], dtype=src.dtype)
    return out.index_add_(0, seg, src)
def _pad(x, pw):
    flat = tuple(v for pair in reversed(pw) for v in pair)
    return torch.nn.functional.pad(x, flat)

kernel_name = 'sequential_preLN__sliding_local_dilated__mixer_channel_mlp__prompt'

def setup_inputs():

    _w = (lambda a: (lambda t: t.to(torch.bfloat16) if t.is_floating_point() else t)(torch.as_tensor(a)))
    def _A_setup():
        B, S, D = 1, 2048, 576
        return {
            "x": _w((lambda *s: torch.randn(*s, dtype=torch.bfloat16) * 0.05)(B, S, D)),
            "wqkv": _w((lambda *s: torch.randn(*s, dtype=torch.bfloat16) * 0.05)(D, 3 * D)),
            "wo": _w((lambda *s: torch.randn(*s, dtype=torch.bfloat16) * 0.05)(D, D)),
        }
    _A_in = _A_setup()

    def _B_setup():
        B, S, D, F = 1, 2048, 576, 4608
        return {
            "x": _w((lambda *s: torch.randn(*s, dtype=torch.bfloat16) * 0.05)(B, S, D)),
            "w1": _w((lambda *s: torch.randn(*s, dtype=torch.bfloat16) * 3.2)(D, F) * (D ** -0.5)),
            "b1": _w((lambda *s: torch.randn(*s, dtype=torch.bfloat16) * 1.6)(F)),
            "w2": _w((lambda *s: torch.randn(*s, dtype=torch.bfloat16) * 0.2)(F, D) * (F ** -0.5)),
            "b2": _w((lambda *s: torch.randn(*s, dtype=torch.bfloat16) * 0.4)(D)),
        }
    _B_in = _B_setup()

    return {
        "A_x": _A_in["x"],
        "A_wqkv": _A_in["wqkv"],
        "A_wo": _A_in["wo"],
        "B_x": _B_in["x"],
        "B_w1": _B_in["w1"],
        "B_b1": _B_in["b1"],
        "B_w2": _B_in["w2"],
        "B_b2": _B_in["b2"],
    }

def reference(A_x, A_wqkv, A_wo, B_x, B_w1, B_b1, B_w2, B_b2):
    def _A_ref(x, wqkv, wo):
        B, S, D = x.shape
        H = 32; Dh = D // H
        W, dil = 8, 2
        qkv = (lambda x, *d: x.permute(*d))((x @ wqkv).reshape(B, S, 3, H, Dh), 2, 0, 3, 1, 4)
        q, k, v = qkv[0], qkv[1], qkv[2]
        scale = Dh ** -0.5
        pos = torch.arange(S)
        off = torch.arange(W) * dil
        idx = pos[:, None] - off[None, :]
        mask = idx >= 0
        idx_c = torch.where(mask, idx, 0).reshape(-1)
        kg = k[:, :, idx_c, :].reshape(B, H, S, W, Dh)
        vg = v[:, :, idx_c, :].reshape(B, H, S, W, Dh)
        logits = torch.einsum("bhsd,bhswd->bhsw", q, kg) * scale
        logits = torch.where(mask[None, None, :, :], logits, -1e9)
        attn = _softmax(logits, axis=-1)
        o = torch.einsum("bhsw,bhswd->bhsd", attn, vg)
        return (lambda x, *d: x.permute(*d))(o, 0, 2, 1, 3).reshape(B, S, D) @ wo

    def _B_ref(x, w1, b1, w2, b2):
        y = _layernorm(x, axis=-1)
        h = _gelu(torch.einsum("bsd,df->bsf", y, w1) + b1)
        return torch.einsum("bsf,fd->bsd", h, w2) + b2

    x = A_x
    h = x + _A_ref(_layernorm(x, axis=-1), A_wqkv, A_wo)
    h2 = h + _B_ref(_layernorm(h, axis=-1), B_w1, B_b1, B_w2, B_b2)
    return h2
```

```cpp
#include <hip/hip_runtime.h>
#include <hip/hip_bf16.h>

typedef __attribute__((ext_vector_type(8))) short s8v;
typedef __attribute__((ext_vector_type(4))) short s4v;
typedef __attribute__((ext_vector_type(4))) short v4i16_t;
typedef __attribute__((ext_vector_type(4))) float f4v;
typedef __attribute__((ext_vector_type(2))) float f2v;
typedef __attribute__((ext_vector_type(4))) unsigned u4v;
typedef __attribute__((ext_vector_type(2))) unsigned u2v;
typedef unsigned short bf16_t;

constexpr int S_ = 2048, D_ = 576, NQKV = 1728, F_ = 4608;

__device__ __forceinline__ void glds16(const void* g, void* l) {
    __builtin_amdgcn_global_load_lds((const __attribute__((address_space(1))) void*)g,
                                     (__attribute__((address_space(3))) void*)l, 16, 0, 0);
}
__device__ __forceinline__ s4v tr_read(const char* p) {
    return __builtin_bit_cast(s4v, __builtin_amdgcn_ds_read_tr16_b64_v4i16((__attribute__((address_space(3))) v4i16_t*)p));
}
__device__ __forceinline__ s8v cat8(s4v a, s4v b) {
    return __builtin_shufflevector(a, b, 0, 1, 2, 3, 4, 5, 6, 7);
}
__device__ __forceinline__ float bf_lo(unsigned u) { return __builtin_bit_cast(float, u << 16); }
__device__ __forceinline__ float bf_hi(unsigned u) { return __builtin_bit_cast(float, u & 0xffff0000u); }
__device__ __forceinline__ float bf2f(bf16_t v) { return __builtin_bit_cast(float, (unsigned)v << 16); }
__device__ __forceinline__ unsigned pack_bf2(float a, float b) {
    __hip_bfloat16 x = __float2bfloat16(a), y = __float2bfloat16(b);
    return (unsigned)__builtin_bit_cast(unsigned short, x) | ((unsigned)__builtin_bit_cast(unsigned short, y) << 16);
}
#define MFMA16(a, b, c) __builtin_amdgcn_mfma_f32_16x16x32_bf16(a, b, c, 0, 0, 0)
#define WAIT_V(n) asm volatile("s_waitcnt vmcnt(" #n ")" ::: "memory")
#define WAIT_L(n) asm volatile("s_waitcnt lgkmcnt(" #n ")" ::: "memory")

constexpr int K1_XN_BYTES = 9 * 80 * 128;
constexpr int K1_RING = 4;
constexpr int K1_LDS = K1_XN_BYTES + 7 * K1_RING * 2048;

__global__ __launch_bounds__(512, 2) void k1_qkv_attn(const bf16_t* __restrict__ x, const bf16_t* __restrict__ wqkv,
                                                      const bf16_t* __restrict__ w1, bf16_t* __restrict__ o,
                                                      float* __restrict__ cs1) {
    extern __shared__ __attribute__((aligned(16))) char smem[];
    const int tid = threadIdx.x, lane = tid & 63;
    const int wave = __builtin_amdgcn_readfirstlane(tid >> 6);
    const int hg = blockIdx.x & 7, rt = blockIdx.x >> 3;
    const int s0 = rt * 64;

    const char* wsrc[2];
    char* ring = smem + K1_XN_BYTES + wave * (K1_RING * 2048);
    if (wave < 7) {
#pragma unroll
        for (int i = 0; i < 2; i++) {
            int k = i * 16 + (lane >> 2);
            int c4 = lane & 3;
            int t = (c4 >> 1) ^ ((k >> 3) & 1);
            int pc = wave * 4 + t * 2 + (c4 & 1);
            if (pc > 26) pc = 26;
            int sec = pc / 9, wi = pc - sec * 9;
            int col = sec * 576 + hg * 72 + wi * 8;
            wsrc[i] = (const char*)(wqkv + (size_t)k * NQKV + col);
        }
    }
    u4v xv[9];
    {
        int r = wave * 8 + (lane >> 3);
        int s = s0 - 16 + r; if (s < 0) s = 0;
        const u4v* p = (const u4v*)(x + (size_t)s * D_) + (lane & 7);
#pragma unroll
        for (int i = 0; i < 9; i++) xv[i] = p[i * 8];
    }
    if (wave < 7) {
#pragma unroll
        for (int st = 0; st < 3; st++) {
#pragma unroll
            for (int i = 0; i < 2; i++) glds16(wsrc[i] + (size_t)st * 32 * NQKV * 2, ring + st * 2048 + i * 1024);
        }
    }
    auto ln_pass = [&](int r) {
        float sum = 0.f;
#pragma unroll
        for (int i = 0; i < 9; i++)
#pragma unroll
            for (int j = 0; j < 4; j++) sum += bf_lo(xv[i][j]) + bf_hi(xv[i][j]);
        sum += __shfl_xor(sum, 1); sum += __shfl_xor(sum, 2); sum += __shfl_xor(sum, 4);
        float mean = sum * (1.f / 576.f);
        float sq = 0.f;
#pragma unroll
        for (int i = 0; i < 9; i++)
#pragma unroll
            for (int j = 0; j < 4; j++) {
                float a = bf_lo(xv[i][j]) - mean, b = bf_hi(xv[i][j]) - mean;
                sq += a * a + b * b;
            }
        sq += __shfl_xor(sq, 1); sq += __shfl_xor(sq, 2); sq += __shfl_xor(sq, 4);
        float rstd = rsqrtf(sq * (1.f / 576.f) + 1e-5f);
#pragma unroll
        for (int i = 0; i < 9; i++) {
            u4v ov;
#pragma unroll
            for (int j = 0; j < 4; j++)
                ov[j] = pack_bf2((bf_lo(xv[i][j]) - mean) * rstd, (bf_hi(xv[i][j]) - mean) * rstd);
            *(u4v*)(smem + i * (80 * 128) + r * 128 + (((lane & 7) ^ (r & 7)) << 4)) = ov;
        }
    };
    ln_pass(wave * 8 + (lane >> 3));
    if (wave < 2) {
        int r = 64 + wave * 8 + (lane >> 3);
        int s = s0 - 16 + r;
        const u4v* p = (const u4v*)(x + (size_t)s * D_) + (lane & 7);
#pragma unroll
        for (int i = 0; i < 9; i++) xv[i] = p[i * 8];
        ln_pass(r);
    }
    if (wave == 7 && blockIdx.x < 144) {
        const u4v* p = (const u4v*)(w1 + (size_t)(lane >> 2) * F_ + blockIdx.x * 32) + (lane & 3);
        float a[8];
#pragma unroll
        for (int j = 0; j < 8; j++) a[j] = 0.f;
#pragma unroll 6
        for (int i = 0; i < 36; i++) {
            u4v v = p[(size_t)i * 16 * F_ / 8];
#pragma unroll
            for (int j = 0; j < 4; j++) { a[2 * j] += bf_lo(v[j]); a[2 * j + 1] += bf_hi(v[j]); }
        }
#pragma unroll
        for (int j = 0; j < 8; j++) {
            float t = a[j];
            t += __shfl_xor(t, 4); t += __shfl_xor(t, 8); t += __shfl_xor(t, 16); t += __shfl_xor(t, 32);
            a[j] = t;
        }
        if (lane < 4) {
            float* q = cs1 + blockIdx.x * 32 + lane * 8;
            *(f4v*)q = f4v{a[0], a[1], a[2], a[3]};
            *(f4v*)(q + 4) = f4v{a[4], a[5], a[6], a[7]};
        }
    }
    __syncthreads();

    f4v acc[5][2];
#pragma unroll
    for (int m = 0; m < 5; m++) { acc[m][0] = f4v{0, 0, 0, 0}; acc[m][1] = f4v{0, 0, 0, 0}; }
    if (wave < 7) {
        const int g = lane >> 4, q = (lane & 15) >> 2, p = lane & 3;
        const int woff0 = (8 * g + q) * 64 + ((0 ^ (g & 1)) << 5) + 8 * p;
        const int woff1 = (8 * g + q) * 64 + ((1 ^ (g & 1)) << 5) + 8 * p;
        const int xr = lane & 15;
        for (int st = 0; st < 18; st++) {
            {
                int sn = st + 3; if (sn > 17) sn = 17;
                int slot = (st + 3) & 3;
#pragma unroll
                for (int i = 0; i < 2; i++) glds16(wsrc[i] + (size_t)sn * 32 * NQKV * 2, ring + slot * 2048 + i * 1024);
            }
            WAIT_V(6);
            const char* wb = ring + (st & 3) * 2048;
            s8v wf[2];
            wf[0] = cat8(tr_read(wb + woff0), tr_read(wb + woff0 + 256));
            wf[1] = cat8(tr_read(wb + woff1), tr_read(wb + woff1 + 256));
            const int kb = st >> 1, ch = (st & 1) * 4 + g;
            const char* xb = smem + kb * (80 * 128);
#pragma unroll
            for (int m = 0; m < 5; m++) {
                if (m == 0 && wave < 2) continue;
                int r = m * 16 + xr;
                s8v xf = *(const s8v*)(xb + r * 128 + ((ch ^ (r & 7)) << 4));
                acc[m][0] = MFMA16(xf, wf[0], acc[m][0]);
                acc[m][1] = MFMA16(xf, wf[1], acc[m][1]);
            }
        }
        WAIT_V(0);
    }
    __syncthreads();
    float* qkv = (float*)smem;
    if (wave < 7) {
#pragma unroll
        for (int m = 0; m < 5; m++)
#pragma unroll
            for (int t = 0; t < 2; t++) {
                int col = wave * 32 + t * 16 + (lane & 15);
                if (col < 216) *(f4v*)(qkv + col * 80 + m * 16 + 4 * (lane >> 4)) = acc[m][t];
            }
    }
    __syncthreads();
    if (wave < 4) {
        const int head = wave;
        const int r = 16 + lane;
        const int s = s0 + lane;
        const float* qp = qkv + (head * 18) * 80 + r;
        const float* kp = qkv + (72 + head * 18) * 80 + r;
        const float* vp = qkv + (144 + head * 18) * 80 + r;
        float qv[18];
#pragma unroll
        for (int d = 0; d < 18; d++) qv[d] = qp[d * 80];
        float lg[8];
        float mx = -3.0e38f;
#pragma unroll
        for (int w = 0; w < 8; w++) {
            float a = 0.f;
#pragma unroll
            for (int d = 0; d < 18; d++) a += qv[d] * kp[d * 80 - 2 * w];
            a *= 0.23570226039551584f;
            lg[w] = (s - 2 * w >= 0) ? a : -1e9f;
            mx = fmaxf(mx, lg[w]);
        }
        float den = 0.f;
#pragma unroll
        for (int w = 0; w < 8; w++) { lg[w] = __expf(lg[w] - mx); den += lg[w]; }
        float inv = 1.f / den;
        float ov[18];
#pragma unroll
        for (int d = 0; d < 18; d++) ov[d] = 0.f;
#pragma unroll
        for (int w = 0; w < 8; w++) {
            float pw = lg[w] * inv;
#pragma unroll
            for (int d = 0; d < 18; d++) ov[d] += pw * vp[d * 80 - 2 * w];
        }
        unsigned* op = (unsigned*)(o + (size_t)s * D_ + hg * 72 + head * 18);
#pragma unroll
        for (int d = 0; d < 9; d++) op[d] = pack_bf2(ov[2 * d], ov[2 * d + 1]);
    }
}

constexpr int K2_A = 9 * 64 * 128;
constexpr int K2_B = 576 * 144;
constexpr int K2_LDS = K2_A + K2_B + 64;

__global__ __launch_bounds__(256, 1) void k2_wo(const bf16_t* __restrict__ o, const bf16_t* __restrict__ wo,
                                                const bf16_t* __restrict__ x, bf16_t* __restrict__ h,
                                                f2v* __restrict__ stats) {
    extern __shared__ __attribute__((aligned(16))) char smem[];
    const int tid = threadIdx.x, lane = tid & 63;
    const int wave = __builtin_amdgcn_readfirstlane(tid >> 6);
    const int ct = blockIdx.x & 7, rt = blockIdx.x >> 3;
    const int r0 = rt * 64, c0 = ct * 72;
#pragma unroll
    for (int i = 0; i < 18; i++) {
        int a = wave * 18 + i;
        int kb = a >> 3, row = (a & 7) * 8 + (lane >> 3);
        int ch = (lane & 7) ^ (row & 7);
        glds16(o + (size_t)(r0 + row) * D_ + kb * 64 + ch * 8, smem + a * 1024);
    }
    for (int b = wave; b < 81; b += 4) {
        int ci = b * 64 + lane;
        int k = ci / 9, c = ci - k * 9;
        glds16(wo + (size_t)k * D_ + c0 + c * 8, smem + K2_A + b * 1024);
    }
    WAIT_V(0);
    __syncthreads();
    const int g = lane >> 4, q = (lane & 15) >> 2, p = lane & 3;
    f4v acc[5];
#pragma unroll
    for (int n = 0; n < 5; n++) acc[n] = f4v{0, 0, 0, 0};
    const int xr = wave * 16 + (lane & 15);
    const char* bb = smem + K2_A + (8 * g + q) * 144 + 8 * p;
#pragma unroll 2
    for (int st = 0; st < 18; st++) {
        const int kb = st >> 1, ch = (st & 1) * 4 + g;
        s8v xf = *(const s8v*)(smem + kb * (64 * 128) + xr * 128 + ((ch ^ (xr & 7)) << 4));
#pragma unroll
        for (int n = 0; n < 5; n++) {
            const char* wb = bb + st * 32 * 144 + n * 32;
            s8v wf = cat8(tr_read(wb), tr_read(wb + 4 * 144));
            acc[n] = MFMA16(wf, xf, acc[n]);
        }
    }
    const int row = r0 + xr;
    float hv[5][4];
    float sum = 0.f;
#pragma unroll
    for (int n = 0; n < 5; n++) {
        int col = n * 16 + 4 * g;
        if (col < 72) {
            u2v xx = *(const u2v*)(x + (size_t)row * D_ + c0 + col);
            unsigned p0 = pack_bf2(bf_lo(xx[0]) + acc[n][0], bf_hi(xx[0]) + acc[n][1]);
            unsigned p1 = pack_bf2(bf_lo(xx[1]) + acc[n][2], bf_hi(xx[1]) + acc[n][3]);
            *(u2v*)(h + (size_t)row * D_ + c0 + col) = u2v{p0, p1};
            hv[n][0] = bf_lo(p0); hv[n][1] = bf_hi(p0); hv[n][2] = bf_lo(p1); hv[n][3] = bf_hi(p1);
            sum += hv[n][0] + hv[n][1] + hv[n][2] + hv[n][3];
        } else {
            hv[n][0] = hv[n][1] = hv[n][2] = hv[n][3] = 0.f;
        }
    }
    sum += __shfl_xor(sum, 16); sum += __shfl_xor(sum, 32);
    float mean = sum * (1.f / 72.f);
    float m2 = 0.f;
#pragma unroll
    for (int n = 0; n < 5; n++) {
        int col = n * 16 + 4 * g;
        if (col < 72) {
#pragma unroll
            for (int j = 0; j < 4; j++) { float d = hv[n][j] - mean; m2 += d * d; }
        }
    }
    m2 += __shfl_xor(m2, 16); m2 += __shfl_xor(m2, 32);
    if (g == 0) stats[(size_t)row * 8 + ct] = f2v{sum, m2};
}

constexpr int K3_STAGE = 16384 + 36864;
constexpr int K3_LDS = 3 * K3_STAGE + 1024;

__device__ __forceinline__ float gelu_tanh(float v) {
    float t = v * v;
    float z = v * (-2.302208198f * 0.044715f * t - 2.302208198f);
    float e = __builtin_amdgcn_exp2f(z);
    return v * __builtin_amdgcn_rcpf(1.f + e);
}

__global__ __launch_bounds__(256, 1) void k3_up(const bf16_t* __restrict__ h, const bf16_t* __restrict__ w1,
                                                const bf16_t* __restrict__ b1, const float* __restrict__ cs1,
                                                const f2v* __restrict__ stats, bf16_t* __restrict__ h1) {
    extern __shared__ __attribute__((aligned(16))) char smem[];
    const int tid = threadIdx.x, lane = tid & 63;
    const int wave = __builtin_amdgcn_readfirstlane(tid >> 6);
    const int xcd = blockIdx.x & 7, idx = blockIdx.x >> 3;
    const int rt = (xcd & 1) * 8 + (idx & 7), ct = (xcd >> 1) * 4 + (idx >> 3);
    const int r0 = rt * 128, c0 = ct * 288;
    const int wm = wave >> 1, wn = wave & 1;
    const char* asrc[4];
    const char* bsrc[9];
#pragma unroll
    for (int i = 0; i < 4; i++) {
        int a = wave * 4 + i;
        int row = a * 8 + (lane >> 3);
        int ch = (lane & 7) ^ (row & 7);
        asrc[i] = (const char*)(h + (size_t)(r0 + row) * D_ + ch * 8);
    }
#pragma unroll
    for (int i = 0; i < 9; i++) {
        int bi = wave * 9 + i;
        int off = bi * 1024 + lane * 16;
        int k = off / 576, r = off - k * 576;
        int nt = (r >> 5) ^ ((k >> 3) & 1);
        bsrc[i] = (const char*)(w1 + (size_t)k * F_ + c0 + nt * 16 + ((r >> 4) & 1) * 8);
    }
    auto stage = [&](int st, int buf) {
        char* base = smem + buf * K3_STAGE;
#pragma unroll
        for (int i = 0; i < 4; i++) glds16(asrc[i] + st * 128, base + (wave * 4 + i) * 1024);
#pragma unroll
        for (int i = 0; i < 9; i++) glds16(bsrc[i] + (size_t)st * 64 * F_ * 2, base + 16384 + (wave * 9 + i) * 1024);
    };
    stage(0, 0);
    stage(1, 1);
    f2v* mu = (f2v*)(smem + 3 * K3_STAGE);
    if (tid < 128) {
        const f2v* sp = stats + (size_t)(r0 + tid) * 8;
        float s[8], m2[8], tot = 0.f;
#pragma unroll
        for (int i = 0; i < 8; i++) { f2v v = sp[i]; s[i] = v[0]; m2[i] = v[1]; tot += v[0]; }
        float mean = tot * (1.f / 576.f);
        float M2 = 0.f;
#pragma unroll
        for (int i = 0; i < 8; i++) { float d = s[i] * (1.f / 72.f) - mean; M2 += m2[i] + 72.f * d * d; }
        mu[tid] = f2v{mean, rsqrtf(M2 * (1.f / 576.f) + 1e-5f)};
    }
    const int g = lane >> 4, q = (lane & 15) >> 2, p = lane & 3;
    f4v acc[4][9];
#pragma unroll
    for (int m = 0; m < 4; m++)
#pragma unroll
        for (int n = 0; n < 9; n++) acc[m][n] = f4v{0, 0, 0, 0};
    const int dlt = (g & 1) ? 32 : 0;
    const int wb0 = 16384 + (8 * g + q) * 576 + wn * 9 * 32 + 8 * p;
    const int wbe = wb0 + ((wn & 1) ? -dlt : dlt);
    const int wbo = wb0 + ((wn & 1) ? dlt : -dlt);
    const int xr = lane & 15;
    for (int st = 0; st < 9; st++) {
        WAIT_V(13);
        __builtin_amdgcn_s_barrier();
        {
            int sn = st + 2; if (sn > 8) sn = 8;
            int buf = st + 2; buf = buf >= 3 ? buf % 3 : buf;
            stage(sn, buf);
        }
        const char* base = smem + (st % 3) * K3_STAGE;
#pragma unroll
        for (int ss = 0; ss < 2; ss++) {
            s8v xf[4], wf[9];
#pragma unroll
            for (int m = 0; m < 4; m++) {
                int r = wm * 64 + m * 16 + xr;
                xf[m] = *(const s8v*)(base + r * 128 + (((ss * 4 + g) ^ (r & 7)) << 4));
            }
#pragma unroll
            for (int n = 0; n < 9; n++) {
                const char* wb = base + ((n & 1) ? wbo : wbe) + n * 32 + ss * 32 * 576;
                wf[n] = cat8(tr_read(wb), tr_read(wb + 4 * 576));
            }
#pragma unroll
            for (int n = 0; n < 9; n++)
#pragma unroll
                for (int m = 0; m < 4; m++) acc[m][n] = MFMA16(wf[n], xf[m], acc[m][n]);
        }
    }
    WAIT_V(0);
    __syncthreads();
#pragma unroll
    for (int m = 0; m < 4; m++) {
        int rl = wm * 64 + m * 16 + xr;
        f2v ms = mu[rl];
        bf16_t* op = h1 + (size_t)(r0 + rl) * F_ + c0 + wn * 144 + 4 * g;
#pragma unroll
        for (int n = 0; n < 9; n++) {
            int col = c0 + wn * 144 + n * 16 + 4 * g;
            f4v c = *(const f4v*)(cs1 + col);
            u2v bb = *(const u2v*)(b1 + col);
            float v0 = gelu_tanh((acc[m][n][0] - ms[0] * c[0]) * ms[1] + bf_lo(bb[0]));
            float v1 = gelu_tanh((acc[m][n][1] - ms[0] * c[1]) * ms[1] + bf_hi(bb[0]));
            float v2 = gelu_tanh((acc[m][n][2] - ms[0] * c[2]) * ms[1] + bf_lo(bb[1]));
            float v3 = gelu_tanh((acc[m][n][3] - ms[0] * c[3]) * ms[1] + bf_hi(bb[1]));
            *(u2v*)(op + n * 16) = u2v{pack_bf2(v0, v1), pack_bf2(v2, v3)};
        }
    }
}

constexpr int K4_BST = 19456;
constexpr int K4_STAGE = 16384 + K4_BST;
constexpr int K4_NBUF = 4;
constexpr int K4_LDS = K4_NBUF * K4_STAGE;

__global__ __launch_bounds__(256, 1) void k4_down(const bf16_t* __restrict__ h1, const bf16_t* __restrict__ w2,
                                                  float* __restrict__ part) {
    extern __shared__ __attribute__((aligned(16))) char smem[];
    const int tid = threadIdx.x, lane = tid & 63;
    const int wave = __builtin_amdgcn_readfirstlane(tid >> 6);
    const int xcd = blockIdx.x & 7, idx = blockIdx.x >> 3;
    const int ks = xcd >> 1;
    const int rt = (xcd & 1) * 8 + (idx & 7), ct = idx >> 3;
    const int r0 = rt * 128, c0 = ct * 144, k0 = ks * 1152;
    const int wm = wave & 1, wk = wave >> 1;
    const char* asrc[4];
    const char* bsrc[5];
#pragma unroll
    for (int i = 0; i < 4; i++) {
        int a = wave * 4 + i;
        int row = a * 8 + (lane >> 3);
        int ch = (lane & 7) ^ (row & 7);
        asrc[i] = (const char*)(h1 + (size_t)(r0 + row) * F_ + k0 + ch * 8);
    }
#pragma unroll
    for (int i = 0; i < 5; i++) {
        int bi = wave + 4 * i; if (bi > 18) bi = 18;
        int off = bi * 1024 + lane * 16;
        int blk = off / 2432, r = off - blk * 2432;
        int kk = r / 288, cr = r - kk * 288;
        if (kk > 7) { kk = 7; cr = 0; }
        bsrc[i] = (const char*)(w2 + (size_t)(k0 + blk * 8 + kk) * D_ + c0) + cr;
    }
    auto stage = [&](int st, int buf) {
        char* base = smem + buf * K4_STAGE;
#pragma unroll
        for (int i = 0; i < 4; i++) glds16(asrc[i] + st * 128, base + (wave * 4 + i) * 1024);
#pragma unroll
        for (int i = 0; i < 5; i++) {
            int bi = wave + 4 * i; if (bi > 18) bi = 18;
            glds16(bsrc[i] + (size_t)st * 64 * D_ * 2, base + 16384 + bi * 1024);
        }
    };
    stage(0, 0); stage(1, 1); stage(2, 2);
    const int g = lane >> 4, q = (lane & 15) >> 2, p = lane & 3;
    f4v acc[4][9];
#pragma unroll
    for (int m = 0; m < 4; m++)
#pragma unroll
        for (int n = 0; n < 9; n++) acc[m][n] = f4v{0, 0, 0, 0};
    const int wb0 = 16384 + (4 * wk + g) * 2432 + q * 288 + 8 * p;
    const int xr = lane & 15;
    for (int st = 0; st < 18; st++) {
        WAIT_V(18);
        __builtin_amdgcn_s_barrier();
        {
            int sn = st + 3; if (sn > 17) sn = 17;
            stage(sn, (st + 3) & 3);
        }
        const char* base = smem + (st & 3) * K4_STAGE;
        s8v xf[4], wf[9];
#pragma unroll
        for (int m = 0; m < 4; m++) {
            int r = wm * 64 + m * 16 + xr;
            xf[m] = *(const s8v*)(base + r * 128 + (((wk * 4 + g) ^ (r & 7)) << 4));
        }
#pragma unroll
        for (int n = 0; n < 9; n++) {
            const char* wb = base + wb0 + n * 32;
            wf[n] = cat8(tr_read(wb), tr_read(wb + 4 * 288));
        }
#pragma unroll
        for (int n = 0; n < 9; n++)
#pragma unroll
            for (int m = 0; m < 4; m++) acc[m][n] = MFMA16(wf[n], xf[m], acc[m][n]);
    }
    WAIT_V(0);
    __syncthreads();
    f4v* red = (f4v*)smem;
    if (wk == 1) {
#pragma unroll
        for (int m = 0; m < 4; m++)
#pragma unroll
            for (int n = 0; n < 9; n++) red[((wm * 4 + m) * 9 + n) * 64 + lane] = acc[m][n];
    }
    __syncthreads();
    if (wk == 0) {
        float* pp = part + (size_t)ks * S_ * D_;
#pragma unroll
        for (int m = 0; m < 4; m++) {
            int row = r0 + wm * 64 + m * 16 + xr;
#pragma unroll
            for (int n = 0; n < 9; n++) {
                f4v v = acc[m][n] + red[((wm * 4 + m) * 9 + n) * 64 + lane];
                *(f4v*)(pp + (size_t)row * D_ + c0 + n * 16 + 4 * g) = v;
            }
        }
    }
}

__global__ __launch_bounds__(256) void k5_reduce(const float* __restrict__ part, const bf16_t* __restrict__ h,
                                                 const bf16_t* __restrict__ b2, bf16_t* __restrict__ out) {
    int i = blockIdx.x * 256 + threadIdx.x;
    size_t e = (size_t)i * 4;
    int col = (int)(e % D_);
    f4v a = *(const f4v*)(part + e);
#pragma unroll
    for (int k = 1; k < 4; k++) a += *(const f4v*)(part + (size_t)k * S_ * D_ + e);
    u2v hh = *(const u2v*)(h + e);
    u2v bb = *(const u2v*)(b2 + col);
    float v0 = bf_lo(hh[0]) + (a[0] + bf_lo(bb[0]));
    float v1 = bf_hi(hh[0]) + (a[1] + bf_hi(bb[0]));
    float v2 = bf_lo(hh[1]) + (a[2] + bf_lo(bb[1]));
    float v3 = bf_hi(hh[1]) + (a[3] + bf_hi(bb[1]));
    *(u2v*)(out + e) = u2v{pack_bf2(v0, v1), pack_bf2(v2, v3)};
}

extern "C" void kernel_launch(void* const* d_in, const int* in_sizes, int n_in,
                              void* d_out, int out_size, void* d_ws, size_t ws_size,
                              hipStream_t stream) {
    const bf16_t* x = (const bf16_t*)d_in[0];
    const bf16_t* wqkv = (const bf16_t*)d_in[1];
    const bf16_t* wo = (const bf16_t*)d_in[2];
    const bf16_t* w1 = (const bf16_t*)d_in[4];
    const bf16_t* b1 = (const bf16_t*)d_in[5];
    const bf16_t* w2 = (const bf16_t*)d_in[6];
    const bf16_t* b2 = (const bf16_t*)d_in[7];
    bf16_t* out = (bf16_t*)d_out;
    char* ws = (char*)d_ws;
    bf16_t* o = (bf16_t*)(ws);
    bf16_t* h = (bf16_t*)(ws + (4 << 20));
    f2v* stats = (f2v*)(ws + (8 << 20));
    float* cs1 = (float*)(ws + (9 << 20));
    bf16_t* h1 = (bf16_t*)(ws + (16 << 20));
    float* part = (float*)(ws + (48 << 20));

    hipFuncSetAttribute((const void*)k1_qkv_attn, hipFuncAttributeMaxDynamicSharedMemorySize, K1_LDS);
    hipFuncSetAttribute((const void*)k2_wo, hipFuncAttributeMaxDynamicSharedMemorySize, K2_LDS);
    hipFuncSetAttribute((const void*)k3_up, hipFuncAttributeMaxDynamicSharedMemorySize, K3_LDS);
    hipFuncSetAttribute((const void*)k4_down, hipFuncAttributeMaxDynamicSharedMemorySize, K4_LDS);

    k1_qkv_attn<<<256, 512, K1_LDS, stream>>>(x, wqkv, w1, o, cs1);
    k2_wo<<<256, 256, K2_LDS, stream>>>(o, wo, x, h, stats);
    k3_up<<<256, 256, K3_LDS, stream>>>(h, w1, b1, cs1, stats, h1);
    k4_down<<<256, 256, K4_LDS, stream>>>(h1, w2, part);
    k5_reduce<<<S_ * D_ / 4 / 256, 256, 0, stream>>>(part, h, b2, out);
}
```

```cpp
#include <hip/hip_runtime.h>
#include <hip/hip_bf16.h>

typedef __attribute__((ext_vector_type(8))) short s8v;
typedef __attribute__((ext_vector_type(4))) short s4v;
typedef __attribute__((ext_vector_type(4))) short v4i16_t;
typedef __attribute__((ext_vector_type(4))) float f4v;
typedef __attribute__((ext_vector_type(2))) float f2v;
typedef __attribute__((ext_vector_type(4))) unsigned u4v;
typedef __attribute__((ext_vector_type(2))) unsigned u2v;
typedef unsigned short bf16_t;

constexpr int S_ = 2048, D_ = 576, NQKV = 1728, F_ = 4608;

__device__ __forceinline__ unsigned lds_addr(const void* l) {
    return (unsigned)(size_t)(__attribute__((address_space(3))) const char*)l;
}
__device__ __forceinline__ void glds16(const void* g, unsigned dst) {
    unsigned keep;
    asm volatile("s_mov_b32 %0, m0\n\ts_mov_b32 m0, %2\n\ts_nop 0\n\tglobal_load_lds_dwordx4 %1, off\n\ts_mov_b32 m0, %0"
                 : "=&s"(keep) : "v"(g), "s"(dst) : "memory");
}
__device__ __forceinline__ void glds16s(const void* sbase, unsigned voff, unsigned dst) {
    unsigned keep;
    asm volatile("s_mov_b32 %0, m0\n\ts_mov_b32 m0, %3\n\ts_nop 0\n\tglobal_load_lds_dwordx4 %1, %2\n\ts_mov_b32 m0, %0"
                 : "=&s"(keep) : "v"(voff), "s"(sbase), "s"(dst) : "memory");
}
__device__ __forceinline__ s4v tr_read(const char* p) {
    return __builtin_bit_cast(s4v, __builtin_amdgcn_ds_read_tr16_b64_v4i16((__attribute__((address_space(3))) v4i16_t*)p));
}
__device__ __forceinline__ s8v cat8(s4v a, s4v b) {
    return __builtin_shufflevector(a, b, 0, 1, 2, 3, 4, 5, 6, 7);
}
__device__ __forceinline__ float bf_lo(unsigned u) { return __builtin_bit_cast(float, u << 16); }
__device__ __forceinline__ float bf_hi(unsigned u) { return __builtin_bit_cast(float, u & 0xffff0000u); }
__device__ __forceinline__ float bf2f(bf16_t v) { return __builtin_bit_cast(float, (unsigned)v << 16); }
__device__ __forceinline__ unsigned pack_bf2(float a, float b) {
    __hip_bfloat16 x = __float2bfloat16(a), y = __float2bfloat16(b);
    return (unsigned)__builtin_bit_cast(unsigned short, x) | ((unsigned)__builtin_bit_cast(unsigned short, y) << 16);
}
#define MFMA16(a, b, c) __builtin_amdgcn_mfma_f32_16x16x32_bf16(a, b, c, 0, 0, 0)
#define WAIT_V(n) asm volatile("s_waitcnt vmcnt(" #n ")" ::: "memory")
#define WAIT_L(n) asm volatile("s_waitcnt lgkmcnt(" #n ")" ::: "memory")

constexpr int K1_XN_BYTES = 9 * 80 * 128;
constexpr int K1_RING = 4;
constexpr int K1_LDS = K1_XN_BYTES + 7 * K1_RING * 2048;

__global__ __launch_bounds__(512, 2) void k1_qkv_attn(const bf16_t* __restrict__ x, const bf16_t* __restrict__ wqkv,
                                                      const bf16_t* __restrict__ w1, bf16_t* __restrict__ o,
                                                      float* __restrict__ cs1) {
    extern __shared__ __attribute__((aligned(16))) char smem[];
    const int tid = threadIdx.x, lane = tid & 63;
    const int wave = __builtin_amdgcn_readfirstlane(tid >> 6);
    const int hg = blockIdx.x & 7, rt = blockIdx.x >> 3;
    const int s0 = rt * 64;

    const char* wsrc[2];
    char* ring = smem + K1_XN_BYTES + wave * (K1_RING * 2048);
    const unsigned ringa = lds_addr(smem) + K1_XN_BYTES + wave * (K1_RING * 2048);
    if (wave < 7) {
#pragma unroll
        for (int i = 0; i < 2; i++) {
            int k = i * 16 + (lane >> 2);
            int c4 = lane & 3;
            int t = (c4 >> 1) ^ ((k >> 3) & 1);
            int pc = wave * 4 + t * 2 + (c4 & 1);
            if (pc > 26) pc = 26;
            int sec = pc / 9, wi = pc - sec * 9;
            int col = sec * 576 + hg * 72 + wi * 8;
            wsrc[i] = (const char*)(wqkv + (size_t)k * NQKV + col);
        }
    }
    u4v xv[9];
    {
        int r = wave * 8 + (lane >> 3);
        int s = s0 - 16 + r; if (s < 0) s = 0;
        const u4v* p = (const u4v*)(x + (size_t)s * D_) + (lane & 7);
#pragma unroll
        for (int i = 0; i < 9; i++) xv[i] = p[i * 8];
    }
    if (wave < 7) {
#pragma unroll
        for (int st = 0; st < 3; st++) {
#pragma unroll
            for (int i = 0; i < 2; i++) glds16(wsrc[i] + (size_t)st * 32 * NQKV * 2, ringa + st * 2048 + i * 1024);
        }
    }
    auto ln_pass = [&](int r) {
        float sum = 0.f;
#pragma unroll
        for (int i = 0; i < 9; i++)
#pragma unroll
            for (int j = 0; j < 4; j++) sum += bf_lo(xv[i][j]) + bf_hi(xv[i][j]);
        sum += __shfl_xor(sum, 1); sum += __shfl_xor(sum, 2); sum += __shfl_xor(sum, 4);
        float mean = sum * (1.f / 576.f);
        float sq = 0.f;
#pragma unroll
        for (int i = 0; i < 9; i++)
#pragma unroll
            for (int j = 0; j < 4; j++) {
                float a = bf_lo(xv[i][j]) - mean, b = bf_hi(xv[i][j]) - mean;
                sq += a * a + b * b;
            }
        sq += __shfl_xor(sq, 1); sq += __shfl_xor(sq, 2); sq += __shfl_xor(sq, 4);
        float rstd = rsqrtf(sq * (1.f / 576.f) + 1e-5f);
#pragma unroll
        for (int i = 0; i < 9; i++) {
            u4v ov;
#pragma unroll
            for (int j = 0; j < 4; j++)
                ov[j] = pack_bf2((bf_lo(xv[i][j]) - mean) * rstd, (bf_hi(xv[i][j]) - mean) * rstd);
            *(u4v*)(smem + i * (80 * 128) + r * 128 + (((lane & 7) ^ (r & 7)) << 4)) = ov;
        }
    };
    ln_pass(wave * 8 + (lane >> 3));
    if (wave < 2) {
        int r = 64 + wave * 8 + (lane >> 3);
        int s = s0 - 16 + r;
        const u4v* p = (const u4v*)(x + (size_t)s * D_) + (lane & 7);
#pragma unroll
        for (int i = 0; i < 9; i++) xv[i] = p[i * 8];
        ln_pass(r);
    }
    if (wave == 7 && blockIdx.x < 144) {
        const u4v* p = (const u4v*)(w1 + (size_t)(lane >> 2) * F_ + blockIdx.x * 32) + (lane & 3);
        float a[8];
#pragma unroll
        for (int j = 0; j < 8; j++) a[j] = 0.f;
#pragma unroll 6
        for (int i = 0; i < 36; i++) {
            u4v v = p[(size_t)i * 16 * F_ / 8];
#pragma unroll
            for (int j = 0; j < 4; j++) { a[2 * j] += bf_lo(v[j]); a[2 * j + 1] += bf_hi(v[j]); }
        }
#pragma unroll
        for (int j = 0; j < 8; j++) {
            float t = a[j];
            t += __shfl_xor(t, 4); t += __shfl_xor(t, 8); t += __shfl_xor(t, 16); t += __shfl_xor(t, 32);
            a[j] = t;
        }
        if (lane < 4) {
            float* q = cs1 + blockIdx.x * 32 + lane * 8;
            *(f4v*)q = f4v{a[0], a[1], a[2], a[3]};
            *(f4v*)(q + 4) = f4v{a[4], a[5], a[6], a[7]};
        }
    }
    __syncthreads();

    f4v acc[5][2];
#pragma unroll
    for (int m = 0; m < 5; m++) { acc[m][0] = f4v{0, 0, 0, 0}; acc[m][1] = f4v{0, 0, 0, 0}; }
    if (wave < 7) {
        const int g = lane >> 4, q = (lane & 15) >> 2, p = lane & 3;
        const int woff0 = (8 * g + q) * 64 + ((0 ^ (g & 1)) << 5) + 8 * p;
        const int woff1 = (8 * g + q) * 64 + ((1 ^ (g & 1)) << 5) + 8 * p;
        const int xr = lane & 15;
        for (int st = 0; st < 18; st++) {
            {
                int sn = st + 3; if (sn > 17) sn = 17;
                int slot = (st + 3) & 3;
#pragma unroll
                for (int i = 0; i < 2; i++) glds16(wsrc[i] + (size_t)sn * 32 * NQKV * 2, ringa + slot * 2048 + i * 1024);
            }
            WAIT_V(6);
            const char* wb = ring + (st & 3) * 2048;
            s8v wf[2];
            wf[0] = cat8(tr_read(wb + woff0), tr_read(wb + woff0 + 256));
            wf[1] = cat8(tr_read(wb + woff1), tr_read(wb + woff1 + 256));
            const int kb = st >> 1, ch = (st & 1) * 4 + g;
            const char* xb = smem + kb * (80 * 128);
#pragma unroll
            for (int m = 0; m < 5; m++) {
                if (m == 0 && wave < 2) continue;
                int r = m * 16 + xr;
                s8v xf = *(const s8v*)(xb + r * 128 + ((ch ^ (r & 7)) << 4));
                acc[m][0] = MFMA16(xf, wf[0], acc[m][0]);
                acc[m][1] = MFMA16(xf, wf[1], acc[m][1]);
            }
        }
        WAIT_V(0);
    }
    __syncthreads();
    float* qkv = (float*)smem;
    if (wave < 7) {
#pragma unroll
        for (int m = 0; m < 5; m++)
#pragma unroll
            for (int t = 0; t < 2; t++) {
                int col = wave * 32 + t * 16 + (lane & 15);
                if (col < 216) *(f4v*)(qkv + col * 80 + m * 16 + 4 * (lane >> 4)) = acc[m][t];
            }
    }
    __syncthreads();
    if (wave < 4) {
        const int head = wave;
        const int r = 16 + lane;
        const int s = s0 + lane;
        const float* qp = qkv + (head * 18) * 80 + r;
        const float* kp = qkv + (72 + head * 18) * 80 + r;
        const float* vp = qkv + (144 + head * 18) * 80 + r;
        float qv[18];
#pragma unroll
        for (int d = 0; d < 18; d++) qv[d] = qp[d * 80];
        float lg[8];
        float mx = -3.0e38f;
#pragma unroll
        for (int w = 0; w < 8; w++) {
            float a = 0.f;
#pragma unroll
            for (int d = 0; d < 18; d++) a += qv[d] * kp[d * 80 - 2 * w];
            a *= 0.23570226039551584f;
            lg[w] = (s - 2 * w >= 0) ? a : -1e9f;
            mx = fmaxf(mx, lg[w]);
        }
        float den = 0.f;
#pragma unroll
        for (int w = 0; w < 8; w++) { lg[w] = __expf(lg[w] - mx); den += lg[w]; }
        float inv = 1.f / den;
        float ov[18];
#pragma unroll
        for (int d = 0; d < 18; d++) ov[d] = 0.f;
#pragma unroll
        for (int w = 0; w < 8; w++) {
            float pw = lg[w] * inv;
#pragma unroll
            for (int d = 0; d < 18; d++) ov[d] += pw * vp[d * 80 - 2 * w];
        }
        unsigned* op = (unsigned*)(o + (size_t)s * D_ + hg * 72 + head * 18);
#pragma unroll
        for (int d = 0; d < 9; d++) op[d] = pack_bf2(ov[2 * d], ov[2 * d + 1]);
    }
}

constexpr int K2_A = 9 * 64 * 128;
constexpr int K2_B = 576 * 144;
constexpr int K2_LDS = K2_A + K2_B + 64;

__global__ __launch_bounds__(256, 1) void k2_wo(const bf16_t* __restrict__ o, const bf16_t* __restrict__ wo,
                                                const bf16_t* __restrict__ x, bf16_t* __restrict__ h,
                                                f2v* __restrict__ stats) {
    extern __shared__ __attribute__((aligned(16))) char smem[];
    const int tid = threadIdx.x, lane = tid & 63;
    const int wave = __builtin_amdgcn_readfirstlane(tid >> 6);
    const int ct = blockIdx.x & 7, rt = blockIdx.x >> 3;
    const int r0 = rt * 64, c0 = ct * 72;
#pragma unroll
    for (int i = 0; i < 18; i++) {
        int a = wave * 18 + i;
        int kb = a >> 3, row = (a & 7) * 8 + (lane >> 3);
        int ch = (lane & 7) ^ (row & 7);
        glds16(o + (size_t)(r0 + row) * D_ + kb * 64 + ch * 8, lds_addr(smem) + a * 1024);
    }
    for (int b = wave; b < 81; b += 4) {
        int ci = b * 64 + lane;
        int k = ci / 9, c = ci - k * 9;
        glds16(wo + (size_t)k * D_ + c0 + c * 8, lds_addr(smem) + K2_A + b * 1024);
    }
    WAIT_V(0);
    __syncthreads();
    const int g = lane >> 4, q = (lane & 15) >> 2, p = lane & 3;
    f4v acc[5];
#pragma unroll
    for (int n = 0; n < 5; n++) acc[n] = f4v{0, 0, 0, 0};
    const int xr = wave * 16 + (lane & 15);
    const char* bb = smem + K2_A + (8 * g + q) * 144 + 8 * p;
#pragma unroll 2
    for (int st = 0; st < 18; st++) {
        const int kb = st >> 1, ch = (st & 1) * 4 + g;
        s8v xf = *(const s8v*)(smem + kb * (64 * 128) + xr * 128 + ((ch ^ (xr & 7)) << 4));
#pragma unroll
        for (int n = 0; n < 5; n++) {
            const char* wb = bb + st * 32 * 144 + n * 32;
            s8v wf = cat8(tr_read(wb), tr_read(wb + 4 * 144));
            acc[n] = MFMA16(wf, xf, acc[n]);
        }
    }
    const int row = r0 + xr;
    float hv[5][4];
    float sum = 0.f;
#pragma unroll
    for (int n = 0; n < 5; n++) {
        int col = n * 16 + 4 * g;
        if (col < 72) {
            u2v xx = *(const u2v*)(x + (size_t)row * D_ + c0 + col);
            unsigned p0 = pack_bf2(bf_lo(xx[0]) + acc[n][0], bf_hi(xx[0]) + acc[n][1]);
            unsigned p1 = pack_bf2(bf_lo(xx[1]) + acc[n][2], bf_hi(xx[1]) + acc[n][3]);
            *(u2v*)(h + (size_t)row * D_ + c0 + col) = u2v{p0, p1};
            hv[n][0] = bf_lo(p0); hv[n][1] = bf_hi(p0); hv[n][2] = bf_lo(p1); hv[n][3] = bf_hi(p1);
            sum += hv[n][0] + hv[n][1] + hv[n][2] + hv[n][3];
        } else {
            hv[n][0] = hv[n][1] = hv[n][2] = hv[n][3] = 0.f;
        }
    }
    sum += __shfl_xor(sum, 16); sum += __shfl_xor(sum, 32);
    float mean = sum * (1.f / 72.f);
    float m2 = 0.f;
#pragma unroll
    for (int n = 0; n < 5; n++) {
        int col = n * 16 + 4 * g;
        if (col < 72) {
#pragma unroll
            for (int j = 0; j < 4; j++) { float d = hv[n][j] - mean; m2 += d * d; }
        }
    }
    m2 += __shfl_xor(m2, 16); m2 += __shfl_xor(m2, 32);
    if (g == 0) stats[(size_t)row * 8 + ct] = f2v{sum, m2};
}

constexpr int K3_STAGE = 16384 + 36864;
constexpr int K3_PAR = 3 * K3_STAGE;
constexpr int K3_LDS = K3_PAR + 1024 + 2304;
#define SB0 __builtin_amdgcn_sched_barrier(0)

__device__ __forceinline__ float gelu_tanh(float v) {
    float t = v * v;
    float z = v * (-2.302208198f * 0.044715f * t - 2.302208198f);
    float e = __builtin_amdgcn_exp2f(z);
    return v * __builtin_amdgcn_rcpf(1.f + e);
}

__global__ __launch_bounds__(512, 2) void k3_up(const bf16_t* __restrict__ h, const bf16_t* __restrict__ w1,
                                                const bf16_t* __restrict__ b1, const float* __restrict__ cs1,
                                                const f2v* __restrict__ stats, char* __restrict__ h1f) {
    extern __shared__ __attribute__((aligned(16))) char smem[];
    const int tid = threadIdx.x, lane = tid & 63;
    const int wave = __builtin_amdgcn_readfirstlane(tid >> 6);
    const int xcd = blockIdx.x & 7, idx = blockIdx.x >> 3;
    const int rt = (xcd & 1) * 8 + (idx & 7), ct = (xcd >> 1) * 4 + (idx >> 3);
    const int r0 = rt * 128, c0 = ct * 288;
    const int wk = wave >> 2, wm = (wave >> 1) & 1, wn = wave & 1;
    const unsigned lbase = lds_addr(smem);
    unsigned asrc[2];
    unsigned bsrc[5];
    const int bi0 = wave * 5;
#pragma unroll
    for (int i = 0; i < 2; i++) {
        int a = wave * 2 + i;
        int row = a * 8 + (lane >> 3);
        int ch = (lane & 7) ^ (row & 7);
        asrc[i] = (unsigned)(((r0 + row) * D_ + ch * 8) * 2);
    }
#pragma unroll
    for (int i = 0; i < 5; i++) {
        int bi = bi0 + i; if (bi > 35) bi = 35;
        int off = bi * 1024 + lane * 16;
        int k = off / 576, r = off - k * 576;
        int nt = (r >> 5) ^ ((k >> 3) & 1);
        bsrc[i] = (unsigned)((k * F_ + c0 + nt * 16 + ((r >> 4) & 1) * 8) * 2);
    }
    auto gl_a = [&](int i, int st, int buf) { glds16s((const char*)h + st * 128, asrc[i], lbase + buf * K3_STAGE + (wave * 2 + i) * 1024); };
    auto gl_b = [&](int i, int st, int buf) { glds16s((const char*)w1 + (size_t)st * 64 * F_ * 2, bsrc[i], lbase + buf * K3_STAGE + 16384 + (bi0 + i > 35 ? 35 : bi0 + i) * 1024); };
#pragma unroll
    for (int st = 0; st < 2; st++) { gl_a(0, st, st); gl_a(1, st, st); for (int i = 0; i < 5; i++) gl_b(i, st, st); }
    f2v* mu = (f2v*)(smem + K3_PAR);
    f2v* cb = (f2v*)(smem + K3_PAR + 1024);
    if (tid < 128) {
        const f2v* sp = stats + (size_t)(r0 + tid) * 8;
        float s[8], m2[8], tot = 0.f;
#pragma unroll
        for (int i = 0; i < 8; i++) { f2v v = sp[i]; s[i] = v[0]; m2[i] = v[1]; tot += v[0]; }
        float mean = tot * (1.f / 576.f);
        float M2 = 0.f;
#pragma unroll
        for (int i = 0; i < 8; i++) { float d = s[i] * (1.f / 72.f) - mean; M2 += m2[i] + 72.f * d * d; }
        mu[tid] = f2v{mean, rsqrtf(M2 * (1.f / 576.f) + 1e-5f)};
    } else if (tid < 128 + 288) {
        int c = tid - 128;
        cb[c] = f2v{cs1[c0 + c], bf2f(b1[c0 + c])};
    }
    const int g = lane >> 4, q = (lane & 15) >> 2, p = lane & 3;
    f4v acc[4][9];
#pragma unroll
    for (int m = 0; m < 4; m++)
#pragma unroll
        for (int n = 0; n < 9; n++) acc[m][n] = f4v{0, 0, 0, 0};
    const int dlt = (g & 1) ? 32 : 0;
    const int wb0 = 16384 + (wk * 32 + 8 * g + q) * 576 + wn * 9 * 32 + 8 * p;
    const int wbe = wb0 + ((wn & 1) ? -dlt : dlt);
    const int wbo = wb0 + ((wn & 1) ? dlt : -dlt);
    const int xr = lane & 15;
    const int xoff = (wm * 64 + xr) * 128 + (((wk * 4 + g) ^ (xr & 7)) << 4);
    s8v xf[4], wf[9];
    auto rd = [&](const char* base) {
#pragma unroll
        for (int m = 0; m < 4; m++) xf[m] = *(const s8v*)(base + xoff + m * 2048);
#pragma unroll
        for (int n = 0; n < 9; n++) {
            const char* wb = base + ((n & 1) ? wbo : wbe) + n * 32;
            wf[n] = cat8(tr_read(wb), tr_read(wb + 4 * 576));
        }
    };
#define K3_MM(n) { _Pragma("unroll") for (int m = 0; m < 4; m++) acc[m][n] = MFMA16(wf[n], xf[m], acc[m][n]); }
#pragma unroll
    for (int n = 0; n < 9; n++) wf[n] = s8v{0, 0, 0, 0, 0, 0, 0, 0};
#pragma unroll
    for (int m = 0; m < 4; m++) xf[m] = s8v{0, 0, 0, 0, 0, 0, 0, 0};
    const bool typeA = wk == 0;
#pragma unroll 1
    for (int st = 0; st < 9; st++) {
        WAIT_V(7);
        __builtin_amdgcn_s_barrier();
        int sn = st + 2; if (sn > 8) sn = 8;
        int buf = st + 2; buf = buf >= 3 ? buf - 3 : buf; buf = buf >= 3 ? buf - 3 : buf; buf = buf >= 3 ? buf - 3 : buf;
        const char* base = smem + (st % 3) * K3_STAGE;
        if (typeA) rd(base);
        SB0; gl_a(0, sn, buf); gl_a(1, sn, buf); SB0;
        K3_MM(0); K3_MM(1); SB0; gl_b(0, sn, buf); SB0;
        K3_MM(2); SB0; gl_b(1, sn, buf); SB0;
        K3_MM(3); K3_MM(4); SB0; gl_b(2, sn, buf); SB0;
        K3_MM(5); SB0; gl_b(3, sn, buf); SB0;
        K3_MM(6); K3_MM(7); SB0; gl_b(4, sn, buf); SB0;
        K3_MM(8); SB0;
        if (!typeA) { rd(base); WAIT_L(0); SB0; }
    }
    if (!typeA) {
#pragma unroll
        for (int n = 0; n < 9; n++) K3_MM(n);
    }
    WAIT_V(0);
    __syncthreads();
    {
        f4v* ex = (f4v*)smem + wave * (18 * 64) + lane;
#pragma unroll
        for (int m = 0; m < 2; m++)
#pragma unroll
            for (int n = 0; n < 9; n++) ex[(m * 9 + n) * 64] = acc[wk == 0 ? m + 2 : m][n];
        __syncthreads();
        const f4v* ey = (const f4v*)smem + (wave ^ 4) * (18 * 64) + lane;
        const int mb = wk * 2;
#pragma unroll
        for (int m = 0; m < 2; m++) {
            int rl = wm * 64 + (mb + m) * 16 + xr;
            f2v ms = mu[rl];
            char* ob = h1f + (size_t)((r0 + wm * 64 + (mb + m) * 16) >> 4) * (144 * 1024) + lane * 16;
#pragma unroll
            for (int n = 0; n < 9; n++) {
                f4v a = (wk == 0 ? acc[m][n] : acc[m + 2][n]) + ey[(m * 9 + n) * 64];
                int cl = wn * 144 + n * 16 + 4 * g;
                f2v c0v = cb[cl], c1v = cb[cl + 1], c2v = cb[cl + 2], c3v = cb[cl + 3];
                float v0 = gelu_tanh((a[0] - ms[0] * c0v[0]) * ms[1] + c0v[1]);
                float v1 = gelu_tanh((a[1] - ms[0] * c1v[0]) * ms[1] + c1v[1]);
                float v2 = gelu_tanh((a[2] - ms[0] * c2v[0]) * ms[1] + c2v[1]);
                float v3 = gelu_tanh((a[3] - ms[0] * c3v[0]) * ms[1] + c3v[1]);
                int cg = c0 + wn * 144 + n * 16;
                *(u2v*)(ob + (cg >> 5) * 1024 + ((cg >> 4) & 1) * 8) = u2v{pack_bf2(v0, v1), pack_bf2(v2, v3)};
            }
        }
    }
}

constexpr int K4_STAGE = 16 * 2432;
constexpr int K4_NBUF = 3;
constexpr int K4_LDS = 147456;

__device__ __forceinline__ void gload16(u4v& dst, const void* sbase, unsigned voff) {
    asm volatile("global_load_dwordx4 %0, %1, %2" : "=v"(dst) : "v"(voff), "s"(sbase) : "memory");
}

__global__ __launch_bounds__(512, 2) void k4_down(const char* __restrict__ h1f, const bf16_t* __restrict__ w2,
                                                  float* __restrict__ part) {
    extern __shared__ __attribute__((aligned(16))) char smem[];
    const int tid = threadIdx.x, lane = tid & 63;
    const int wave = __builtin_amdgcn_readfirstlane(tid >> 6);
    const int xcd = blockIdx.x & 7, idx = blockIdx.x >> 3;
    const int ks = xcd >> 1;
    const int rt = (xcd & 1) * 8 + (idx & 7), ct = idx >> 3;
    const int r0 = rt * 128, c0 = ct * 144, k0 = ks * 1152;
    const int wk = wave >> 1, wm = wave & 1;
    const unsigned lbase = lds_addr(smem);
    unsigned bsrc[5];
#pragma unroll
    for (int i = 0; i < 5; i++) {
        int bi = wave * 5 + i; if (bi > 37) bi = 37;
        int off = bi * 1024 + lane * 16;
        int blk = off / 2432, r = off - blk * 2432;
        int kk = r / 288, cr = r - kk * 288;
        if (kk > 7) { kk = 7; cr = 0; }
        bsrc[i] = (unsigned)(((k0 + blk * 8 + kk) * D_ + c0) * 2 + cr);
    }
    auto gl_b = [&](int i, int st, int buf) {
        int bi = wave * 5 + i; if (bi > 37) bi = 37;
        glds16s((const char*)w2 + (size_t)st * 128 * D_ * 2, bsrc[i], lbase + buf * K4_STAGE + bi * 1024);
    };
    const char* xsrc = h1f + ((size_t)((r0 + wm * 64) >> 4) * 144 + (k0 >> 5) + wk) * 1024;
    const unsigned xvo = lane * 16;
    u4v xa[4], xb[4];
    auto ldx = [&](u4v* dst, int st) {
#pragma unroll
        for (int m = 0; m < 4; m++) gload16(dst[m], xsrc + (size_t)m * (144 * 1024) + st * 4096, xvo);
    };
    const int g = lane >> 4, q = (lane & 15) >> 2, p = lane & 3;
    f4v acc[4][9];
#pragma unroll
    for (int m = 0; m < 4; m++)
#pragma unroll
        for (int n = 0; n < 9; n++) acc[m][n] = f4v{0, 0, 0, 0};
    const int kA = wk * 32 + 4 * g + q, kB = kA + 16;
    const int wbA = (kA >> 3) * 2432 + (kA & 7) * 288 + 8 * p;
    const int wbB = (kB >> 3) * 2432 + (kB & 7) * 288 + 8 * p;
    s8v wf[9];
    auto rd = [&](const char* base) {
#pragma unroll
        for (int n = 0; n < 9; n++) wf[n] = cat8(tr_read(base + wbA + n * 32), tr_read(base + wbB + n * 32));
    };
#define K4_MM(n, X) { _Pragma("unroll") for (int m = 0; m < 4; m++) acc[m][n] = MFMA16(wf[n], __builtin_bit_cast(s8v, X[m]), acc[m][n]); }
#define XWAIT(N, X) asm volatile("s_waitcnt vmcnt(" #N ")" : "+v"(X[0]), "+v"(X[1]), "+v"(X[2]), "+v"(X[3]) :: "memory")
    const bool typeA = wk < 2;
    const int dB = typeA ? 0 : 1;
#pragma unroll
    for (int n = 0; n < 9; n++) wf[n] = s8v{0, 0, 0, 0, 0, 0, 0, 0};
#pragma unroll
    for (int m = 0; m < 4; m++) { xa[m] = u4v{0, 0, 0, 0}; xb[m] = u4v{0, 0, 0, 0}; }
    for (int i = 0; i < 5; i++) gl_b(i, 0, 0);
    if (typeA) ldx(xa, 0);
    for (int i = 0; i < 5; i++) gl_b(i, 1, 1);
#define K4_ITER(XC, XN) { \
        XWAIT(5, XC); \
        __builtin_amdgcn_s_barrier(); \
        int sn = st + 2; if (sn > 8) sn = 8; \
        int sx = st + 1 - dB; if (sx > 8) sx = 8; \
        int buf = (st + 2) % 3; \
        const char* base = smem + (st % 3) * K4_STAGE; \
        if (typeA) rd(base); \
        SB0; ldx(XN, sx); SB0; \
        K4_MM(0, XC); K4_MM(1, XC); SB0; gl_b(0, sn, buf); SB0; \
        K4_MM(2, XC); SB0; gl_b(1, sn, buf); SB0; \
        K4_MM(3, XC); K4_MM(4, XC); SB0; gl_b(2, sn, buf); SB0; \
        K4_MM(5, XC); SB0; gl_b(3, sn, buf); SB0; \
        K4_MM(6, XC); K4_MM(7, XC); SB0; gl_b(4, sn, buf); SB0; \
        K4_MM(8, XC); SB0; \
        if (!typeA) { rd(base); WAIT_L(0); SB0; } }
#pragma unroll 1
    for (int st = 0; st < 8; st += 2) {
        K4_ITER(xa, xb);
        st++; K4_ITER(xb, xa); st--;
    }
    { int st = 8; K4_ITER(xa, xb); }
    XWAIT(0, xb);
    if (!typeA) {
#pragma unroll
        for (int n = 0; n < 9; n++) K4_MM(n, xb);
    }
    WAIT_V(0);
    __syncthreads();
    f4v r1[2][9];
    {
        f4v* ex = (f4v*)smem + wave * (18 * 64) + lane;
        const bool lo = (wk & 1) == 0;
#pragma unroll
        for (int m = 0; m < 2; m++)
#pragma unroll
            for (int n = 0; n < 9; n++) ex[(m * 9 + n) * 64] = lo ? acc[m + 2][n] : acc[m][n];
        __syncthreads();
        const f4v* ey = (const f4v*)smem + (wave ^ 2) * (18 * 64) + lane;
#pragma unroll
        for (int m = 0; m < 2; m++)
#pragma unroll
            for (int n = 0; n < 9; n++) r1[m][n] = (lo ? acc[m][n] : acc[m + 2][n]) + ey[(m * 9 + n) * 64];
        __syncthreads();
    }
    {
        f4v* ex = (f4v*)smem + wave * (9 * 64) + lane;
        const bool lo = wk < 2;
#pragma unroll
        for (int n = 0; n < 9; n++) ex[n * 64] = lo ? r1[1][n] : r1[0][n];
        __syncthreads();
        const f4v* ey = (const f4v*)smem + (wave ^ 4) * (9 * 64) + lane;
        const int mt = (wk & 1) * 2 + (lo ? 0 : 1);
        const int row = r0 + wm * 64 + mt * 16 + (lane & 15);
        float* pp = part + (size_t)ks * S_ * D_ + (size_t)row * D_ + c0 + 4 * g;
#pragma unroll
        for (int n = 0; n < 9; n++) {
            f4v v = (lo ? r1[0][n] : r1[1][n]) + ey[n * 64];
            *(f4v*)(pp + n * 16) = v;
        }
    }
}

__global__ __launch_bounds__(256) void k5_reduce(const float* __restrict__ part, const bf16_t* __restrict__ h,
                                                 const bf16_t* __restrict__ b2, bf16_t* __restrict__ out) {
    int i = blockIdx.x * 256 + threadIdx.x;
    size_t e = (size_t)i * 4;
    int col = (int)(e % D_);
    f4v a = *(const f4v*)(part + e);
#pragma unroll
    for (int k = 1; k < 4; k++) a += *(const f4v*)(part + (size_t)k * S_ * D_ + e);
    u2v hh = *(const u2v*)(h + e);
    u2v bb = *(const u2v*)(b2 + col);
    float v0 = bf_lo(hh[0]) + (a[0] + bf_lo(bb[0]));
    float v1 = bf_hi(hh[0]) + (a[1] + bf_hi(bb[0]));
    float v2 = bf_lo(hh[1]) + (a[2] + bf_lo(bb[1]));
    float v3 = bf_hi(hh[1]) + (a[3] + bf_hi(bb[1]));
    *(u2v*)(out + e) = u2v{pack_bf2(v0, v1), pack_bf2(v2, v3)};
}

extern "C" void kernel_launch(void* const* d_in, const int* in_sizes, int n_in,
                              void* d_out, int out_size, void* d_ws, size_t ws_size,
                              hipStream_t stream) {
    const bf16_t* x = (const bf16_t*)d_in[0];
    const bf16_t* wqkv = (const bf16_t*)d_in[1];
    const bf16_t* wo = (const bf16_t*)d_in[2];
    const bf16_t* w1 = (const bf16_t*)d_in[4];
    const bf16_t* b1 = (const bf16_t*)d_in[5];
    const bf16_t* w2 = (const bf16_t*)d_in[6];
    const bf16_t* b2 = (const bf16_t*)d_in[7];
    bf16_t* out = (bf16_t*)d_out;
    char* ws = (char*)d_ws;
    bf16_t* o = (bf16_t*)(ws);
    bf16_t* h = (bf16_t*)(ws + (4 << 20));
    f2v* stats = (f2v*)(ws + (8 << 20));
    float* cs1 = (float*)(ws + (9 << 20));
    char* h1 = (char*)(ws + (16 << 20));
    float* part = (float*)(ws + (48 << 20));

    hipFuncSetAttribute((const void*)k1_qkv_attn, hipFuncAttributeMaxDynamicSharedMemorySize, K1_LDS);
    hipFuncSetAttribute((const void*)k2_wo, hipFuncAttributeMaxDynamicSharedMemorySize, K2_LDS);
    hipFuncSetAttribute((const void*)k3_up, hipFuncAttributeMaxDynamicSharedMemorySize, K3_LDS);
    hipFuncSetAttribute((const void*)k4_down, hipFuncAttributeMaxDynamicSharedMemorySize, K4_LDS);

    k1_qkv_attn<<<256, 512, K1_LDS, stream>>>(x, wqkv, w1, o, cs1);
    k2_wo<<<256, 256, K2_LDS, stream>>>(o, wo, x, h, stats);
    k3_up<<<256, 512, K3_LDS, stream>>>(h, w1, b1, cs1, stats, h1);
    k4_down<<<256, 512, K4_LDS, stream>>>(h1, w2, part);
    k5_reduce<<<S_ * D_ / 4 / 256, 256, 0, stream>>>(part, h, b2, out);
}
```

```cpp
#include <hip/hip_runtime.h>
#include <hip/hip_bf16.h>

typedef __attribute__((ext_vector_type(8))) short s8v;
typedef __attribute__((ext_vector_type(4))) short s4v;
typedef __attribute__((ext_vector_type(4))) short v4i16_t;
typedef __attribute__((ext_vector_type(4))) float f4v;
typedef __attribute__((ext_vector_type(2))) float f2v;
typedef __attribute__((ext_vector_type(4))) unsigned u4v;
typedef __attribute__((ext_vector_type(2))) unsigned u2v;
typedef unsigned short bf16_t;

constexpr int S_ = 2048, D_ = 576, NQKV = 1728, F_ = 4608;

__device__ __forceinline__ unsigned lds_addr(const void* l) {
    return (unsigned)(size_t)(__attribute__((address_space(3))) const char*)l;
}
__device__ __forceinline__ void glds16(const void* g, unsigned dst) {
    unsigned keep;
    asm volatile("s_mov_b32 %0, m0\n\ts_mov_b32 m0, %2\n\ts_nop 0\n\tglobal_load_lds_dwordx4 %1, off\n\ts_mov_b32 m0, %0"
                 : "=&s"(keep) : "v"(g), "s"(dst) : "memory");
}
__device__ __forceinline__ void glds16s(const void* sbase, unsigned voff, unsigned dst) {
    unsigned keep;
    asm volatile("s_mov_b32 %0, m0\n\ts_mov_b32 m0, %3\n\ts_nop 0\n\tglobal_load_lds_dwordx4 %1, %2\n\ts_mov_b32 m0, %0"
                 : "=&s"(keep) : "v"(voff), "s"(sbase), "s"(dst) : "memory");
}
__device__ __forceinline__ s4v tr_read(const char* p) {
    return __builtin_bit_cast(s4v, __builtin_amdgcn_ds_read_tr16_b64_v4i16((__attribute__((address_space(3))) v4i16_t*)p));
}
__device__ __forceinline__ s8v cat8(s4v a, s4v b) {
    return __builtin_shufflevector(a, b, 0, 1, 2, 3, 4, 5, 6, 7);
}
__device__ __forceinline__ float bf_lo(unsigned u) { return __builtin_bit_cast(float, u << 16); }
__device__ __forceinline__ float bf_hi(unsigned u) { return __builtin_bit_cast(float, u & 0xffff0000u); }
__device__ __forceinline__ float bf2f(bf16_t v) { return __builtin_bit_cast(float, (unsigned)v << 16); }
__device__ __forceinline__ unsigned pack_bf2(float a, float b) {
    __hip_bfloat16 x = __float2bfloat16(a), y = __float2bfloat16(b);
    return (unsigned)__builtin_bit_cast(unsigned short, x) | ((unsigned)__builtin_bit_cast(unsigned short, y) << 16);
}
#define MFMA16(a, b, c) __builtin_amdgcn_mfma_f32_16x16x32_bf16(a, b, c, 0, 0, 0)
#define WAIT_V(n) asm volatile("s_waitcnt vmcnt(" #n ")" ::: "memory")
#define WAIT_L(n) asm volatile("s_waitcnt lgkmcnt(" #n ")" ::: "memory")

constexpr int K1_XN_BYTES = 9 * 80 * 128;
constexpr int K1_RING = 4;
constexpr int K1_LDS = K1_XN_BYTES + 7 * K1_RING * 2048;

__global__ __launch_bounds__(512, 2) void k1_qkv_attn(const bf16_t* __restrict__ x, const bf16_t* __restrict__ wqkv,
                                                      const bf16_t* __restrict__ w1, bf16_t* __restrict__ o,
                                                      float* __restrict__ cs1) {
    extern __shared__ __attribute__((aligned(16))) char smem[];
    const int tid = threadIdx.x, lane = tid & 63;
    const int wave = __builtin_amdgcn_readfirstlane(tid >> 6);
    const int hg = blockIdx.x & 7, rt = blockIdx.x >> 3;
    const int s0 = rt * 64;

    const char* wsrc[2];
    char* ring = smem + K1_XN_BYTES + wave * (K1_RING * 2048);
    const unsigned ringa = lds_addr(smem) + K1_XN_BYTES + wave * (K1_RING * 2048);
    if (wave < 7) {
#pragma unroll
        for (int i = 0; i < 2; i++) {
            int k = i * 16 + (lane >> 2);
            int c4 = lane & 3;
            int t = (c4 >> 1) ^ ((k >> 3) & 1);
            int pc = wave * 4 + t * 2 + (c4 & 1);
            if (pc > 26) pc = 26;
            int sec = pc / 9, wi = pc - sec * 9;
            int col = sec * 576 + hg * 72 + wi * 8;
            wsrc[i] = (const char*)(wqkv + (size_t)k * NQKV + col);
        }
    }
    u4v xv[9];
    {
        int r = wave * 8 + (lane >> 3);
        int s = s0 - 16 + r; if (s < 0) s = 0;
        const u4v* p = (const u4v*)(x + (size_t)s * D_) + (lane & 7);
#pragma unroll
        for (int i = 0; i < 9; i++) xv[i] = p[i * 8];
    }
    if (wave < 7) {
#pragma unroll
        for (int st = 0; st < 3; st++) {
#pragma unroll
            for (int i = 0; i < 2; i++) glds16(wsrc[i] + (size_t)st * 32 * NQKV * 2, ringa + st * 2048 + i * 1024);
        }
    }
    auto ln_pass = [&](int r) {
        float sum = 0.f;
#pragma unroll
        for (int i = 0; i < 9; i++)
#pragma unroll
            for (int j = 0; j < 4; j++) sum += bf_lo(xv[i][j]) + bf_hi(xv[i][j]);
        sum += __shfl_xor(sum, 1); sum += __shfl_xor(sum, 2); sum += __shfl_xor(sum, 4);
        float mean = sum * (1.f / 576.f);
        float sq = 0.f;
#pragma unroll
        for (int i = 0; i < 9; i++)
#pragma unroll
            for (int j = 0; j < 4; j++) {
                float a = bf_lo(xv[i][j]) - mean, b = bf_hi(xv[i][j]) - mean;
                sq += a * a + b * b;
            }
        sq += __shfl_xor(sq, 1); sq += __shfl_xor(sq, 2); sq += __shfl_xor(sq, 4);
        float rstd = rsqrtf(sq * (1.f / 576.f) + 1e-5f);
#pragma unroll
        for (int i = 0; i < 9; i++) {
            u4v ov;
#pragma unroll
            for (int j = 0; j < 4; j++)
                ov[j] = pack_bf2((bf_lo(xv[i][j]) - mean) * rstd, (bf_hi(xv[i][j]) - mean) * rstd);
            *(u4v*)(smem + i * (80 * 128) + r * 128 + (((lane & 7) ^ (r & 7)) << 4)) = ov;
        }
    };
    ln_pass(wave * 8 + (lane >> 3));
    if (wave < 2) {
        int r = 64 + wave * 8 + (lane >> 3);
        int s = s0 - 16 + r;
        const u4v* p = (const u4v*)(x + (size_t)s * D_) + (lane & 7);
#pragma unroll
        for (int i = 0; i < 9; i++) xv[i] = p[i * 8];
        ln_pass(r);
    }
    if (wave == 7 && blockIdx.x < 144) {
        const u4v* p = (const u4v*)(w1 + (size_t)(lane >> 2) * F_ + blockIdx.x * 32) + (lane & 3);
        float a[8];
#pragma unroll
        for (int j = 0; j < 8; j++) a[j] = 0.f;
#pragma unroll 6
        for (int i = 0; i < 36; i++) {
            u4v v = p[(size_t)i * 16 * F_ / 8];
#pragma unroll
            for (int j = 0; j < 4; j++) { a[2 * j] += bf_lo(v[j]); a[2 * j + 1] += bf_hi(v[j]); }
        }
#pragma unroll
        for (int j = 0; j < 8; j++) {
            float t = a[j];
            t += __shfl_xor(t, 4); t += __shfl_xor(t, 8); t += __shfl_xor(t, 16); t += __shfl_xor(t, 32);
            a[j] = t;
        }
        if (lane < 4) {
            float* q = cs1 + blockIdx.x * 32 + lane * 8;
            *(f4v*)q = f4v{a[0], a[1], a[2], a[3]};
            *(f4v*)(q + 4) = f4v{a[4], a[5], a[6], a[7]};
        }
    }
    __syncthreads();

    f4v acc[5][2];
#pragma unroll
    for (int m = 0; m < 5; m++) { acc[m][0] = f4v{0, 0, 0, 0}; acc[m][1] = f4v{0, 0, 0, 0}; }
    if (wave < 7) {
        const int g = lane >> 4, q = (lane & 15) >> 2, p = lane & 3;
        const int woff0 = (8 * g + q) * 64 + ((0 ^ (g & 1)) << 5) + 8 * p;
        const int woff1 = (8 * g + q) * 64 + ((1 ^ (g & 1)) << 5) + 8 * p;
        const int xr = lane & 15;
        for (int st = 0; st < 18; st++) {
            {
                int sn = st + 3; if (sn > 17) sn = 17;
                int slot = (st + 3) & 3;
#pragma unroll
                for (int i = 0; i < 2; i++) glds16(wsrc[i] + (size_t)sn * 32 * NQKV * 2, ringa + slot * 2048 + i * 1024);
            }
            WAIT_V(6);
            const char* wb = ring + (st & 3) * 2048;
            s8v wf[2];
            wf[0] = cat8(tr_read(wb + woff0), tr_read(wb + woff0 + 256));
            wf[1] = cat8(tr_read(wb + woff1), tr_read(wb + woff1 + 256));
            const int kb = st >> 1, ch = (st & 1) * 4 + g;
            const char* xb = smem + kb * (80 * 128);
#pragma unroll
            for (int m = 0; m < 5; m++) {
                if (m == 0 && wave < 2) continue;
                int r = m * 16 + xr;
                s8v xf = *(const s8v*)(xb + r * 128 + ((ch ^ (r & 7)) << 4));
                acc[m][0] = MFMA16(xf, wf[0], acc[m][0]);
                acc[m][1] = MFMA16(xf, wf[1], acc[m][1]);
            }
        }
        WAIT_V(0);
    }
    __syncthreads();
    float* qkv = (float*)smem;
    if (wave < 7) {
#pragma unroll
        for (int m = 0; m < 5; m++)
#pragma unroll
            for (int t = 0; t < 2; t++) {
                int col = wave * 32 + t * 16 + (lane & 15);
                if (col < 216) *(f4v*)(qkv + col * 80 + m * 16 + 4 * (lane >> 4)) = acc[m][t];
            }
    }
    __syncthreads();
    if (wave < 4) {
        const int head = wave;
        const int r = 16 + lane;
        const int s = s0 + lane;
        const float* qp = qkv + (head * 18) * 80 + r;
        const float* kp = qkv + (72 + head * 18) * 80 + r;
        const float* vp = qkv + (144 + head * 18) * 80 + r;
        float qv[18];
#pragma unroll
        for (int d = 0; d < 18; d++) qv[d] = qp[d * 80];
        float lg[8];
        float mx = -3.0e38f;
#pragma unroll
        for (int w = 0; w < 8; w++) {
            float a = 0.f;
#pragma unroll
            for (int d = 0; d < 18; d++) a += qv[d] * kp[d * 80 - 2 * w];
            a *= 0.23570226039551584f;
            lg[w] = (s - 2 * w >= 0) ? a : -1e9f;
            mx = fmaxf(mx, lg[w]);
        }
        float den = 0.f;
#pragma unroll
        for (int w = 0; w < 8; w++) { lg[w] = __expf(lg[w] - mx); den += lg[w]; }
        float inv = 1.f / den;
        float ov[18];
#pragma unroll
        for (int d = 0; d < 18; d++) ov[d] = 0.f;
#pragma unroll
        for (int w = 0; w < 8; w++) {
            float pw = lg[w] * inv;
#pragma unroll
            for (int d = 0; d < 18; d++) ov[d] += pw * vp[d * 80 - 2 * w];
        }
        unsigned* op = (unsigned*)(o + (size_t)s * D_ + hg * 72 + head * 18);
#pragma unroll
        for (int d = 0; d < 9; d++) op[d] = pack_bf2(ov[2 * d], ov[2 * d + 1]);
    }
}

constexpr int K2_A = 9 * 64 * 128;
constexpr int K2_B = 576 * 144;
constexpr int K2_LDS = K2_A + K2_B + 64;

__global__ __launch_bounds__(256, 1) void k2_wo(const bf16_t* __restrict__ o, const bf16_t* __restrict__ wo,
                                                const bf16_t* __restrict__ x, bf16_t* __restrict__ h,
                                                f2v* __restrict__ stats) {
    extern __shared__ __attribute__((aligned(16))) char smem[];
    const int tid = threadIdx.x, lane = tid & 63;
    const int wave = __builtin_amdgcn_readfirstlane(tid >> 6);
    const int ct = blockIdx.x & 7, rt = blockIdx.x >> 3;
    const int r0 = rt * 64, c0 = ct * 72;
#pragma unroll
    for (int i = 0; i < 18; i++) {
        int a = wave * 18 + i;
        int kb = a >> 3, row = (a & 7) * 8 + (lane >> 3);
        int ch = (lane & 7) ^ (row & 7);
        glds16(o + (size_t)(r0 + row) * D_ + kb * 64 + ch * 8, lds_addr(smem) + a * 1024);
    }
    for (int b = wave; b < 81; b += 4) {
        int ci = b * 64 + lane;
        int k = ci / 9, c = ci - k * 9;
        glds16(wo + (size_t)k * D_ + c0 + c * 8, lds_addr(smem) + K2_A + b * 1024);
    }
    WAIT_V(0);
    __syncthreads();
    const int g = lane >> 4, q = (lane & 15) >> 2, p = lane & 3;
    f4v acc[5];
#pragma unroll
    for (int n = 0; n < 5; n++) acc[n] = f4v{0, 0, 0, 0};
    const int xr = wave * 16 + (lane & 15);
    const char* bb = smem + K2_A + (8 * g + q) * 144 + 8 * p;
#pragma unroll 2
    for (int st = 0; st < 18; st++) {
        const int kb = st >> 1, ch = (st & 1) * 4 + g;
        s8v xf = *(const s8v*)(smem + kb * (64 * 128) + xr * 128 + ((ch ^ (xr & 7)) << 4));
#pragma unroll
        for (int n = 0; n < 5; n++) {
            const char* wb = bb + st * 32 * 144 + n * 32;
            s8v wf = cat8(tr_read(wb), tr_read(wb + 4 * 144));
            acc[n] = MFMA16(wf, xf, acc[n]);
        }
    }
    const int row = r0 + xr;
    float hv[5][4];
    float sum = 0.f;
#pragma unroll
    for (int n = 0; n < 5; n++) {
        int col = n * 16 + 4 * g;
        if (col < 72) {
            u2v xx = *(const u2v*)(x + (size_t)row * D_ + c0 + col);
            unsigned p0 = pack_bf2(bf_lo(xx[0]) + acc[n][0], bf_hi(xx[0]) + acc[n][1]);
            unsigned p1 = pack_bf2(bf_lo(xx[1]) + acc[n][2], bf_hi(xx[1]) + acc[n][3]);
            *(u2v*)(h + (size_t)row * D_ + c0 + col) = u2v{p0, p1};
            hv[n][0] = bf_lo(p0); hv[n][1] = bf_hi(p0); hv[n][2] = bf_lo(p1); hv[n][3] = bf_hi(p1);
            sum += hv[n][0] + hv[n][1] + hv[n][2] + hv[n][3];
        } else {
            hv[n][0] = hv[n][1] = hv[n][2] = hv[n][3] = 0.f;
        }
    }
    sum += __shfl_xor(sum, 16); sum += __shfl_xor(sum, 32);
    float mean = sum * (1.f / 72.f);
    float m2 = 0.f;
#pragma unroll
    for (int n = 0; n < 5; n++) {
        int col = n * 16 + 4 * g;
        if (col < 72) {
#pragma unroll
            for (int j = 0; j < 4; j++) { float d = hv[n][j] - mean; m2 += d * d; }
        }
    }
    m2 += __shfl_xor(m2, 16); m2 += __shfl_xor(m2, 32);
    if (g == 0) stats[(size_t)row * 8 + ct] = f2v{sum, m2};
}

constexpr int K3_STAGE = 8192 + 18432;
constexpr int K3_NB = 5;
constexpr int K3_PAR = K3_NB * K3_STAGE;
constexpr int K3_LDS = K3_PAR + 1024 + 2304;
#define SB0 __builtin_amdgcn_sched_barrier(0)
#define BAR __builtin_amdgcn_s_barrier()

__device__ __forceinline__ float gelu_tanh(float v) {
    float t = v * v;
    float z = v * (-2.302208198f * 0.044715f * t - 2.302208198f);
    float e = __builtin_amdgcn_exp2f(z);
    return v * __builtin_amdgcn_rcpf(1.f + e);
}

__global__ __launch_bounds__(512, 2) void k3_up(const bf16_t* __restrict__ h, const bf16_t* __restrict__ w1,
                                                const bf16_t* __restrict__ b1, const float* __restrict__ cs1,
                                                const f2v* __restrict__ stats, char* __restrict__ h1f) {
    extern __shared__ __attribute__((aligned(16))) char smem[];
    const int tid = threadIdx.x, lane = tid & 63;
    const int wave = __builtin_amdgcn_readfirstlane(tid >> 6);
    const int xcd = blockIdx.x & 7, idx = blockIdx.x >> 3;
    const int rt = (xcd & 1) * 8 + (idx & 7), ct = (xcd >> 1) * 4 + (idx >> 3);
    const int r0 = rt * 128, c0 = ct * 288;
    const unsigned lbase = lds_addr(smem);
    const int g = lane >> 4, q = (lane & 15) >> 2, p = lane & 3;
    const int xr = lane & 15;
    f2v* mu = (f2v*)(smem + K3_PAR);
    f2v* cb = (f2v*)(smem + K3_PAR + 1024);
    f4v acc[4][9];
    const int wm = (wave >> 1) & 1, wn = wave & 1;
    if (wave >= 4) {
        const int lw = wave - 4;
        unsigned asrc[2], bsrc[5];
#pragma unroll
        for (int i = 0; i < 2; i++) {
            int a = lw * 2 + i;
            int row = a * 16 + (lane >> 2);
            int ch = (lane & 3) ^ ((row & 8) >> 2);
            asrc[i] = (unsigned)(((r0 + row) * D_ + ch * 8) * 2);
        }
#pragma unroll
        for (int i = 0; i < 5; i++) {
            int bi = lw * 5 + i; if (bi > 17) bi = 17;
            int off = bi * 1024 + lane * 16;
            int k = off / 576, r = off - k * 576;
            int nt = (r >> 5) ^ ((k >> 3) & 1);
            bsrc[i] = (unsigned)((k * F_ + c0 + nt * 16 + ((r >> 4) & 1) * 8) * 2);
        }
        auto issue = [&](int st) {
            int sc = st > 17 ? 17 : st;
            unsigned lb = lbase + (st % K3_NB) * K3_STAGE;
            const char* ga = (const char*)h + sc * 64;
            const char* gb = (const char*)w1 + (size_t)sc * 32 * F_ * 2;
            glds16s(ga, asrc[0], lb + (lw * 2) * 1024);
            glds16s(ga, asrc[1], lb + (lw * 2 + 1) * 1024);
#pragma unroll
            for (int i = 0; i < 5; i++) glds16s(gb, bsrc[i], lb + 8192 + (lw * 5 + i > 17 ? 17 : lw * 5 + i) * 1024);
        };
#pragma unroll
        for (int st = 0; st < K3_NB - 1; st++) issue(st);
        {
            int t = tid - 256;
            if (t < 128) {
                const f2v* sp = stats + (size_t)(r0 + t) * 8;
                float sm[8], m2[8], tot = 0.f;
#pragma unroll
                for (int i = 0; i < 8; i++) { f2v v = sp[i]; sm[i] = v[0]; m2[i] = v[1]; tot += v[0]; }
                float mean = tot * (1.f / 576.f);
                float M2 = 0.f;
#pragma unroll
                for (int i = 0; i < 8; i++) { float d = sm[i] * (1.f / 72.f) - mean; M2 += m2[i] + 72.f * d * d; }
                mu[t] = f2v{mean, rsqrtf(M2 * (1.f / 576.f) + 1e-5f)};
            }
            for (int c = t; c < 288; c += 256) cb[c] = f2v{cs1[c0 + c], bf2f(b1[c0 + c])};
        }
        WAIT_V(21);
        BAR;
        for (int i = -1; i < 18; i++) {
            if (i >= 0) issue(i + K3_NB - 1);
            WAIT_V(14);
            BAR;
        }
        WAIT_V(0);
    } else {
#pragma unroll
        for (int m = 0; m < 4; m++)
#pragma unroll
            for (int n = 0; n < 9; n++) acc[m][n] = f4v{0, 0, 0, 0};
        const int dlt = (g & 1) ? 32 : 0;
        const int wb0 = 8192 + (8 * g + q) * 576 + wn * 9 * 32 + 8 * p;
        const int wbe = wb0 + ((wn & 1) ? -dlt : dlt);
        const int wbo = wb0 + ((wn & 1) ? dlt : -dlt);
        const int xoff = (wm * 64 + xr) * 64 + ((g ^ ((xr & 8) >> 2)) << 4);
        s8v xa[4], xb[4], wf[9];
        BAR;
        {
            const char* base = smem;
#pragma unroll
            for (int m = 0; m < 4; m++) xa[m] = *(const s8v*)(base + xoff + m * 1024);
#pragma unroll
            for (int n = 0; n < 9; n++) {
                const char* wb = base + ((n & 1) ? wbo : wbe) + n * 32;
                wf[n] = cat8(tr_read(wb), tr_read(wb + 4 * 576));
            }
        }
        WAIT_L(0);
        BAR;
#define K3_STEP(XC, XN) { \
            const char* base = smem + ((i + 1) % K3_NB) * K3_STAGE; \
            _Pragma("unroll") for (int m = 0; m < 4; m++) XN[m] = *(const s8v*)(base + xoff + m * 1024); \
            SB0; \
            _Pragma("unroll") for (int n = 0; n < 9; n++) { \
                _Pragma("unroll") for (int m = 0; m < 4; m++) acc[m][n] = MFMA16(wf[n], XC[m], acc[m][n]); \
                SB0; \
                const char* wb = base + ((n & 1) ? wbo : wbe) + n * 32; \
                wf[n] = cat8(tr_read(wb), tr_read(wb + 4 * 576)); \
                SB0; \
            } \
            BAR; }
#pragma unroll 1
        for (int i = 0; i < 18; i += 2) {
            K3_STEP(xa, xb);
            i++; K3_STEP(xb, xa); i--;
        }
    }
    __syncthreads();
    f4v* ex = (f4v*)smem + (wave & 3) * (18 * 64) + lane;
    if (wave < 4) {
#pragma unroll
        for (int m = 0; m < 2; m++)
#pragma unroll
            for (int n = 0; n < 9; n++) ex[(m * 9 + n) * 64] = acc[m + 2][n];
    }
    __syncthreads();
    if (wave >= 4) {
#pragma unroll
        for (int m = 0; m < 2; m++)
#pragma unroll
            for (int n = 0; n < 9; n++) acc[m][n] = ex[(m * 9 + n) * 64];
    }
    {
        const int mb = (wave >> 2) * 2;
#pragma unroll
        for (int m = 0; m < 2; m++) {
            int rl = wm * 64 + (mb + m) * 16 + xr;
            f2v ms = mu[rl];
            char* ob = h1f + (size_t)((r0 + wm * 64 + (mb + m) * 16) >> 4) * (144 * 1024) + lane * 16;
#pragma unroll
            for (int n = 0; n < 9; n++) {
                f4v a = acc[m][n];
                int cl = wn * 144 + n * 16 + 4 * g;
                f4v cA = *(const f4v*)(cb + cl), cB = *(const f4v*)(cb + cl + 2);
                float v0 = gelu_tanh((a[0] - ms[0] * cA[0]) * ms[1] + cA[1]);
                float v1 = gelu_tanh((a[1] - ms[0] * cA[2]) * ms[1] + cA[3]);
                float v2 = gelu_tanh((a[2] - ms[0] * cB[0]) * ms[1] + cB[1]);
                float v3 = gelu_tanh((a[3] - ms[0] * cB[2]) * ms[1] + cB[3]);
                int cg = c0 + wn * 144 + n * 16;
                *(u2v*)(ob + (cg >> 5) * 1024 + ((cg >> 4) & 1) * 8) = u2v{pack_bf2(v0, v1), pack_bf2(v2, v3)};
            }
        }
    }
}

constexpr int K4_STAGE = 16384 + 8 * 2432;
constexpr int K4_NB = 4;
constexpr int K4_LDS = 147456;

__global__ __launch_bounds__(512, 2) void k4_down(const char* __restrict__ h1f, const bf16_t* __restrict__ w2,
                                                  float* __restrict__ part) {
    extern __shared__ __attribute__((aligned(16))) char smem[];
    const int tid = threadIdx.x, lane = tid & 63;
    const int wave = __builtin_amdgcn_readfirstlane(tid >> 6);
    const int xcd = blockIdx.x & 7, idx = blockIdx.x >> 3;
    const int ks = xcd >> 1;
    const int rt = (xcd & 1) * 8 + (idx & 7), ct = idx >> 3;
    const int r0 = rt * 128, c0 = ct * 144, k0 = ks * 1152;
    const unsigned lbase = lds_addr(smem);
    const int g = lane >> 4, q = (lane & 15) >> 2, p = lane & 3;
    const int wk = (wave >> 1) & 1, wm = wave & 1;
    f4v acc[4][9];
    if (wave >= 4) {
        const int lw = wave - 4;
        unsigned bsrc[5];
#pragma unroll
        for (int i = 0; i < 5; i++) {
            int bi = lw * 5 + i; if (bi > 18) bi = 18;
            int off = bi * 1024 + lane * 16;
            int blk = off / 2432, r = off - blk * 2432;
            int kk = r / 288, cr = r - kk * 288;
            if (kk > 7) { kk = 7; cr = 0; }
            bsrc[i] = (unsigned)(((k0 + blk * 8 + kk) * D_ + c0) * 2 + cr);
        }
        const char* xg = h1f + ((size_t)(rt * 8 + lw * 2) * 144 + (k0 >> 5)) * 1024;
        const unsigned xvo = lane * 16;
        auto issue = [&](int st) {
            int sc = st > 17 ? 17 : st;
            unsigned lb = lbase + (st % K4_NB) * K4_STAGE;
            const char* gx = xg + sc * 2048;
            glds16s(gx, xvo, lb + (lw * 4) * 1024);
            glds16s(gx + 1024, xvo, lb + (lw * 4 + 1) * 1024);
            glds16s(gx + 144 * 1024, xvo, lb + (lw * 4 + 2) * 1024);
            glds16s(gx + 145 * 1024, xvo, lb + (lw * 4 + 3) * 1024);
            const char* gb = (const char*)w2 + (size_t)sc * 64 * D_ * 2;
#pragma unroll
            for (int i = 0; i < 5; i++) glds16s(gb, bsrc[i], lb + 16384 + (lw * 5 + i > 18 ? 18 : lw * 5 + i) * 1024);
        };
#pragma unroll
        for (int st = 0; st < K4_NB - 1; st++) issue(st);
        WAIT_V(18);
        BAR;
        for (int i = -1; i < 18; i++) {
            if (i >= 0) issue(i + K4_NB - 1);
            WAIT_V(9);
            BAR;
        }
        WAIT_V(0);
    } else {
#pragma unroll
        for (int m = 0; m < 4; m++)
#pragma unroll
            for (int n = 0; n < 9; n++) acc[m][n] = f4v{0, 0, 0, 0};
        const int kA = wk * 32 + 4 * g + q, kB = kA + 16;
        const int wbA = 16384 + (kA >> 3) * 2432 + (kA & 7) * 288 + 8 * p;
        const int wbB = 16384 + (kB >> 3) * 2432 + (kB & 7) * 288 + 8 * p;
        const int xoff = (wm * 8 + wk) * 1024 + lane * 16;
        s8v xa[4], xb[4], wf[9];
        BAR;
        {
            const char* base = smem;
#pragma unroll
            for (int m = 0; m < 4; m++) xa[m] = *(const s8v*)(base + xoff + m * 2048);
#pragma unroll
            for (int n = 0; n < 9; n++) wf[n] = cat8(tr_read(base + wbA + n * 32), tr_read(base + wbB + n * 32));
        }
        WAIT_L(0);
        BAR;
#define K4_STEP(XC, XN) { \
            const char* base = smem + ((i + 1) % K4_NB) * K4_STAGE; \
            _Pragma("unroll") for (int m = 0; m < 4; m++) XN[m] = *(const s8v*)(base + xoff + m * 2048); \
            SB0; \
            _Pragma("unroll") for (int n = 0; n < 9; n++) { \
                _Pragma("unroll") for (int m = 0; m < 4; m++) acc[m][n] = MFMA16(wf[n], XC[m], acc[m][n]); \
                SB0; \
                wf[n] = cat8(tr_read(base + wbA + n * 32), tr_read(base + wbB + n * 32)); \
                SB0; \
            } \
            BAR; }
#pragma unroll 1
        for (int i = 0; i < 18; i += 2) {
            K4_STEP(xa, xb);
            i++; K4_STEP(xb, xa); i--;
        }
    }
    __syncthreads();
    if (wave < 4) {
        f4v* ex = (f4v*)smem + wave * (36 * 64) + lane;
#pragma unroll
        for (int m = 0; m < 4; m++)
#pragma unroll
            for (int n = 0; n < 9; n++) ex[(m * 9 + n) * 64] = acc[m][n];
    }
    __syncthreads();
    {
        const int om = wave >> 1;
        const f4v* e0 = (const f4v*)smem + (wm) * (36 * 64) + (om * 9) * 64 + lane;
        const f4v* e1 = (const f4v*)smem + (2 + wm) * (36 * 64) + (om * 9) * 64 + lane;
        const int row = r0 + wm * 64 + om * 16 + (lane & 15);
        float* pp = part + (size_t)ks * S_ * D_ + (size_t)row * D_ + c0 + 4 * g;
#pragma unroll
        for (int n = 0; n < 9; n++) *(f4v*)(pp + n * 16) = e0[n * 64] + e1[n * 64];
    }
}

__global__ __launch_bounds__(256) void k5_reduce(const float* __restrict__ part, const bf16_t* __restrict__ h,
                                                 const bf16_t* __restrict__ b2, bf16_t* __restrict__ out) {
    int i = blockIdx.x * 256 + threadIdx.x;
    size_t e = (size_t)i * 4;
    int col = (int)(e % D_);
    f4v a = *(const f4v*)(part + e);
#pragma unroll
    for (int k = 1; k < 4; k++) a += *(const f4v*)(part + (size_t)k * S_ * D_ + e);
    u2v hh = *(const u2v*)(h + e);
    u2v bb = *(const u2v*)(b2 + col);
    float v0 = bf_lo(hh[0]) + (a[0] + bf_lo(bb[0]));
    float v1 = bf_hi(hh[0]) + (a[1] + bf_hi(bb[0]));
    float v2 = bf_lo(hh[1]) + (a[2] + bf_lo(bb[1]));
    float v3 = bf_hi(hh[1]) + (a[3] + bf_hi(bb[1]));
    *(u2v*)(out + e) = u2v{pack_bf2(v0, v1), pack_bf2(v2, v3)};
}

extern "C" void kernel_launch(void* const* d_in, const int* in_sizes, int n_in,
                              void* d_out, int out_size, void* d_ws, size_t ws_size,
                              hipStream_t stream) {
    const bf16_t* x = (const bf16_t*)d_in[0];
    const bf16_t* wqkv = (const bf16_t*)d_in[1];
    const bf16_t* wo = (const bf16_t*)d_in[2];
    const bf16_t* w1 = (const bf16_t*)d_in[4];
    const bf16_t* b1 = (const bf16_t*)d_in[5];
    const bf16_t* w2 = (const bf16_t*)d_in[6];
    const bf16_t* b2 = (const bf16_t*)d_in[7];
    bf16_t* out = (bf16_t*)d_out;
    char* ws = (char*)d_ws;
    bf16_t* o = (bf16_t*)(ws);
    bf16_t* h = (bf16_t*)(ws + (4 << 20));
    f2v* stats = (f2v*)(ws + (8 << 20));
    float* cs1 = (float*)(ws + (9 << 20));
    char* h1 = (char*)(ws + (16 << 20));
    float* part = (float*)(ws + (48 << 20));

    hipFuncSetAttribute((const void*)k1_qkv_attn, hipFuncAttributeMaxDynamicSharedMemorySize, K1_LDS);
    hipFuncSetAttribute((const void*)k2_wo, hipFuncAttributeMaxDynamicSharedMemorySize, K2_LDS);
    hipFuncSetAttribute((const void*)k3_up, hipFuncAttributeMaxDynamicSharedMemorySize, K3_LDS);
    hipFuncSetAttribute((const void*)k4_down, hipFuncAttributeMaxDynamicSharedMemorySize, K4_LDS);

    k1_qkv_attn<<<256, 512, K1_LDS, stream>>>(x, wqkv, w1, o, cs1);
    k2_wo<<<256, 256, K2_LDS, stream>>>(o, wo, x, h, stats);
    k3_up<<<256, 512, K3_LDS, stream>>>(h, w1, b1, cs1, stats, h1);
    k4_down<<<256, 512, K4_LDS, stream>>>(h1, w2, part);
    k5_reduce<<<S_ * D_ / 4 / 256, 256, 0, stream>>>(part, h, b2, out);
}
```

```cpp
#include <hip/hip_runtime.h>
#include <hip/hip_bf16.h>

typedef __attribute__((ext_vector_type(8))) short s8v;
typedef __attribute__((ext_vector_type(4))) short s4v;
typedef __attribute__((ext_vector_type(4))) short v4i16_t;
typedef __attribute__((ext_vector_type(4))) float f4v;
typedef __attribute__((ext_vector_type(2))) float f2v;
typedef __attribute__((ext_vector_type(4))) unsigned u4v;
typedef __attribute__((ext_vector_type(2))) unsigned u2v;
typedef unsigned short bf16_t;

constexpr int S_ = 2048, D_ = 576, NQKV = 1728, F_ = 4608;

__device__ __forceinline__ unsigned lds_addr(const void* l) {
    return (unsigned)(size_t)(__attribute__((address_space(3))) const char*)l;
}
__device__ __forceinline__ void glds16(const void* g, unsigned dst) {
    unsigned keep;
    asm volatile("s_mov_b32 %0, m0\n\ts_mov_b32 m0, %2\n\ts_nop 0\n\tglobal_load_lds_dwordx4 %1, off\n\ts_mov_b32 m0, %0"
                 : "=&s"(keep) : "v"(g), "s"(dst) : "memory");
}
__device__ __forceinline__ void glds16s(const void* sbase, unsigned voff, unsigned dst) {
    unsigned keep;
    asm volatile("s_mov_b32 %0, m0\n\ts_mov_b32 m0, %3\n\ts_nop 0\n\tglobal_load_lds_dwordx4 %1, %2\n\ts_mov_b32 m0, %0"
                 : "=&s"(keep) : "v"(voff), "s"(sbase), "s"(dst) : "memory");
}
__device__ __forceinline__ s4v tr_read(const char* p) {
    return __builtin_bit_cast(s4v, __builtin_amdgcn_ds_read_tr16_b64_v4i16((__attribute__((address_space(3))) v4i16_t*)p));
}
__device__ __forceinline__ s8v cat8(s4v a, s4v b) {
    return __builtin_shufflevector(a, b, 0, 1, 2, 3, 4, 5, 6, 7);
}
__device__ __forceinline__ float bf_lo(unsigned u) { return __builtin_bit_cast(float, u << 16); }
__device__ __forceinline__ float bf_hi(unsigned u) { return __builtin_bit_cast(float, u & 0xffff0000u); }
__device__ __forceinline__ float bf2f(bf16_t v) { return __builtin_bit_cast(float, (unsigned)v << 16); }
__device__ __forceinline__ unsigned pack_bf2(float a, float b) {
    unsigned r;
    asm("v_cvt_pk_bf16_f32 %0, %1, %2" : "=v"(r) : "v"(a), "v"(b));
    return r;
}
#define MFMA16(a, b, c) __builtin_amdgcn_mfma_f32_16x16x32_bf16(a, b, c, 0, 0, 0)
#define WAIT_V(n) asm volatile("s_waitcnt vmcnt(" #n ")" ::: "memory")
#define WAIT_L(n) asm volatile("s_waitcnt lgkmcnt(" #n ")" ::: "memory")

#define SB0 __builtin_amdgcn_sched_barrier(0)
#define BAR __builtin_amdgcn_s_barrier()
constexpr int K1_XN_BYTES = 9 * 80 * 128;
constexpr int K1_STAGE = 14336;
constexpr int K1_NB = 4;
constexpr int K1_LDS = K1_XN_BYTES + K1_NB * K1_STAGE;

__global__ __launch_bounds__(512, 2) void k1_qkv_attn(const bf16_t* __restrict__ x, const bf16_t* __restrict__ wqkv,
                                                      const bf16_t* __restrict__ w1, bf16_t* __restrict__ o,
                                                      float* __restrict__ cs1) {
    extern __shared__ __attribute__((aligned(16))) char smem[];
    const int tid = threadIdx.x, lane = tid & 63;
    const int wave = __builtin_amdgcn_readfirstlane(tid >> 6);
    const int hg = blockIdx.x & 7, rt = blockIdx.x >> 3;
    const int s0 = rt * 64;
    const unsigned lring = lds_addr(smem) + K1_XN_BYTES;
    const int g = lane >> 4, q = (lane & 15) >> 2, p = lane & 3;

    u4v xv[9], xw[9];
    {
        int r = wave * 8 + (lane >> 3);
        int s = s0 - 16 + r; if (s < 0) s = 0;
        const u4v* pp = (const u4v*)(x + (size_t)s * D_) + (lane & 7);
#pragma unroll
        for (int i = 0; i < 9; i++) xv[i] = pp[i * 8];
        if (wave < 2) {
            const u4v* p2 = (const u4v*)(x + (size_t)(s0 + 48 + wave * 8 + (lane >> 3)) * D_) + (lane & 7);
#pragma unroll
            for (int i = 0; i < 9; i++) xw[i] = p2[i * 8];
        }
    }
    const int lw = wave - 4;
    unsigned bsrc[4];
    auto issue = [&](int st) {
        int sc = st > 17 ? 17 : st;
        unsigned lb = lring + (st % K1_NB) * K1_STAGE;
        const char* gb = (const char*)wqkv + (size_t)sc * 32 * NQKV * 2;
#pragma unroll
        for (int i = 0; i < 4; i++) glds16s(gb, bsrc[i], lb + (lw * 4 + i > 13 ? 13 : lw * 4 + i) * 1024);
    };
    if (wave >= 4) {
#pragma unroll
        for (int i = 0; i < 4; i++) {
            int bi = lw * 4 + i; if (bi > 13) bi = 13;
            int off = bi * 1024 + lane * 16;
            int k = off / 448, r = off - k * 448;
            int nt = (r >> 5) ^ ((k >> 3) & 1);
            int pc = nt * 2 + ((r >> 4) & 1); if (pc > 26) pc = 26;
            int sec = pc / 9, wi = pc - sec * 9;
            bsrc[i] = (unsigned)((k * NQKV + sec * 576 + hg * 72 + wi * 8) * 2);
        }
#pragma unroll
        for (int st = 0; st < K1_NB - 1; st++) issue(st);
    }
    auto ln_pass = [&](const u4v* xx, int r) {
        float sum = 0.f;
#pragma unroll
        for (int i = 0; i < 9; i++)
#pragma unroll
            for (int j = 0; j < 4; j++) sum += bf_lo(xx[i][j]) + bf_hi(xx[i][j]);
        sum += __shfl_xor(sum, 1); sum += __shfl_xor(sum, 2); sum += __shfl_xor(sum, 4);
        float mean = sum * (1.f / 576.f);
        float sq = 0.f;
#pragma unroll
        for (int i = 0; i < 9; i++)
#pragma unroll
            for (int j = 0; j < 4; j++) {
                float a = bf_lo(xx[i][j]) - mean, b = bf_hi(xx[i][j]) - mean;
                sq += a * a + b * b;
            }
        sq += __shfl_xor(sq, 1); sq += __shfl_xor(sq, 2); sq += __shfl_xor(sq, 4);
        float rstd = rsqrtf(sq * (1.f / 576.f) + 1e-5f);
        float mr = -mean * rstd;
#pragma unroll
        for (int i = 0; i < 9; i++) {
            u4v ov;
#pragma unroll
            for (int j = 0; j < 4; j++)
                ov[j] = pack_bf2(fmaf(bf_lo(xx[i][j]), rstd, mr), fmaf(bf_hi(xx[i][j]), rstd, mr));
            *(u4v*)(smem + i * (80 * 128) + r * 128 + (((lane & 7) ^ (r & 7)) << 4)) = ov;
        }
    };
    ln_pass(xv, wave * 8 + (lane >> 3));
    if (wave < 2) ln_pass(xw, 64 + wave * 8 + (lane >> 3));

    f4v acc[5][4];
    if (wave >= 4) {
        WAIT_V(8);
        __syncthreads();
        for (int i = -1; i < 18; i++) {
            if (i >= 0) issue(i + K1_NB - 1);
            WAIT_V(4);
            BAR;
        }
        if (wave == 7 && blockIdx.x < 144) {
            const u4v* pw = (const u4v*)(w1 + (size_t)(lane >> 2) * F_ + blockIdx.x * 32) + (lane & 3);
            float a[8];
#pragma unroll
            for (int j = 0; j < 8; j++) a[j] = 0.f;
#pragma unroll 12
            for (int i = 0; i < 36; i++) {
                u4v v = pw[(size_t)i * 16 * F_ / 8];
#pragma unroll
                for (int j = 0; j < 4; j++) { a[2 * j] += bf_lo(v[j]); a[2 * j + 1] += bf_hi(v[j]); }
            }
#pragma unroll
            for (int j = 0; j < 8; j++) {
                float t = a[j];
                t += __shfl_xor(t, 4); t += __shfl_xor(t, 8); t += __shfl_xor(t, 16); t += __shfl_xor(t, 32);
                a[j] = t;
            }
            if (lane < 4) {
                float* qq = cs1 + blockIdx.x * 32 + lane * 8;
                *(f4v*)qq = f4v{a[0], a[1], a[2], a[3]};
                *(f4v*)(qq + 4) = f4v{a[4], a[5], a[6], a[7]};
            }
        }
        WAIT_V(0);
    } else {
        const int nt0 = wave == 0 ? 0 : 1 + 3 * wave;
        const int nn = (wave == 0 || wave == 3) ? 4 : 3;
#pragma unroll
        for (int m = 0; m < 5; m++)
#pragma unroll
            for (int n = 0; n < 4; n++) acc[m][n] = f4v{0, 0, 0, 0};
        const int xr = lane & 15;
        int woff[4];
#pragma unroll
        for (int n = 0; n < 4; n++) woff[n] = K1_XN_BYTES + (8 * g + q) * 448 + (((nt0 + n) ^ (g & 1)) << 5) + 8 * p;
        s8v xa[5], xb[5], wf[4];
        auto rdx = [&](s8v* X, int st) {
            const char* xbp = smem + (st >> 1) * (80 * 128) + xr * 128 + ((((st & 1) * 4 + g) ^ (xr & 7)) << 4);
#pragma unroll
            for (int m = 0; m < 5; m++) X[m] = *(const s8v*)(xbp + m * 2048);
        };
        __syncthreads();
        rdx(xa, 0);
#pragma unroll
        for (int n = 0; n < 4; n++) wf[n] = cat8(tr_read(smem + woff[n]), tr_read(smem + woff[n] + 4 * 448));
        WAIT_L(0);
        BAR;
#define K1_STEP(XC, XN) { \
            const char* base = smem + ((i + 1) % K1_NB) * K1_STAGE; \
            rdx(XN, i + 1 > 17 ? 17 : i + 1); \
            SB0; \
            _Pragma("unroll") for (int n = 0; n < 4; n++) { \
                if (n < 3 || nn == 4) { \
                    if (wave != 0) acc[0][n] = MFMA16(XC[0], wf[n], acc[0][n]); \
                    _Pragma("unroll") for (int m = 1; m < 5; m++) acc[m][n] = MFMA16(XC[m], wf[n], acc[m][n]); \
                    SB0; \
                    wf[n] = cat8(tr_read(base + woff[n]), tr_read(base + woff[n] + 4 * 448)); \
                    SB0; \
                } \
            } \
            BAR; }
#pragma unroll 1
        for (int i = 0; i < 18; i += 2) {
            K1_STEP(xa, xb);
            i++; K1_STEP(xb, xa); i--;
        }
    }
    __syncthreads();
    float* qkv = (float*)smem;
    if (wave < 4) {
        const int nt0 = wave == 0 ? 0 : 1 + 3 * wave;
        const int nn = (wave == 0 || wave == 3) ? 4 : 3;
#pragma unroll
        for (int m = 0; m < 5; m++)
#pragma unroll
            for (int n = 0; n < 4; n++) {
                int col = (nt0 + n) * 16 + (lane & 15);
                if (n < nn && col < 216) *(f4v*)(qkv + col * 80 + m * 16 + 4 * g) = acc[m][n];
            }
    }
    __syncthreads();
    {
        const int head = wave >> 1;
        const int rl = (wave & 1) * 32 + (lane & 31);
        const int hw = lane >> 5;
        const int r = 16 + rl;
        const int s = s0 + rl;
        const float* qp = qkv + (head * 18) * 80 + r;
        const float* kp = qkv + (72 + head * 18) * 80 + r - 8 * hw;
        const float* vp = qkv + (144 + head * 18) * 80 + r - 8 * hw;
        float qv[18];
#pragma unroll
        for (int d = 0; d < 18; d++) qv[d] = qp[d * 80];
        float lg[4];
        float mx = -3.0e38f;
#pragma unroll
        for (int w = 0; w < 4; w++) {
            float a = 0.f;
#pragma unroll
            for (int d = 0; d < 18; d++) a += qv[d] * kp[d * 80 - 2 * w];
            a *= 0.23570226039551584f;
            lg[w] = (s - 2 * (w + 4 * hw) >= 0) ? a : -1e9f;
            mx = fmaxf(mx, lg[w]);
        }
        mx = fmaxf(mx, __shfl_xor(mx, 32));
        float den = 0.f;
#pragma unroll
        for (int w = 0; w < 4; w++) { lg[w] = __expf(lg[w] - mx); den += lg[w]; }
        den += __shfl_xor(den, 32);
        float inv = 1.f / den;
        float ov[18];
#pragma unroll
        for (int d = 0; d < 18; d++) ov[d] = 0.f;
#pragma unroll
        for (int w = 0; w < 4; w++) {
            float pw = lg[w] * inv;
#pragma unroll
            for (int d = 0; d < 18; d++) ov[d] += pw * vp[d * 80 - 2 * w];
        }
#pragma unroll
        for (int d = 0; d < 18; d++) ov[d] += __shfl_xor(ov[d], 32);
        unsigned* op = (unsigned*)(o + (size_t)s * D_ + hg * 72 + head * 18);
        if (hw == 0) {
#pragma unroll
            for (int d = 0; d < 5; d++) op[d] = pack_bf2(ov[2 * d], ov[2 * d + 1]);
        } else {
#pragma unroll
            for (int d = 5; d < 9; d++) op[d] = pack_bf2(ov[2 * d], ov[2 * d + 1]);
        }
    }
}

constexpr int K2_CH = 8192 + 64 * 144;
constexpr int K2_LDS = 9 * K2_CH + 64;

__global__ __launch_bounds__(512, 2) void k2_wo(const bf16_t* __restrict__ o, const bf16_t* __restrict__ wo,
                                                const bf16_t* __restrict__ x, bf16_t* __restrict__ h,
                                                f2v* __restrict__ stats) {
    extern __shared__ __attribute__((aligned(16))) char smem[];
    const int tid = threadIdx.x, lane = tid & 63;
    const int wave = __builtin_amdgcn_readfirstlane(tid >> 6);
    const int ct = blockIdx.x & 7, rt = blockIdx.x >> 3;
    const int r0 = rt * 64, c0 = ct * 72;
    const int g = lane >> 4, q = (lane & 15) >> 2, p = lane & 3;
    if (wave >= 4) {
        const int lw = wave - 4;
        const unsigned lbase = lds_addr(smem);
        unsigned asrc[2], bsrc[3];
#pragma unroll
        for (int i = 0; i < 2; i++) {
            int a = lw * 2 + i;
            int row = a * 8 + (lane >> 3);
            int ch = (lane & 7) ^ (row & 7);
            asrc[i] = (unsigned)(((r0 + row) * D_ + ch * 8) * 2);
        }
#pragma unroll
        for (int i = 0; i < 3; i++) {
            int bi = lw + 4 * i; if (bi > 8) bi = 8;
            int ci = bi * 64 + lane;
            int k = ci / 9, c = ci - k * 9;
            bsrc[i] = (unsigned)((k * D_ + c0 + c * 8) * 2);
        }
#pragma unroll
        for (int c = 0; c < 9; c++) {
            unsigned lb = lbase + c * K2_CH;
            glds16s((const char*)o + c * 128, asrc[0], lb + (lw * 2) * 1024);
            glds16s((const char*)o + c * 128, asrc[1], lb + (lw * 2 + 1) * 1024);
#pragma unroll
            for (int i = 0; i < 3; i++) glds16s((const char*)wo + (size_t)c * 64 * D_ * 2, bsrc[i], lb + 8192 + (lw + 4 * i > 8 ? 8 : lw + 4 * i) * 1024);
        }
        WAIT_V(40); BAR;
        WAIT_V(35); BAR;
        WAIT_V(30); BAR;
        WAIT_V(25); BAR;
        WAIT_V(20); BAR;
        WAIT_V(15); BAR;
        WAIT_V(10); BAR;
        WAIT_V(5); BAR;
        WAIT_V(0); BAR;
        return;
    }
    const int xr = wave * 16 + (lane & 15);
    const int row = r0 + xr;
    u2v xx[5];
#pragma unroll
    for (int n = 0; n < 5; n++) {
        int col = n * 16 + 4 * g; if (col > 68) col = 68;
        xx[n] = *(const u2v*)(x + (size_t)row * D_ + c0 + col);
    }
    f4v acc[5];
#pragma unroll
    for (int n = 0; n < 5; n++) acc[n] = f4v{0, 0, 0, 0};
    const int boff = 8192 + (8 * g + q) * 144 + 8 * p;
#pragma unroll 1
    for (int c = 0; c < 9; c++) {
        BAR;
        const char* base = smem + c * K2_CH;
        s8v xf[2], wf[2][5];
#pragma unroll
        for (int ss = 0; ss < 2; ss++) {
            xf[ss] = *(const s8v*)(base + xr * 128 + (((ss * 4 + g) ^ (xr & 7)) << 4));
#pragma unroll
            for (int n = 0; n < 5; n++) {
                const char* wb = base + boff + ss * 32 * 144 + n * 32;
                wf[ss][n] = cat8(tr_read(wb), tr_read(wb + 4 * 144));
            }
        }
#pragma unroll
        for (int ss = 0; ss < 2; ss++)
#pragma unroll
            for (int n = 0; n < 5; n++) acc[n] = MFMA16(wf[ss][n], xf[ss], acc[n]);
    }
    float hv[5][4];
    float sum = 0.f;
#pragma unroll
    for (int n = 0; n < 5; n++) {
        int col = n * 16 + 4 * g;
        if (col < 72) {
            unsigned p0 = pack_bf2(bf_lo(xx[n][0]) + acc[n][0], bf_hi(xx[n][0]) + acc[n][1]);
            unsigned p1 = pack_bf2(bf_lo(xx[n][1]) + acc[n][2], bf_hi(xx[n][1]) + acc[n][3]);
            *(u2v*)(h + (size_t)row * D_ + c0 + col) = u2v{p0, p1};
            hv[n][0] = bf_lo(p0); hv[n][1] = bf_hi(p0); hv[n][2] = bf_lo(p1); hv[n][3] = bf_hi(p1);
            sum += hv[n][0] + hv[n][1] + hv[n][2] + hv[n][3];
        } else {
            hv[n][0] = hv[n][1] = hv[n][2] = hv[n][3] = 0.f;
        }
    }
    sum += __shfl_xor(sum, 16); sum += __shfl_xor(sum, 32);
    float mean = sum * (1.f / 72.f);
    float m2 = 0.f;
#pragma unroll
    for (int n = 0; n < 5; n++) {
        int col = n * 16 + 4 * g;
        if (col < 72) {
#pragma unroll
            for (int j = 0; j < 4; j++) { float d = hv[n][j] - mean; m2 += d * d; }
        }
    }
    m2 += __shfl_xor(m2, 16); m2 += __shfl_xor(m2, 32);
    if (g == 0) stats[(size_t)row * 8 + ct] = f2v{sum, m2};
}

constexpr int K3_STAGE = 8192 + 18432;
constexpr int K3_NB = 5;
constexpr int K3_PAR = K3_NB * K3_STAGE;
constexpr int K3_LDS = K3_PAR + 1024 + 2304;

__device__ __forceinline__ float gelu_tanh(float v) {
    float t = v * v;
    float z = v * (-2.302208198f * 0.044715f * t - 2.302208198f);
    float e = __builtin_amdgcn_exp2f(z);
    return v * __builtin_amdgcn_rcpf(1.f + e);
}

__device__ __forceinline__ f2v gelu_tanh2(f2v v) {
    f2v t = v * v;
    f2v z = v * (t * (-2.302208198f * 0.044715f) + (-2.302208198f));
    f2v d = f2v{__builtin_amdgcn_exp2f(z[0]), __builtin_amdgcn_exp2f(z[1])} + 1.f;
    return v * f2v{__builtin_amdgcn_rcpf(d[0]), __builtin_amdgcn_rcpf(d[1])};
}

__global__ __launch_bounds__(512, 2) void k3_up(const bf16_t* __restrict__ h, const bf16_t* __restrict__ w1,
                                                const bf16_t* __restrict__ b1, const float* __restrict__ cs1,
                                                const f2v* __restrict__ stats, char* __restrict__ h1f) {
    extern __shared__ __attribute__((aligned(16))) char smem[];
    const int tid = threadIdx.x, lane = tid & 63;
    const int wave = __builtin_amdgcn_readfirstlane(tid >> 6);
    const int xcd = blockIdx.x & 7, idx = blockIdx.x >> 3;
    const int rt = (xcd & 1) * 8 + (idx & 7), ct = (xcd >> 1) * 4 + (idx >> 3);
    const int r0 = rt * 128, c0 = ct * 288;
    const unsigned lbase = lds_addr(smem);
    const int g = lane >> 4, q = (lane & 15) >> 2, p = lane & 3;
    const int xr = lane & 15;
    f2v* mu = (f2v*)(smem + K3_PAR);
    float* csl = (float*)(smem + K3_PAR + 1024);
    float* bbl = csl + 288;
    f4v acc[4][9];
    const int wm = (wave >> 1) & 1, wn = wave & 1;
    if (wave >= 4) {
        const int lw = wave - 4;
        unsigned asrc[2], bsrc[5];
#pragma unroll
        for (int i = 0; i < 2; i++) {
            int a = lw * 2 + i;
            int row = a * 16 + (lane >> 2);
            int ch = (lane & 3) ^ ((row & 8) >> 2);
            asrc[i] = (unsigned)(((r0 + row) * D_ + ch * 8) * 2);
        }
#pragma unroll
        for (int i = 0; i < 5; i++) {
            int bi = lw * 5 + i; if (bi > 17) bi = 17;
            int off = bi * 1024 + lane * 16;
            int k = off / 576, r = off - k * 576;
            int nt = (r >> 5) ^ ((k >> 3) & 1);
            bsrc[i] = (unsigned)((k * F_ + c0 + nt * 16 + ((r >> 4) & 1) * 8) * 2);
        }
        auto issue = [&](int st) {
            int sc = st > 17 ? 17 : st;
            unsigned lb = lbase + (st % K3_NB) * K3_STAGE;
            const char* ga = (const char*)h + sc * 64;
            const char* gb = (const char*)w1 + (size_t)sc * 32 * F_ * 2;
            glds16s(ga, asrc[0], lb + (lw * 2) * 1024);
            glds16s(ga, asrc[1], lb + (lw * 2 + 1) * 1024);
#pragma unroll
            for (int i = 0; i < 5; i++) glds16s(gb, bsrc[i], lb + 8192 + (lw * 5 + i > 17 ? 17 : lw * 5 + i) * 1024);
        };
#pragma unroll
        for (int st = 0; st < K3_NB - 1; st++) issue(st);
        WAIT_V(21);
        BAR;
        for (int i = -1; i < 18; i++) {
            if (i >= 0) issue(i + K3_NB - 1);
            WAIT_V(14);
            BAR;
        }
        WAIT_V(0);
    } else {
        {
            float c0v = cs1[c0 + tid], b0v = bf2f(b1[c0 + tid]);
            float c1v = 0.f, b1v = 0.f;
            if (tid < 32) { c1v = cs1[c0 + 256 + tid]; b1v = bf2f(b1[c0 + 256 + tid]); }
            if (tid < 128) {
                const f4v* sp = (const f4v*)(stats + (size_t)(r0 + tid) * 8);
                f4v v[4];
#pragma unroll
                for (int i = 0; i < 4; i++) v[i] = sp[i];
                float tot = 0.f;
#pragma unroll
                for (int i = 0; i < 4; i++) tot += v[i][0] + v[i][2];
                float mean = tot * (1.f / 576.f);
                float M2 = 0.f;
#pragma unroll
                for (int i = 0; i < 4; i++) {
                    float d0 = v[i][0] * (1.f / 72.f) - mean, d1 = v[i][2] * (1.f / 72.f) - mean;
                    M2 += v[i][1] + v[i][3] + 72.f * (d0 * d0 + d1 * d1);
                }
                float rstd = rsqrtf(M2 * (1.f / 576.f) + 1e-5f);
                mu[tid] = f2v{mean * rstd, rstd};
            }
            csl[tid] = c0v; bbl[tid] = b0v;
            if (tid < 32) { csl[256 + tid] = c1v; bbl[256 + tid] = b1v; }
        }
#pragma unroll
        for (int m = 0; m < 4; m++)
#pragma unroll
            for (int n = 0; n < 9; n++) acc[m][n] = f4v{0, 0, 0, 0};
        const int dlt = (g & 1) ? 32 : 0;
        const int wb0 = 8192 + (8 * g + q) * 576 + wn * 9 * 32 + 8 * p;
        const int wbe = wb0 + ((wn & 1) ? -dlt : dlt);
        const int wbo = wb0 + ((wn & 1) ? dlt : -dlt);
        const int xoff = (wm * 64 + xr) * 64 + ((g ^ ((xr & 8) >> 2)) << 4);
        s8v xa[4], xb[4], wf[9];
        BAR;
        {
            const char* base = smem;
#pragma unroll
            for (int m = 0; m < 4; m++) xa[m] = *(const s8v*)(base + xoff + m * 1024);
#pragma unroll
            for (int n = 0; n < 9; n++) {
                const char* wb = base + ((n & 1) ? wbo : wbe) + n * 32;
                wf[n] = cat8(tr_read(wb), tr_read(wb + 4 * 576));
            }
        }
        WAIT_L(0);
        BAR;
#define K3_STEP(XC, XN) { \
            const char* base = smem + ((i + 1) % K3_NB) * K3_STAGE; \
            _Pragma("unroll") for (int m = 0; m < 4; m++) XN[m] = *(const s8v*)(base + xoff + m * 1024); \
            SB0; \
            _Pragma("unroll") for (int n = 0; n < 9; n++) { \
                _Pragma("unroll") for (int m = 0; m < 4; m++) acc[m][n] = MFMA16(wf[n], XC[m], acc[m][n]); \
                SB0; \
                const char* wb = base + ((n & 1) ? wbo : wbe) + n * 32; \
                wf[n] = cat8(tr_read(wb), tr_read(wb + 4 * 576)); \
                SB0; \
            } \
            BAR; }
#pragma unroll 1
        for (int i = 0; i < 18; i += 2) {
            K3_STEP(xa, xb);
            i++; K3_STEP(xb, xa); i--;
        }
    }
    __syncthreads();
    f4v* ex = (f4v*)smem + (wave & 3) * (18 * 64) + lane;
    if (wave < 4) {
#pragma unroll
        for (int m = 0; m < 2; m++)
#pragma unroll
            for (int n = 0; n < 9; n++) ex[(m * 9 + n) * 64] = acc[m + 2][n];
    }
    __syncthreads();
    if (wave >= 4) {
#pragma unroll
        for (int m = 0; m < 2; m++)
#pragma unroll
            for (int n = 0; n < 9; n++) acc[m][n] = ex[(m * 9 + n) * 64];
    }
    {
        const int mb = (wave >> 2) * 2;
#pragma unroll
        for (int m = 0; m < 2; m++) {
            int rl = wm * 64 + (mb + m) * 16 + xr;
            f2v ms = mu[rl];
            char* ob = h1f + (size_t)((r0 + wm * 64 + (mb + m) * 16) >> 4) * (144 * 1024) + lane * 16;
#pragma unroll
            for (int n = 0; n < 9; n++) {
                f4v a = acc[m][n];
                int cl = wn * 144 + n * 16 + 4 * g;
                f4v cs = *(const f4v*)(csl + cl), bb = *(const f4v*)(bbl + cl);
                f2v t01 = f2v{bb[0], bb[1]} - f2v{cs[0], cs[1]} * ms[0];
                f2v t23 = f2v{bb[2], bb[3]} - f2v{cs[2], cs[3]} * ms[0];
                f2v v01 = gelu_tanh2(f2v{a[0], a[1]} * ms[1] + t01);
                f2v v23 = gelu_tanh2(f2v{a[2], a[3]} * ms[1] + t23);
                int cg = c0 + wn * 144 + n * 16;
                *(u2v*)(ob + (cg >> 5) * 1024 + ((cg >> 4) & 1) * 8) = u2v{pack_bf2(v01[0], v01[1]), pack_bf2(v23[0], v23[1])};
            }
        }
    }
}

constexpr int K4_STAGE = 16384 + 8 * 2432;
constexpr int K4_NB = 4;
constexpr int K4_LDS = 147456;

__global__ __launch_bounds__(512, 2) void k4_down(const char* __restrict__ h1f, const bf16_t* __restrict__ w2,
                                                  float* __restrict__ part) {
    extern __shared__ __attribute__((aligned(16))) char smem[];
    const int tid = threadIdx.x, lane = tid & 63;
    const int wave = __builtin_amdgcn_readfirstlane(tid >> 6);
    const int xcd = blockIdx.x & 7, idx = blockIdx.x >> 3;
    const int ks = xcd >> 1;
    const int rt = (xcd & 1) * 8 + (idx & 7), ct = idx >> 3;
    const int r0 = rt * 128, c0 = ct * 144, k0 = ks * 1152;
    const unsigned lbase = lds_addr(smem);
    const int g = lane >> 4, q = (lane & 15) >> 2, p = lane & 3;
    const int wk = (wave >> 1) & 1, wm = wave & 1;
    f4v acc[4][9];
    if (wave >= 4) {
        const int lw = wave - 4;
        unsigned bsrc[5];
#pragma unroll
        for (int i = 0; i < 5; i++) {
            int bi = lw * 5 + i; if (bi > 18) bi = 18;
            int off = bi * 1024 + lane * 16;
            int blk = off / 2432, r = off - blk * 2432;
            int kk = r / 288, cr = r - kk * 288;
            if (kk > 7) { kk = 7; cr = 0; }
            bsrc[i] = (unsigned)(((k0 + blk * 8 + kk) * D_ + c0) * 2 + cr);
        }
        const char* xg = h1f + ((size_t)(rt * 8 + lw * 2) * 144 + (k0 >> 5)) * 1024;
        const unsigned xvo = lane * 16;
        auto issue = [&](int st) {
            int sc = st > 17 ? 17 : st;
            unsigned lb = lbase + (st % K4_NB) * K4_STAGE;
            const char* gx = xg + sc * 2048;
            glds16s(gx, xvo, lb + (lw * 4) * 1024);
            glds16s(gx + 1024, xvo, lb + (lw * 4 + 1) * 1024);
            glds16s(gx + 144 * 1024, xvo, lb + (lw * 4 + 2) * 1024);
            glds16s(gx + 145 * 1024, xvo, lb + (lw * 4 + 3) * 1024);
            const char* gb = (const char*)w2 + (size_t)sc * 64 * D_ * 2;
#pragma unroll
            for (int i = 0; i < 5; i++) glds16s(gb, bsrc[i], lb + 16384 + (lw * 5 + i > 18 ? 18 : lw * 5 + i) * 1024);
        };
#pragma unroll
        for (int st = 0; st < K4_NB - 1; st++) issue(st);
        WAIT_V(18);
        BAR;
        for (int i = -1; i < 18; i++) {
            if (i >= 0) issue(i + K4_NB - 1);
            WAIT_V(9);
            BAR;
        }
        WAIT_V(0);
    } else {
#pragma unroll
        for (int m = 0; m < 4; m++)
#pragma unroll
            for (int n = 0; n < 9; n++) acc[m][n] = f4v{0, 0, 0, 0};
        const int kA = wk * 32 + 4 * g + q, kB = kA + 16;
        const int wbA = 16384 + (kA >> 3) * 2432 + (kA & 7) * 288 + 8 * p;
        const int wbB = 16384 + (kB >> 3) * 2432 + (kB & 7) * 288 + 8 * p;
        const int xoff = (wm * 8 + wk) * 1024 + lane * 16;
        s8v xa[4], xb[4], wf[9];
        BAR;
        {
            const char* base = smem;
#pragma unroll
            for (int m = 0; m < 4; m++) xa[m] = *(const s8v*)(base + xoff + m * 2048);
#pragma unroll
            for (int n = 0; n < 9; n++) wf[n] = cat8(tr_read(base + wbA + n * 32), tr_read(base + wbB + n * 32));
        }
        WAIT_L(0);
        BAR;
#define K4_STEP(XC, XN) { \
            const char* base = smem + ((i + 1) % K4_NB) * K4_STAGE; \
            _Pragma("unroll") for (int m = 0; m < 4; m++) XN[m] = *(const s8v*)(base + xoff + m * 2048); \
            SB0; \
            _Pragma("unroll") for (int n = 0; n < 9; n++) { \
                _Pragma("unroll") for (int m = 0; m < 4; m++) acc[m][n] = MFMA16(wf[n], XC[m], acc[m][n]); \
                SB0; \
                wf[n] = cat8(tr_read(base + wbA + n * 32), tr_read(base + wbB + n * 32)); \
                SB0; \
            } \
            BAR; }
#pragma unroll 1
        for (int i = 0; i < 18; i += 2) {
            K4_STEP(xa, xb);
            i++; K4_STEP(xb, xa); i--;
        }
    }
    __syncthreads();
    if (wave < 4) {
        f4v* ex = (f4v*)smem + wave * (36 * 64) + lane;
#pragma unroll
        for (int m = 0; m < 4; m++)
#pragma unroll
            for (int n = 0; n < 9; n++) ex[(m * 9 + n) * 64] = acc[m][n];
    }
    __syncthreads();
    {
        const int om = wave >> 1;
        const f4v* e0 = (const f4v*)smem + (wm) * (36 * 64) + (om * 9) * 64 + lane;
        const f4v* e1 = (const f4v*)smem + (2 + wm) * (36 * 64) + (om * 9) * 64 + lane;
        const int row = r0 + wm * 64 + om * 16 + (lane & 15);
        float* pp = part + (size_t)ks * S_ * D_ + (size_t)row * D_ + c0 + 4 * g;
#pragma unroll
        for (int n = 0; n < 9; n++) *(f4v*)(pp + n * 16) = e0[n * 64] + e1[n * 64];
    }
}

__global__ __launch_bounds__(256) void k5_reduce(const float* __restrict__ part, const bf16_t* __restrict__ h,
                                                 const bf16_t* __restrict__ b2, bf16_t* __restrict__ out) {
    int i = blockIdx.x * 256 + threadIdx.x;
    size_t e = (size_t)i * 4;
    int col = (int)(e % D_);
    f4v a = *(const f4v*)(part + e);
#pragma unroll
    for (int k = 1; k < 4; k++) a += *(const f4v*)(part + (size_t)k * S_ * D_ + e);
    u2v hh = *(const u2v*)(h + e);
    u2v bb = *(const u2v*)(b2 + col);
    float v0 = bf_lo(hh[0]) + (a[0] + bf_lo(bb[0]));
    float v1 = bf_hi(hh[0]) + (a[1] + bf_hi(bb[0]));
    float v2 = bf_lo(hh[1]) + (a[2] + bf_lo(bb[1]));
    float v3 = bf_hi(hh[1]) + (a[3] + bf_hi(bb[1]));
    *(u2v*)(out + e) = u2v{pack_bf2(v0, v1), pack_bf2(v2, v3)};
}

extern "C" void kernel_launch(void* const* d_in, const int* in_sizes, int n_in,
                              void* d_out, int out_size, void* d_ws, size_t ws_size,
                              hipStream_t stream) {
    const bf16_t* x = (const bf16_t*)d_in[0];
    const bf16_t* wqkv = (const bf16_t*)d_in[1];
    const bf16_t* wo = (const bf16_t*)d_in[2];
    const bf16_t* w1 = (const bf16_t*)d_in[4];
    const bf16_t* b1 = (const bf16_t*)d_in[5];
    const bf16_t* w2 = (const bf16_t*)d_in[6];
    const bf16_t* b2 = (const bf16_t*)d_in[7];
    bf16_t* out = (bf16_t*)d_out;
    char* ws = (char*)d_ws;
    bf16_t* o = (bf16_t*)(ws);
    bf16_t* h = (bf16_t*)(ws + (4 << 20));
    f2v* stats = (f2v*)(ws + (8 << 20));
    float* cs1 = (float*)(ws + (9 << 20));
    char* h1 = (char*)(ws + (16 << 20));
    float* part = (float*)(ws + (48 << 20));

    hipFuncSetAttribute((const void*)k1_qkv_attn, hipFuncAttributeMaxDynamicSharedMemorySize, K1_LDS);
    hipFuncSetAttribute((const void*)k2_wo, hipFuncAttributeMaxDynamicSharedMemorySize, K2_LDS);
    hipFuncSetAttribute((const void*)k3_up, hipFuncAttributeMaxDynamicSharedMemorySize, K3_LDS);
    hipFuncSetAttribute((const void*)k4_down, hipFuncAttributeMaxDynamicSharedMemorySize, K4_LDS);

    k1_qkv_attn<<<256, 512, K1_LDS, stream>>>(x, wqkv, w1, o, cs1);
    k2_wo<<<256, 512, K2_LDS, stream>>>(o, wo, x, h, stats);
    k3_up<<<256, 512, K3_LDS, stream>>>(h, w1, b1, cs1, stats, h1);
    k4_down<<<256, 512, K4_LDS, stream>>>(h1, w2, part);
    k5_reduce<<<S_ * D_ / 4 / 256, 256, 0, stream>>>(part, h, b2, out);
}
```

```cpp
#include <hip/hip_runtime.h>
#include <hip/hip_bf16.h>

typedef __attribute__((ext_vector_type(8))) short s8v;
typedef __attribute__((ext_vector_type(4))) short s4v;
typedef __attribute__((ext_vector_type(4))) short v4i16_t;
typedef __attribute__((ext_vector_type(4))) float f4v;
typedef __attribute__((ext_vector_type(2))) float f2v;
typedef __attribute__((ext_vector_type(4))) unsigned u4v;
typedef __attribute__((ext_vector_type(2))) unsigned u2v;
typedef unsigned short bf16_t;

constexpr int S_ = 2048, D_ = 576, NQKV = 1728, F_ = 4608;

__device__ __forceinline__ unsigned lds_addr(const void* l) {
    return (unsigned)(size_t)(__attribute__((address_space(3))) const char*)l;
}
__device__ __forceinline__ void glds16(const void* g, unsigned dst) {
    unsigned keep;
    asm volatile("s_mov_b32 %0, m0\n\ts_mov_b32 m0, %2\n\ts_nop 0\n\tglobal_load_lds_dwordx4 %1, off\n\ts_mov_b32 m0, %0"
                 : "=&s"(keep) : "v"(g), "s"(dst) : "memory");
}
__device__ __forceinline__ void glds16s(const void* sbase, unsigned voff, unsigned dst) {
    unsigned keep;
    asm volatile("s_mov_b32 %0, m0\n\ts_mov_b32 m0, %3\n\ts_nop 0\n\tglobal_load_lds_dwordx4 %1, %2\n\ts_mov_b32 m0, %0"
                 : "=&s"(keep) : "v"(voff), "s"(sbase), "s"(dst) : "memory");
}
__device__ __forceinline__ s4v tr_read(const char* p) {
    return __builtin_bit_cast(s4v, __builtin_amdgcn_ds_read_tr16_b64_v4i16((__attribute__((address_space(3))) v4i16_t*)p));
}
__device__ __forceinline__ s8v cat8(s4v a, s4v b) {
    return __builtin_shufflevector(a, b, 0, 1, 2, 3, 4, 5, 6, 7);
}
__device__ __forceinline__ float bf_lo(unsigned u) { return __builtin_bit_cast(float, u << 16); }
__device__ __forceinline__ float bf_hi(unsigned u) { return __builtin_bit_cast(float, u & 0xffff0000u); }
__device__ __forceinline__ float bf2f(bf16_t v) { return __builtin_bit_cast(float, (unsigned)v << 16); }
__device__ __forceinline__ unsigned pack_bf2(float a, float b) {
    unsigned r;
    asm("v_cvt_pk_bf16_f32 %0, %1, %2" : "=v"(r) : "v"(a), "v"(b));
    return r;
}
#define MFMA16(a, b, c) __builtin_amdgcn_mfma_f32_16x16x32_bf16(a, b, c, 0, 0, 0)
#define WAIT_V(n) asm volatile("s_waitcnt vmcnt(" #n ")" ::: "memory")
#define WAIT_L(n) asm volatile("s_waitcnt lgkmcnt(" #n ")" ::: "memory")

#define SB0 __builtin_amdgcn_sched_barrier(0)
#define BAR __builtin_amdgcn_s_barrier()
constexpr int K1_XN_BYTES = 9 * 80 * 128;
constexpr int K1_STAGE = 14336;
constexpr int K1_NB = 4;
constexpr int K1_DUMMY = K1_XN_BYTES + K1_NB * K1_STAGE;
constexpr int K1_RED = K1_DUMMY + 4096;
constexpr int K1_LDS = K1_RED + 2048;

__global__ __launch_bounds__(512, 2) void k1_qkv_attn(const bf16_t* __restrict__ x, const bf16_t* __restrict__ wqkv,
                                                      const bf16_t* __restrict__ w1, const bf16_t* __restrict__ wo,
                                                      bf16_t* __restrict__ o,
                                                      float* __restrict__ cs1) {
    extern __shared__ __attribute__((aligned(16))) char smem[];
    const int tid = threadIdx.x, lane = tid & 63;
    const int wave = __builtin_amdgcn_readfirstlane(tid >> 6);
    const int xcd = blockIdx.x & 7, idx = blockIdx.x >> 3;
    const int hg = (xcd & 1) * 4 + (idx >> 3), rt = (xcd >> 1) * 8 + (idx & 7);
    const int s0 = rt * 64;
    const unsigned lring = lds_addr(smem) + K1_XN_BYTES;
    const int g = lane >> 4, q = (lane & 15) >> 2, p = lane & 3;

    const int lw = wave - 4;
    unsigned bsrc[4];
    auto issue = [&](int st) {
        int sc = st > 17 ? 17 : st;
        unsigned lb = lring + (st % K1_NB) * K1_STAGE;
        const char* gb = (const char*)wqkv + (size_t)sc * 32 * NQKV * 2;
#pragma unroll
        for (int i = 0; i < 4; i++) glds16s(gb, bsrc[i], lb + (lw * 4 + i > 13 ? 13 : lw * 4 + i) * 1024);
    };
    u4v cw[6];
    const int cu = blockIdx.x < 216 ? blockIdx.x : blockIdx.x - 216;
    const int cchunk = cu % 72, rthird = cu / 72;
    const unsigned dz = lds_addr(smem) + K1_DUMMY + (wave & 3) * 1024;
    if (wave >= 4) {
#pragma unroll
        for (int i = 0; i < 4; i++) {
            int bi = lw * 4 + i; if (bi > 13) bi = 13;
            int off = bi * 1024 + lane * 16;
            int k = off / 448, r = off - k * 448;
            int nt = (r >> 5) ^ ((k >> 3) & 1);
            int pc = nt * 2 + ((r >> 4) & 1); if (pc > 26) pc = 26;
            int sec = pc / 9, wi = pc - sec * 9;
            bsrc[i] = (unsigned)((k * NQKV + sec * 576 + hg * 72 + wi * 8) * 2);
        }
#pragma unroll
        for (int st = 0; st < K1_NB - 1; st++) issue(st);
#pragma unroll
        for (int j = 0; j < 8; j++) {
            int c = (lw * 8 + j) * 64 + lane; if (c > 1943) c = 1943;
            int seg = c / 9, pc = c - seg * 9;
            int sec = seg / 72, rr = seg - sec * 72;
            glds16s(wqkv, (unsigned)((((idx & 7) * 72 + rr) * NQKV + sec * 576 + hg * 72 + pc * 8) * 2), dz);
        }
    } else {
        u4v xv[3][9];
#pragma unroll
        for (int j = 0; j < 3; j++) {
            if (j < 2 || wave < 2) {
                int r = (4 * j + wave) * 8 + (lane >> 3);
                int s = s0 - 16 + r; if (s < 0) s = 0;
                const u4v* pp = (const u4v*)(x + (size_t)s * D_) + (lane & 7);
#pragma unroll
                for (int i = 0; i < 9; i++) xv[j][i] = pp[i * 8];
            }
            if (j == 0) {
            }
        }
#pragma unroll
        for (int j = 0; j < 3; j++) {
            if (j < 2 || wave < 2) {
                const int r = (4 * j + wave) * 8 + (lane >> 3);
                f2v v[36];
                f2v sm = f2v{0.f, 0.f};
#pragma unroll
                for (int i = 0; i < 9; i++)
#pragma unroll
                    for (int t = 0; t < 4; t++) { v[i * 4 + t] = f2v{bf_lo(xv[j][i][t]), bf_hi(xv[j][i][t])}; sm += v[i * 4 + t]; }
                float sum = sm[0] + sm[1];
                sum += __shfl_xor(sum, 1); sum += __shfl_xor(sum, 2); sum += __shfl_xor(sum, 4);
                const float mean = sum * (1.f / 576.f);
                f2v sq = f2v{0.f, 0.f};
#pragma unroll
                for (int i = 0; i < 36; i++) { v[i] = v[i] - mean; sq += v[i] * v[i]; }
                float sqs = sq[0] + sq[1];
                sqs += __shfl_xor(sqs, 1); sqs += __shfl_xor(sqs, 2); sqs += __shfl_xor(sqs, 4);
                const float rstd = rsqrtf(sqs * (1.f / 576.f) + 1e-5f);
#pragma unroll
                for (int i = 0; i < 9; i++) {
                    u4v ov;
#pragma unroll
                    for (int t = 0; t < 4; t++) { f2v w = v[i * 4 + t] * rstd; ov[t] = pack_bf2(w[0], w[1]); }
                    *(u4v*)(smem + i * (80 * 128) + r * 128 + (((lane & 7) ^ (r & 7)) << 4)) = ov;
                }
                if (j == 0) {
                }
                if (j == 1) {
                }
            }
        }
    }

    f4v acc[5][4];
    if (wave >= 4) {
        WAIT_V(16);
        __syncthreads();
        WAIT_V(12); BAR;
        issue(3); WAIT_V(12); BAR;
        for (int i = 1; i < 18; i++) {
            issue(i + K1_NB - 1);
            WAIT_V(4);
            BAR;
        }
        WAIT_V(0);
        {
            const u4v* pw = (const u4v*)(w1 + (size_t)(rthird * 192 + lw * 48 + (lane >> 3)) * F_ + cchunk * 64) + (lane & 7);
#pragma unroll
            for (int j = 0; j < 6; j++) cw[j] = pw[(size_t)j * 8 * F_ / 8];
            int c = blockIdx.x * 4 + lw; if (c > 647) c = 647;
            glds16s((const char*)wo + (size_t)c * 1024, (unsigned)(lane * 16), dz);
        }
    } else {
        const int nt0 = wave == 0 ? 0 : 1 + 3 * wave;
        const int nn = (wave == 0 || wave == 3) ? 4 : 3;
#pragma unroll
        for (int m = 0; m < 5; m++)
#pragma unroll
            for (int n = 0; n < 4; n++) acc[m][n] = f4v{0, 0, 0, 0};
        const int xr = lane & 15;
        int woff[4];
#pragma unroll
        for (int n = 0; n < 4; n++) woff[n] = K1_XN_BYTES + (8 * g + q) * 448 + (((nt0 + n) ^ (g & 1)) << 5) + 8 * p;
        s8v xa[5], xb[5], wf[4];
        auto rdx = [&](s8v* X, int st) {
            const char* xbp = smem + (st >> 1) * (80 * 128) + xr * 128 + ((((st & 1) * 4 + g) ^ (xr & 7)) << 4);
#pragma unroll
            for (int m = 0; m < 5; m++) X[m] = *(const s8v*)(xbp + m * 2048);
        };
        __syncthreads();
        rdx(xa, 0);
#pragma unroll
        for (int n = 0; n < 4; n++) wf[n] = cat8(tr_read(smem + woff[n]), tr_read(smem + woff[n] + 4 * 448));
        WAIT_L(0);
        BAR;
#define K1_STEP(XC, XN) { \
            const char* base = smem + ((i + 1) % K1_NB) * K1_STAGE; \
            rdx(XN, i + 1 > 17 ? 17 : i + 1); \
            SB0; \
            _Pragma("unroll") for (int n = 0; n < 4; n++) { \
                if (n < 3 || nn == 4) { \
                    if (wave != 0) acc[0][n] = MFMA16(XC[0], wf[n], acc[0][n]); \
                    _Pragma("unroll") for (int m = 1; m < 5; m++) acc[m][n] = MFMA16(XC[m], wf[n], acc[m][n]); \
                    SB0; \
                    wf[n] = cat8(tr_read(base + woff[n]), tr_read(base + woff[n] + 4 * 448)); \
                    SB0; \
                } \
            } \
            BAR; }
#pragma unroll 1
        for (int i = 0; i < 18; i += 2) {
            K1_STEP(xa, xb);
            i++; K1_STEP(xb, xa); i--;
        }
    }
    __syncthreads();
    float* qkv = (float*)smem;
    if (wave < 4) {
        const int nt0 = wave == 0 ? 0 : 1 + 3 * wave;
        const int nn = (wave == 0 || wave == 3) ? 4 : 3;
#pragma unroll
        for (int m = 0; m < 5; m++)
#pragma unroll
            for (int n = 0; n < 4; n++) {
                int col = (nt0 + n) * 16 + (lane & 15);
                if (n < nn && col < 216) *(f4v*)(qkv + col * 80 + m * 16 + 4 * g) = acc[m][n];
            }
    }
    __syncthreads();
    {
        const int head = wave >> 1;
        const int rl = (wave & 1) * 32 + (lane & 31);
        const int hw = lane >> 5;
        const int r = 16 + rl;
        const int s = s0 + rl;
        const float* qp = qkv + (head * 18) * 80 + r;
        const float* kp = qkv + (72 + head * 18) * 80 + r - 8 * hw;
        const float* vp = qkv + (144 + head * 18) * 80 + r - 8 * hw;
        float qv[18];
#pragma unroll
        for (int d = 0; d < 18; d++) qv[d] = qp[d * 80];
        float lg[4];
        float mx = -3.0e38f;
#pragma unroll
        for (int w = 0; w < 4; w++) {
            float a = 0.f;
#pragma unroll
            for (int d = 0; d < 18; d++) a += qv[d] * kp[d * 80 - 2 * w];
            a *= 0.23570226039551584f;
            lg[w] = (s - 2 * (w + 4 * hw) >= 0) ? a : -1e9f;
            mx = fmaxf(mx, lg[w]);
        }
        mx = fmaxf(mx, __shfl_xor(mx, 32));
        float den = 0.f;
#pragma unroll
        for (int w = 0; w < 4; w++) { lg[w] = __expf(lg[w] - mx); den += lg[w]; }
        den += __shfl_xor(den, 32);
        float inv = 1.f / den;
        float ov[18];
#pragma unroll
        for (int d = 0; d < 18; d++) ov[d] = 0.f;
#pragma unroll
        for (int w = 0; w < 4; w++) {
            float pw = lg[w] * inv;
#pragma unroll
            for (int d = 0; d < 18; d++) ov[d] += pw * vp[d * 80 - 2 * w];
        }
#pragma unroll
        for (int d = 0; d < 18; d++) ov[d] += __shfl_xor(ov[d], 32);
        unsigned* op = (unsigned*)(o + (size_t)s * D_ + hg * 72 + head * 18);
        if (hw == 0) {
#pragma unroll
            for (int d = 0; d < 5; d++) op[d] = pack_bf2(ov[2 * d], ov[2 * d + 1]);
        } else {
#pragma unroll
            for (int d = 5; d < 9; d++) op[d] = pack_bf2(ov[2 * d], ov[2 * d + 1]);
        }
    }
    if (wave >= 4) {
        float a[8];
#pragma unroll
        for (int j = 0; j < 8; j++) a[j] = 0.f;
#pragma unroll
        for (int i = 0; i < 6; i++)
#pragma unroll
            for (int j = 0; j < 4; j++) { a[2 * j] += bf_lo(cw[i][j]); a[2 * j + 1] += bf_hi(cw[i][j]); }
#pragma unroll
        for (int j = 0; j < 8; j++) {
            float t = a[j];
            t += __shfl_xor(t, 8); t += __shfl_xor(t, 16); t += __shfl_xor(t, 32);
            a[j] = t;
        }
        if (lane < 8) {
            float* rq = (float*)(smem + K1_RED) + lw * 64 + lane * 8;
            *(f4v*)rq = f4v{a[0], a[1], a[2], a[3]};
            *(f4v*)(rq + 4) = f4v{a[4], a[5], a[6], a[7]};
        }
        WAIT_V(0);
    }
    __syncthreads();
    if (wave == 7 && blockIdx.x < 216) {
        const float* rq = (const float*)(smem + K1_RED) + lane;
        cs1[rthird * F_ + cchunk * 64 + lane] = (rq[0] + rq[64]) + (rq[128] + rq[192]);
    }
}

constexpr int K2_CH = 8192 + 64 * 144;
constexpr int K2_LDS = 9 * K2_CH + 64;

__global__ __launch_bounds__(512, 2) void k2_wo(const bf16_t* __restrict__ o, const bf16_t* __restrict__ wo,
                                                const bf16_t* __restrict__ x, bf16_t* __restrict__ h,
                                                f2v* __restrict__ stats) {
    extern __shared__ __attribute__((aligned(16))) char smem[];
    const int tid = threadIdx.x, lane = tid & 63;
    const int wave = __builtin_amdgcn_readfirstlane(tid >> 6);
    const int rt = (blockIdx.x & 7) * 4 + ((blockIdx.x >> 3) & 3), ct = blockIdx.x >> 5;
    const int r0 = rt * 64, c0 = ct * 72;
    const int g = lane >> 4, q = (lane & 15) >> 2, p = lane & 3;
    if (wave >= 4) {
        const int lw = wave - 4;
        const unsigned lbase = lds_addr(smem);
        unsigned asrc[2], bsrc[3];
#pragma unroll
        for (int i = 0; i < 2; i++) {
            int a = lw * 2 + i;
            int row = a * 8 + (lane >> 3);
            int ch = (lane & 7) ^ (row & 7);
            asrc[i] = (unsigned)(((r0 + row) * D_ + ch * 8) * 2);
        }
#pragma unroll
        for (int i = 0; i < 3; i++) {
            int bi = lw + 4 * i; if (bi > 8) bi = 8;
            int ci = bi * 64 + lane;
            int k = ci / 9, c = ci - k * 9;
            bsrc[i] = (unsigned)((k * D_ + c0 + c * 8) * 2);
        }
#pragma unroll
        for (int c = 0; c < 9; c++) {
            unsigned lb = lbase + c * K2_CH;
            glds16s((const char*)o + c * 128, asrc[0], lb + (lw * 2) * 1024);
            glds16s((const char*)o + c * 128, asrc[1], lb + (lw * 2 + 1) * 1024);
#pragma unroll
            for (int i = 0; i < 3; i++) glds16s((const char*)wo + (size_t)c * 64 * D_ * 2, bsrc[i], lb + 8192 + (lw + 4 * i > 8 ? 8 : lw + 4 * i) * 1024);
        }
        WAIT_V(40); BAR;
        WAIT_V(35); BAR;
        WAIT_V(30); BAR;
        WAIT_V(25); BAR;
        WAIT_V(20); BAR;
        WAIT_V(15); BAR;
        WAIT_V(10); BAR;
        WAIT_V(5); BAR;
        WAIT_V(0); BAR;
        return;
    }
    const int xr = wave * 16 + (lane & 15);
    const int row = r0 + xr;
    u2v xx[5];
#pragma unroll
    for (int n = 0; n < 5; n++) {
        int col = n * 16 + 4 * g; if (col > 68) col = 68;
        xx[n] = *(const u2v*)(x + (size_t)row * D_ + c0 + col);
    }
    f4v acc[5];
#pragma unroll
    for (int n = 0; n < 5; n++) acc[n] = f4v{0, 0, 0, 0};
    const int boff = 8192 + (8 * g + q) * 144 + 8 * p;
#pragma unroll 1
    for (int c = 0; c < 9; c++) {
        BAR;
        const char* base = smem + c * K2_CH;
        s8v xf[2], wf[2][5];
#pragma unroll
        for (int ss = 0; ss < 2; ss++) {
            xf[ss] = *(const s8v*)(base + xr * 128 + (((ss * 4 + g) ^ (xr & 7)) << 4));
#pragma unroll
            for (int n = 0; n < 5; n++) {
                const char* wb = base + boff + ss * 32 * 144 + n * 32;
                wf[ss][n] = cat8(tr_read(wb), tr_read(wb + 4 * 144));
            }
        }
#pragma unroll
        for (int ss = 0; ss < 2; ss++)
#pragma unroll
            for (int n = 0; n < 5; n++) acc[n] = MFMA16(wf[ss][n], xf[ss], acc[n]);
    }
    float hv[5][4];
    float sum = 0.f;
#pragma unroll
    for (int n = 0; n < 5; n++) {
        int col = n * 16 + 4 * g;
        if (col < 72) {
            unsigned p0 = pack_bf2(bf_lo(xx[n][0]) + acc[n][0], bf_hi(xx[n][0]) + acc[n][1]);
            unsigned p1 = pack_bf2(bf_lo(xx[n][1]) + acc[n][2], bf_hi(xx[n][1]) + acc[n][3]);
            *(u2v*)(h + (size_t)row * D_ + c0 + col) = u2v{p0, p1};
            hv[n][0] = bf_lo(p0); hv[n][1] = bf_hi(p0); hv[n][2] = bf_lo(p1); hv[n][3] = bf_hi(p1);
            sum += hv[n][0] + hv[n][1] + hv[n][2] + hv[n][3];
        } else {
            hv[n][0] = hv[n][1] = hv[n][2] = hv[n][3] = 0.f;
        }
    }
    sum += __shfl_xor(sum, 16); sum += __shfl_xor(sum, 32);
    float mean = sum * (1.f / 72.f);
    float m2 = 0.f;
#pragma unroll
    for (int n = 0; n < 5; n++) {
        int col = n * 16 + 4 * g;
        if (col < 72) {
#pragma unroll
            for (int j = 0; j < 4; j++) { float d = hv[n][j] - mean; m2 += d * d; }
        }
    }
    m2 += __shfl_xor(m2, 16); m2 += __shfl_xor(m2, 32);
    if (g == 0) stats[(size_t)row * 8 + ct] = f2v{sum, m2};
}

constexpr int K3_STAGE = 8192 + 18432;
constexpr int K3_NB = 5;
constexpr int K3_PAR = K3_NB * K3_STAGE;
constexpr int K3_LDS = K3_PAR + 1024 + 2304;

__device__ __forceinline__ float gelu_tanh(float v) {
    float t = v * v;
    float z = v * (-2.302208198f * 0.044715f * t - 2.302208198f);
    float e = __builtin_amdgcn_exp2f(z);
    return v * __builtin_amdgcn_rcpf(1.f + e);
}

__device__ __forceinline__ f2v gelu_tanh2(f2v v) {
    f2v t = v * v;
    f2v z = v * (t * (-2.302208198f * 0.044715f) + (-2.302208198f));
    f2v d = f2v{__builtin_amdgcn_exp2f(z[0]), __builtin_amdgcn_exp2f(z[1])} + 1.f;
    return v * f2v{__builtin_amdgcn_rcpf(d[0]), __builtin_amdgcn_rcpf(d[1])};
}

__global__ __launch_bounds__(512, 2) void k3_up(const bf16_t* __restrict__ h, const bf16_t* __restrict__ w1,
                                                const bf16_t* __restrict__ b1, const float* __restrict__ cs1,
                                                const f2v* __restrict__ stats, const bf16_t* __restrict__ w2,
                                                char* __restrict__ h1f) {
    extern __shared__ __attribute__((aligned(16))) char smem[];
    const int tid = threadIdx.x, lane = tid & 63;
    const int wave = __builtin_amdgcn_readfirstlane(tid >> 6);
    const int xcd = blockIdx.x & 7, idx = blockIdx.x >> 3;
    const int rt = (xcd & 1) * 8 + (idx & 7), ct = (xcd >> 1) * 4 + (idx >> 3);
    const int r0 = rt * 128, c0 = ct * 288;
    const unsigned lbase = lds_addr(smem);
    const int g = lane >> 4, q = (lane & 15) >> 2, p = lane & 3;
    const int xr = lane & 15;
    f2v* mu = (f2v*)(smem + K3_PAR);
    float* csl = (float*)(smem + K3_PAR + 1024);
    float* bbl = csl + 288;
    f4v acc[4][9];
    const int wm = (wave >> 1) & 1, wn = wave & 1;
    if (wave >= 4) {
        const int lw = wave - 4;
        unsigned asrc[2], bsrc[5];
#pragma unroll
        for (int i = 0; i < 2; i++) {
            int a = lw * 2 + i;
            int row = a * 16 + (lane >> 2);
            int ch = (lane & 3) ^ ((row & 8) >> 2);
            asrc[i] = (unsigned)(((r0 + row) * D_ + ch * 8) * 2);
        }
#pragma unroll
        for (int i = 0; i < 5; i++) {
            int bi = lw * 5 + i; if (bi > 17) bi = 17;
            int off = bi * 1024 + lane * 16;
            int k = off / 576, r = off - k * 576;
            int nt = (r >> 5) ^ ((k >> 3) & 1);
            bsrc[i] = (unsigned)((k * F_ + c0 + nt * 16 + ((r >> 4) & 1) * 8) * 2);
        }
        auto issue = [&](int st) {
            int sc = st > 17 ? 17 : st;
            unsigned lb = lbase + (st % K3_NB) * K3_STAGE;
            const char* ga = (const char*)h + sc * 64;
            const char* gb = (const char*)w1 + (size_t)sc * 32 * F_ * 2;
            glds16s(ga, asrc[0], lb + (lw * 2) * 1024);
            glds16s(ga, asrc[1], lb + (lw * 2 + 1) * 1024);
#pragma unroll
            for (int i = 0; i < 5; i++) glds16s(gb, bsrc[i], lb + 8192 + (lw * 5 + i > 17 ? 17 : lw * 5 + i) * 1024);
        };
#pragma unroll
        for (int st = 0; st < K3_NB - 1; st++) issue(st);
        WAIT_V(21);
        BAR;
        for (int i = -1; i < 18; i++) {
            if (i >= 0) issue(i + K3_NB - 1);
            WAIT_V(14);
            BAR;
        }
        WAIT_V(0);
    } else {
        {
            float c0v = (cs1[c0 + tid] + cs1[F_ + c0 + tid]) + cs1[2 * F_ + c0 + tid], b0v = bf2f(b1[c0 + tid]);
            float c1v = 0.f, b1v = 0.f;
            if (tid < 32) { c1v = (cs1[c0 + 256 + tid] + cs1[F_ + c0 + 256 + tid]) + cs1[2 * F_ + c0 + 256 + tid]; b1v = bf2f(b1[c0 + 256 + tid]); }
            if (tid < 128) {
                const f4v* sp = (const f4v*)(stats + (size_t)(r0 + tid) * 8);
                f4v v[4];
#pragma unroll
                for (int i = 0; i < 4; i++) v[i] = sp[i];
                float tot = 0.f;
#pragma unroll
                for (int i = 0; i < 4; i++) tot += v[i][0] + v[i][2];
                float mean = tot * (1.f / 576.f);
                float M2 = 0.f;
#pragma unroll
                for (int i = 0; i < 4; i++) {
                    float d0 = v[i][0] * (1.f / 72.f) - mean, d1 = v[i][2] * (1.f / 72.f) - mean;
                    M2 += v[i][1] + v[i][3] + 72.f * (d0 * d0 + d1 * d1);
                }
                float rstd = rsqrtf(M2 * (1.f / 576.f) + 1e-5f);
                mu[tid] = f2v{mean * rstd, rstd};
            }
            csl[tid] = c0v; bbl[tid] = b0v;
            if (tid < 32) { csl[256 + tid] = c1v; bbl[256 + tid] = b1v; }
        }
#pragma unroll
        for (int m = 0; m < 4; m++)
#pragma unroll
            for (int n = 0; n < 9; n++) acc[m][n] = f4v{0, 0, 0, 0};
        const int dlt = (g & 1) ? 32 : 0;
        const int wb0 = 8192 + (8 * g + q) * 576 + wn * 9 * 32 + 8 * p;
        const int wbe = wb0 + ((wn & 1) ? -dlt : dlt);
        const int wbo = wb0 + ((wn & 1) ? dlt : -dlt);
        const int xoff = (wm * 64 + xr) * 64 + ((g ^ ((xr & 8) >> 2)) << 4);
        s8v xa[4], xb[4], wf[9];
        BAR;
        {
            const char* base = smem;
#pragma unroll
            for (int m = 0; m < 4; m++) xa[m] = *(const s8v*)(base + xoff + m * 1024);
#pragma unroll
            for (int n = 0; n < 9; n++) {
                const char* wb = base + ((n & 1) ? wbo : wbe) + n * 32;
                wf[n] = cat8(tr_read(wb), tr_read(wb + 4 * 576));
            }
        }
        WAIT_L(0);
        BAR;
#define K3_STEP(XC, XN) { \
            const char* base = smem + ((i + 1) % K3_NB) * K3_STAGE; \
            _Pragma("unroll") for (int m = 0; m < 4; m++) XN[m] = *(const s8v*)(base + xoff + m * 1024); \
            SB0; \
            _Pragma("unroll") for (int n = 0; n < 9; n++) { \
                _Pragma("unroll") for (int m = 0; m < 4; m++) acc[m][n] = MFMA16(wf[n], XC[m], acc[m][n]); \
                SB0; \
                const char* wb = base + ((n & 1) ? wbo : wbe) + n * 32; \
                wf[n] = cat8(tr_read(wb), tr_read(wb + 4 * 576)); \
                SB0; \
            } \
            BAR; }
#pragma unroll 1
        for (int i = 0; i < 18; i += 2) {
            K3_STEP(xa, xb);
            i++; K3_STEP(xb, xa); i--;
        }
    }
    __syncthreads();
#pragma unroll
    for (int j = 0; j < 3; j++) {
        int c = blockIdx.x * 24 + wave * 3 + j; if (c > 5183) c = 5183;
        glds16s((const char*)w2 + (size_t)c * 1024, (unsigned)(lane * 16), lbase + 98304 + wave * 1024);
    }
    f4v* ex = (f4v*)smem + (wave & 3) * (18 * 64) + lane;
    if (wave < 4) {
#pragma unroll
        for (int m = 0; m < 2; m++)
#pragma unroll
            for (int n = 0; n < 9; n++) ex[(m * 9 + n) * 64] = acc[m + 2][n];
    }
    __syncthreads();
    if (wave >= 4) {
#pragma unroll
        for (int m = 0; m < 2; m++)
#pragma unroll
            for (int n = 0; n < 9; n++) acc[m][n] = ex[(m * 9 + n) * 64];
    }
    {
        const int mb = (wave >> 2) * 2;
#pragma unroll
        for (int m = 0; m < 2; m++) {
            int rl = wm * 64 + (mb + m) * 16 + xr;
            f2v ms = mu[rl];
            char* ob = h1f + (size_t)((r0 + wm * 64 + (mb + m) * 16) >> 4) * (144 * 1024) + lane * 16;
#pragma unroll
            for (int n = 0; n < 9; n++) {
                f4v a = acc[m][n];
                int cl = wn * 144 + n * 16 + 4 * g;
                f4v cs = *(const f4v*)(csl + cl), bb = *(const f4v*)(bbl + cl);
                f2v t01 = f2v{bb[0], bb[1]} - f2v{cs[0], cs[1]} * ms[0];
                f2v t23 = f2v{bb[2], bb[3]} - f2v{cs[2], cs[3]} * ms[0];
                f2v v01 = gelu_tanh2(f2v{a[0], a[1]} * ms[1] + t01);
                f2v v23 = gelu_tanh2(f2v{a[2], a[3]} * ms[1] + t23);
                int cg = c0 + wn * 144 + n * 16;
                *(u2v*)(ob + (cg >> 5) * 1024 + ((cg >> 4) & 1) * 8) = u2v{pack_bf2(v01[0], v01[1]), pack_bf2(v23[0], v23[1])};
            }
        }
    }
    WAIT_V(0);
}

constexpr int K4_STAGE = 16384 + 8 * 2432;
constexpr int K4_NB = 4;
constexpr int K4_LDS = 147456;

__global__ __launch_bounds__(512, 2) void k4_down(const char* __restrict__ h1f, const bf16_t* __restrict__ w2,
                                                  float* __restrict__ part) {
    extern __shared__ __attribute__((aligned(16))) char smem[];
    const int tid = threadIdx.x, lane = tid & 63;
    const int wave = __builtin_amdgcn_readfirstlane(tid >> 6);
    const int xcd = blockIdx.x & 7, idx = blockIdx.x >> 3;
    const int ks = xcd >> 1;
    const int rt = (xcd & 1) * 8 + (idx & 7), ct = idx >> 3;
    const int r0 = rt * 128, c0 = ct * 144, k0 = ks * 1152;
    const unsigned lbase = lds_addr(smem);
    const int g = lane >> 4, q = (lane & 15) >> 2, p = lane & 3;
    const int wk = (wave >> 1) & 1, wm = wave & 1;
    f4v acc[4][9];
    if (wave >= 4) {
        const int lw = wave - 4;
        unsigned bsrc[5];
#pragma unroll
        for (int i = 0; i < 5; i++) {
            int bi = lw * 5 + i; if (bi > 18) bi = 18;
            int off = bi * 1024 + lane * 16;
            int blk = off / 2432, r = off - blk * 2432;
            int kk = r / 288, cr = r - kk * 288;
            if (kk > 7) { kk = 7; cr = 0; }
            bsrc[i] = (unsigned)(((k0 + blk * 8 + kk) * D_ + c0) * 2 + cr);
        }
        const char* xg = h1f + ((size_t)(rt * 8 + lw * 2) * 144 + (k0 >> 5)) * 1024;
        const unsigned xvo = lane * 16;
        auto issue = [&](int st) {
            int sc = st > 17 ? 17 : st;
            unsigned lb = lbase + (st % K4_NB) * K4_STAGE;
            const char* gx = xg + sc * 2048;
            glds16s(gx, xvo, lb + (lw * 4) * 1024);
            glds16s(gx + 1024, xvo, lb + (lw * 4 + 1) * 1024);
            glds16s(gx + 144 * 1024, xvo, lb + (lw * 4 + 2) * 1024);
            glds16s(gx + 145 * 1024, xvo, lb + (lw * 4 + 3) * 1024);
            const char* gb = (const char*)w2 + (size_t)sc * 64 * D_ * 2;
#pragma unroll
            for (int i = 0; i < 5; i++) glds16s(gb, bsrc[i], lb + 16384 + (lw * 5 + i > 18 ? 18 : lw * 5 + i) * 1024);
        };
#pragma unroll
        for (int st = 0; st < K4_NB - 1; st++) issue(st);
        WAIT_V(18);
        BAR;
        for (int i = -1; i < 18; i++) {
            if (i >= 0) issue(i + K4_NB - 1);
            WAIT_V(9);
            BAR;
        }
        WAIT_V(0);
    } else {
#pragma unroll
        for (int m = 0; m < 4; m++)
#pragma unroll
            for (int n = 0; n < 9; n++) acc[m][n] = f4v{0, 0, 0, 0};
        const int kA = wk * 32 + 4 * g + q, kB = kA + 16;
        const int wbA = 16384 + (kA >> 3) * 2432 + (kA & 7) * 288 + 8 * p;
        const int wbB = 16384 + (kB >> 3) * 2432 + (kB & 7) * 288 + 8 * p;
        const int xoff = (wm * 8 + wk) * 1024 + lane * 16;
        s8v xa[4], xb[4], wf[9];
        BAR;
        {
            const char* base = smem;
#pragma unroll
            for (int m = 0; m < 4; m++) xa[m] = *(const s8v*)(base + xoff + m * 2048);
#pragma unroll
            for (int n = 0; n < 9; n++) wf[n] = cat8(tr_read(base + wbA + n * 32), tr_read(base + wbB + n * 32));
        }
        WAIT_L(0);
        BAR;
#define K4_STEP(XC, XN) { \
            const char* base = smem + ((i + 1) % K4_NB) * K4_STAGE; \
            _Pragma("unroll") for (int m = 0; m < 4; m++) XN[m] = *(const s8v*)(base + xoff + m * 2048); \
            SB0; \
            _Pragma("unroll") for (int n = 0; n < 9; n++) { \
                _Pragma("unroll") for (int m = 0; m < 4; m++) acc[m][n] = MFMA16(wf[n], XC[m], acc[m][n]); \
                SB0; \
                wf[n] = cat8(tr_read(base + wbA + n * 32), tr_read(base + wbB + n * 32)); \
                SB0; \
            } \
            BAR; }
#pragma unroll 1
        for (int i = 0; i < 18; i += 2) {
            K4_STEP(xa, xb);
            i++; K4_STEP(xb, xa); i--;
        }
    }
    __syncthreads();
    if (wave < 4) {
        f4v* ex = (f4v*)smem + wave * (36 * 64) + lane;
#pragma unroll
        for (int m = 0; m < 4; m++)
#pragma unroll
            for (int n = 0; n < 9; n++) ex[(m * 9 + n) * 64] = acc[m][n];
    }
    __syncthreads();
    {
        const int om = wave >> 1;
        const f4v* e0 = (const f4v*)smem + (wm) * (36 * 64) + (om * 9) * 64 + lane;
        const f4v* e1 = (const f4v*)smem + (2 + wm) * (36 * 64) + (om * 9) * 64 + lane;
        const int row = r0 + wm * 64 + om * 16 + (lane & 15);
        float* pp = part + (size_t)ks * S_ * D_ + (size_t)row * D_ + c0 + 4 * g;
#pragma unroll
        for (int n = 0; n < 9; n++) *(f4v*)(pp + n * 16) = e0[n * 64] + e1[n * 64];
    }
}

__global__ __launch_bounds__(256) void k5_reduce(const float* __restrict__ part, const bf16_t* __restrict__ h,
                                                 const bf16_t* __restrict__ b2, bf16_t* __restrict__ out) {
    int i = blockIdx.x * 256 + threadIdx.x;
    size_t e = (size_t)i * 4;
    int col = (int)(e % D_);
    f4v a = *(const f4v*)(part + e);
#pragma unroll
    for (int k = 1; k < 4; k++) a += *(const f4v*)(part + (size_t)k * S_ * D_ + e);
    u2v hh = *(const u2v*)(h + e);
    u2v bb = *(const u2v*)(b2 + col);
    float v0 = bf_lo(hh[0]) + (a[0] + bf_lo(bb[0]));
    float v1 = bf_hi(hh[0]) + (a[1] + bf_hi(bb[0]));
    float v2 = bf_lo(hh[1]) + (a[2] + bf_lo(bb[1]));
    float v3 = bf_hi(hh[1]) + (a[3] + bf_hi(bb[1]));
    *(u2v*)(out + e) = u2v{pack_bf2(v0, v1), pack_bf2(v2, v3)};
}

extern "C" void kernel_launch(void* const* d_in, const int* in_sizes, int n_in,
                              void* d_out, int out_size, void* d_ws, size_t ws_size,
                              hipStream_t stream) {
    const bf16_t* x = (const bf16_t*)d_in[0];
    const bf16_t* wqkv = (const bf16_t*)d_in[1];
    const bf16_t* wo = (const bf16_t*)d_in[2];
    const bf16_t* w1 = (const bf16_t*)d_in[4];
    const bf16_t* b1 = (const bf16_t*)d_in[5];
    const bf16_t* w2 = (const bf16_t*)d_in[6];
    const bf16_t* b2 = (const bf16_t*)d_in[7];
    bf16_t* out = (bf16_t*)d_out;
    char* ws = (char*)d_ws;
    bf16_t* o = (bf16_t*)(ws);
    bf16_t* h = (bf16_t*)(ws + (4 << 20));
    f2v* stats = (f2v*)(ws + (8 << 20));
    float* cs1 = (float*)(ws + (9 << 20));
    char* h1 = (char*)(ws + (16 << 20));
    float* part = (float*)(ws + (48 << 20));

    hipFuncSetAttribute((const void*)k1_qkv_attn, hipFuncAttributeMaxDynamicSharedMemorySize, K1_LDS);
    hipFuncSetAttribute((const void*)k2_wo, hipFuncAttributeMaxDynamicSharedMemorySize, K2_LDS);
    hipFuncSetAttribute((const void*)k3_up, hipFuncAttributeMaxDynamicSharedMemorySize, K3_LDS);
    hipFuncSetAttribute((const void*)k4_down, hipFuncAttributeMaxDynamicSharedMemorySize, K4_LDS);

    k1_qkv_attn<<<256, 512, K1_LDS, stream>>>(x, wqkv, w1, wo, o, cs1);
    k2_wo<<<256, 512, K2_LDS, stream>>>(o, wo, x, h, stats);
    k3_up<<<256, 512, K3_LDS, stream>>>(h, w1, b1, cs1, stats, w2, h1);
    k4_down<<<256, 512, K4_LDS, stream>>>(h1, w2, part);
    k5_reduce<<<S_ * D_ / 4 / 256, 256, 0, stream>>>(part, h, b2, out);
}
```

```cpp
#include <hip/hip_runtime.h>
#include <hip/hip_bf16.h>

typedef __attribute__((ext_vector_type(8))) short s8v;
typedef __attribute__((ext_vector_type(4))) short s4v;
typedef __attribute__((ext_vector_type(4))) short v4i16_t;
typedef __attribute__((ext_vector_type(4))) float f4v;
typedef __attribute__((ext_vector_type(2))) float f2v;
typedef __attribute__((ext_vector_type(4))) unsigned u4v;
typedef __attribute__((ext_vector_type(2))) unsigned u2v;
typedef unsigned short bf16_t;

constexpr int S_ = 2048, D_ = 576, NQKV = 1728, F_ = 4608;

__device__ __forceinline__ unsigned lds_addr(const void* l) {
    return (unsigned)(size_t)(__attribute__((address_space(3))) const char*)l;
}
__device__ __forceinline__ void glds16(const void* g, unsigned dst) {
    unsigned keep;
    asm volatile("s_mov_b32 %0, m0\n\ts_mov_b32 m0, %2\n\ts_nop 0\n\tglobal_load_lds_dwordx4 %1, off\n\ts_mov_b32 m0, %0"
                 : "=&s"(keep) : "v"(g), "s"(dst) : "memory");
}
__device__ __forceinline__ void glds16s(const void* sbase, unsigned voff, unsigned dst) {
    unsigned keep;
    asm volatile("s_mov_b32 %0, m0\n\ts_mov_b32 m0, %3\n\ts_nop 0\n\tglobal_load_lds_dwordx4 %1, %2\n\ts_mov_b32 m0, %0"
                 : "=&s"(keep) : "v"(voff), "s"(sbase), "s"(dst) : "memory");
}
__device__ __forceinline__ s4v tr_read(const char* p) {
    return __builtin_bit_cast(s4v, __builtin_amdgcn_ds_read_tr16_b64_v4i16((__attribute__((address_space(3))) v4i16_t*)p));
}
__device__ __forceinline__ s8v cat8(s4v a, s4v b) {
    return __builtin_shufflevector(a, b, 0, 1, 2, 3, 4, 5, 6, 7);
}
__device__ __forceinline__ float bf_lo(unsigned u) { return __builtin_bit_cast(float, u << 16); }
__device__ __forceinline__ float bf_hi(unsigned u) { return __builtin_bit_cast(float, u & 0xffff0000u); }
__device__ __forceinline__ float bf2f(bf16_t v) { return __builtin_bit_cast(float, (unsigned)v << 16); }
__device__ __forceinline__ unsigned pack_bf2(float a, float b) {
    unsigned r;
    asm("v_cvt_pk_bf16_f32 %0, %1, %2" : "=v"(r) : "v"(a), "v"(b));
    return r;
}
#define MFMA16(a, b, c) __builtin_amdgcn_mfma_f32_16x16x32_bf16(a, b, c, 0, 0, 0)
#define WAIT_V(n) asm volatile("s_waitcnt vmcnt(" #n ")" ::: "memory")
#define WAIT_L(n) asm volatile("s_waitcnt lgkmcnt(" #n ")" ::: "memory")

#define SB0 __builtin_amdgcn_sched_barrier(0)
#define BAR __builtin_amdgcn_s_barrier()
constexpr int K1_XN_BYTES = 9 * 80 * 128;
constexpr int K1_STAGE = 14336;
constexpr int K1_NB = 4;
constexpr int K1_DUMMY = K1_XN_BYTES + K1_NB * K1_STAGE;
constexpr int K1_RED = K1_DUMMY + 4096;
constexpr int K1_LDS = K1_RED + 2048;

__global__ __launch_bounds__(512, 2) void k1_qkv_attn(const bf16_t* __restrict__ x, const bf16_t* __restrict__ wqkv,
                                                      const bf16_t* __restrict__ w1, const bf16_t* __restrict__ wo,
                                                      bf16_t* __restrict__ o,
                                                      float* __restrict__ cs1) {
    extern __shared__ __attribute__((aligned(16))) char smem[];
    const int tid = threadIdx.x, lane = tid & 63;
    const int wave = __builtin_amdgcn_readfirstlane(tid >> 6);
    const int xcd = blockIdx.x & 7, idx = blockIdx.x >> 3;
    const int hg = (xcd & 1) * 4 + (idx >> 3), rt = (xcd >> 1) * 8 + (idx & 7);
    const int s0 = rt * 64;
    const unsigned lring = lds_addr(smem) + K1_XN_BYTES;
    const int g = lane >> 4, q = (lane & 15) >> 2, p = lane & 3;

    const int lw = wave - 4;
    unsigned bsrc[4];
    auto issue = [&](int st) {
        int sc = st > 17 ? 17 : st;
        unsigned lb = lring + (st % K1_NB) * K1_STAGE;
        const char* gb = (const char*)wqkv + (size_t)sc * 32 * NQKV * 2;
#pragma unroll
        for (int i = 0; i < 4; i++) glds16s(gb, bsrc[i], lb + (lw * 4 + i > 13 ? 13 : lw * 4 + i) * 1024);
    };
    u4v cw[6];
    const int cu = blockIdx.x < 216 ? blockIdx.x : blockIdx.x - 216;
    const int cchunk = cu % 72, rthird = cu / 72;
    const unsigned dz = lds_addr(smem) + K1_DUMMY + (wave & 3) * 1024;
    if (wave >= 4) {
#pragma unroll
        for (int i = 0; i < 4; i++) {
            int bi = lw * 4 + i; if (bi > 13) bi = 13;
            int off = bi * 1024 + lane * 16;
            int k = off / 448, r = off - k * 448;
            int nt = (r >> 5) ^ ((k >> 3) & 1);
            int pc = nt * 2 + ((r >> 4) & 1); if (pc > 26) pc = 26;
            int sec = pc / 9, wi = pc - sec * 9;
            bsrc[i] = (unsigned)((k * NQKV + sec * 576 + hg * 72 + wi * 8) * 2);
        }
#pragma unroll
        for (int st = 0; st < K1_NB - 1; st++) issue(st);
#pragma unroll
        for (int j = 0; j < 8; j++) {
            int c = (lw * 8 + j) * 64 + lane; if (c > 1943) c = 1943;
            int seg = c / 9, pc = c - seg * 9;
            int sec = seg / 72, rr = seg - sec * 72;
            glds16s(wqkv, (unsigned)((((idx & 7) * 72 + rr) * NQKV + sec * 576 + hg * 72 + pc * 8) * 2), dz);
        }
    } else {
        u4v xv[3][9];
#pragma unroll
        for (int j = 0; j < 3; j++) {
            if (j < 2 || wave < 2) {
                int r = (4 * j + wave) * 8 + (lane >> 3);
                int s = s0 - 16 + r; if (s < 0) s = 0;
                const u4v* pp = (const u4v*)(x + (size_t)s * D_) + (lane & 7);
#pragma unroll
                for (int i = 0; i < 9; i++) xv[j][i] = pp[i * 8];
            }
            if (j == 0) {
            }
        }
#pragma unroll
        for (int j = 0; j < 3; j++) {
            if (j < 2 || wave < 2) {
                const int r = (4 * j + wave) * 8 + (lane >> 3);
                f2v v[36];
                f2v sm = f2v{0.f, 0.f};
#pragma unroll
                for (int i = 0; i < 9; i++)
#pragma unroll
                    for (int t = 0; t < 4; t++) { v[i * 4 + t] = f2v{bf_lo(xv[j][i][t]), bf_hi(xv[j][i][t])}; sm += v[i * 4 + t]; }
                float sum = sm[0] + sm[1];
                sum += __shfl_xor(sum, 1); sum += __shfl_xor(sum, 2); sum += __shfl_xor(sum, 4);
                const float mean = sum * (1.f / 576.f);
                f2v sq = f2v{0.f, 0.f};
#pragma unroll
                for (int i = 0; i < 36; i++) { v[i] = v[i] - mean; sq += v[i] * v[i]; }
                float sqs = sq[0] + sq[1];
                sqs += __shfl_xor(sqs, 1); sqs += __shfl_xor(sqs, 2); sqs += __shfl_xor(sqs, 4);
                const float rstd = rsqrtf(sqs * (1.f / 576.f) + 1e-5f);
#pragma unroll
                for (int i = 0; i < 9; i++) {
                    u4v ov;
#pragma unroll
                    for (int t = 0; t < 4; t++) { f2v w = v[i * 4 + t] * rstd; ov[t] = pack_bf2(w[0], w[1]); }
                    *(u4v*)(smem + i * (80 * 128) + r * 128 + (((lane & 7) ^ (r & 7)) << 4)) = ov;
                }
                if (j == 0) {
                }
                if (j == 1) {
                }
            }
        }
    }

    f4v acc[5][4];
    if (wave >= 4) {
        WAIT_V(16);
        __syncthreads();
        WAIT_V(12); BAR;
        issue(3); WAIT_V(12); BAR;
        for (int i = 1; i < 18; i++) {
            issue(i + K1_NB - 1);
            WAIT_V(4);
            BAR;
        }
        WAIT_V(0);
        {
            const u4v* pw = (const u4v*)(w1 + (size_t)(rthird * 192 + lw * 48 + (lane >> 3)) * F_ + cchunk * 64) + (lane & 7);
#pragma unroll
            for (int j = 0; j < 6; j++) cw[j] = pw[(size_t)j * 8 * F_ / 8];
            int c = blockIdx.x * 4 + lw; if (c > 647) c = 647;
            glds16s((const char*)wo + (size_t)c * 1024, (unsigned)(lane * 16), dz);
        }
    } else {
        const int nt0 = wave == 0 ? 0 : 1 + 3 * wave;
        const int nn = (wave == 0 || wave == 3) ? 4 : 3;
#pragma unroll
        for (int m = 0; m < 5; m++)
#pragma unroll
            for (int n = 0; n < 4; n++) acc[m][n] = f4v{0, 0, 0, 0};
        const int xr = lane & 15;
        int woff[4];
#pragma unroll
        for (int n = 0; n < 4; n++) woff[n] = K1_XN_BYTES + (8 * g + q) * 448 + (((nt0 + n) ^ (g & 1)) << 5) + 8 * p;
        s8v xa[5], xb[5], wf[4];
        auto rdx = [&](s8v* X, int st) {
            const char* xbp = smem + (st >> 1) * (80 * 128) + xr * 128 + ((((st & 1) * 4 + g) ^ (xr & 7)) << 4);
#pragma unroll
            for (int m = 0; m < 5; m++) X[m] = *(const s8v*)(xbp + m * 2048);
        };
        __syncthreads();
        rdx(xa, 0);
#pragma unroll
        for (int n = 0; n < 4; n++) wf[n] = cat8(tr_read(smem + woff[n]), tr_read(smem + woff[n] + 4 * 448));
        WAIT_L(0);
        BAR;
#define K1_STEP(XC, XN) { \
            const char* base = smem + ((i + 1) % K1_NB) * K1_STAGE; \
            rdx(XN, i + 1 > 17 ? 17 : i + 1); \
            SB0; \
            _Pragma("unroll") for (int n = 0; n < 4; n++) { \
                if (n < 3 || nn == 4) { \
                    if (wave != 0) acc[0][n] = MFMA16(XC[0], wf[n], acc[0][n]); \
                    _Pragma("unroll") for (int m = 1; m < 5; m++) acc[m][n] = MFMA16(XC[m], wf[n], acc[m][n]); \
                    SB0; \
                    wf[n] = cat8(tr_read(base + woff[n]), tr_read(base + woff[n] + 4 * 448)); \
                    SB0; \
                } \
            } \
            BAR; }
#pragma unroll 1
        for (int i = 0; i < 18; i += 2) {
            K1_STEP(xa, xb);
            i++; K1_STEP(xb, xa); i--;
        }
    }
    __syncthreads();
    float* qkv = (float*)smem;
    if (wave < 4) {
        const int nt0 = wave == 0 ? 0 : 1 + 3 * wave;
        const int nn = (wave == 0 || wave == 3) ? 4 : 3;
#pragma unroll
        for (int m = 0; m < 5; m++)
#pragma unroll
            for (int n = 0; n < 4; n++) {
                int col = (nt0 + n) * 16 + (lane & 15);
                if (n < nn && col < 216) *(f4v*)(qkv + col * 80 + m * 16 + 4 * g) = acc[m][n];
            }
    }
    __syncthreads();
    {
        const int head = wave >> 1;
        const int rl = (wave & 1) * 32 + (lane & 31);
        const int hw = lane >> 5;
        const int r = 16 + rl;
        const int s = s0 + rl;
        const float* qp = qkv + (head * 18) * 80 + r;
        const float* kp = qkv + (72 + head * 18) * 80 + r - 8 * hw;
        const float* vp = qkv + (144 + head * 18) * 80 + r - 8 * hw;
        float qv[18];
#pragma unroll
        for (int d = 0; d < 18; d++) qv[d] = qp[d * 80];
        float lg[4];
        float mx = -3.0e38f;
#pragma unroll
        for (int w = 0; w < 4; w++) {
            float a = 0.f;
#pragma unroll
            for (int d = 0; d < 18; d++) a += qv[d] * kp[d * 80 - 2 * w];
            a *= 0.23570226039551584f;
            lg[w] = (s - 2 * (w + 4 * hw) >= 0) ? a : -1e9f;
            mx = fmaxf(mx, lg[w]);
        }
        mx = fmaxf(mx, __shfl_xor(mx, 32));
        float den = 0.f;
#pragma unroll
        for (int w = 0; w < 4; w++) { lg[w] = __expf(lg[w] - mx); den += lg[w]; }
        den += __shfl_xor(den, 32);
        float inv = 1.f / den;
        float ov[18];
#pragma unroll
        for (int d = 0; d < 18; d++) ov[d] = 0.f;
#pragma unroll
        for (int w = 0; w < 4; w++) {
            float pw = lg[w] * inv;
#pragma unroll
            for (int d = 0; d < 18; d++) ov[d] += pw * vp[d * 80 - 2 * w];
        }
#pragma unroll
        for (int d = 0; d < 18; d++) ov[d] += __shfl_xor(ov[d], 32);
        unsigned* op = (unsigned*)(o + (size_t)s * D_ + hg * 72 + head * 18);
        if (hw == 0) {
#pragma unroll
            for (int d = 0; d < 5; d++) op[d] = pack_bf2(ov[2 * d], ov[2 * d + 1]);
        } else {
#pragma unroll
            for (int d = 5; d < 9; d++) op[d] = pack_bf2(ov[2 * d], ov[2 * d + 1]);
        }
    }
    if (wave >= 4) {
        float a[8];
#pragma unroll
        for (int j = 0; j < 8; j++) a[j] = 0.f;
#pragma unroll
        for (int i = 0; i < 6; i++)
#pragma unroll
            for (int j = 0; j < 4; j++) { a[2 * j] += bf_lo(cw[i][j]); a[2 * j + 1] += bf_hi(cw[i][j]); }
#pragma unroll
        for (int j = 0; j < 8; j++) {
            float t = a[j];
            t += __shfl_xor(t, 8); t += __shfl_xor(t, 16); t += __shfl_xor(t, 32);
            a[j] = t;
        }
        if (lane < 8) {
            float* rq = (float*)(smem + K1_RED) + lw * 64 + lane * 8;
            *(f4v*)rq = f4v{a[0], a[1], a[2], a[3]};
            *(f4v*)(rq + 4) = f4v{a[4], a[5], a[6], a[7]};
        }
        WAIT_V(0);
    }
    __syncthreads();
    if (wave == 7 && blockIdx.x < 216) {
        const float* rq = (const float*)(smem + K1_RED) + lane;
        cs1[rthird * F_ + cchunk * 64 + lane] = (rq[0] + rq[64]) + (rq[128] + rq[192]);
    }
}

constexpr int K2_CH = 8192 + 64 * 144;
constexpr int K2_LDS = 9 * K2_CH + 64;

__global__ __launch_bounds__(512, 2) void k2_wo(const bf16_t* __restrict__ o, const bf16_t* __restrict__ wo,
                                                const bf16_t* __restrict__ x, bf16_t* __restrict__ h,
                                                char* __restrict__ hT, f2v* __restrict__ stats) {
    extern __shared__ __attribute__((aligned(16))) char smem[];
    const int tid = threadIdx.x, lane = tid & 63;
    const int wave = __builtin_amdgcn_readfirstlane(tid >> 6);
    const int rt = (blockIdx.x & 7) * 4 + ((blockIdx.x >> 3) & 3), ct = blockIdx.x >> 5;
    const int r0 = rt * 64, c0 = ct * 72;
    const int g = lane >> 4, q = (lane & 15) >> 2, p = lane & 3;
    if (wave >= 4) {
        const int lw = wave - 4;
        const unsigned lbase = lds_addr(smem);
        unsigned asrc[2], bsrc[3];
#pragma unroll
        for (int i = 0; i < 2; i++) {
            int a = lw * 2 + i;
            int row = a * 8 + (lane >> 3);
            int ch = (lane & 7) ^ (row & 7);
            asrc[i] = (unsigned)(((r0 + row) * D_ + ch * 8) * 2);
        }
#pragma unroll
        for (int i = 0; i < 3; i++) {
            int bi = lw + 4 * i; if (bi > 8) bi = 8;
            int ci = bi * 64 + lane;
            int k = ci / 9, c = ci - k * 9;
            bsrc[i] = (unsigned)((k * D_ + c0 + c * 8) * 2);
        }
#pragma unroll
        for (int c = 0; c < 9; c++) {
            unsigned lb = lbase + c * K2_CH;
            glds16s((const char*)o + c * 128, asrc[0], lb + (lw * 2) * 1024);
            glds16s((const char*)o + c * 128, asrc[1], lb + (lw * 2 + 1) * 1024);
#pragma unroll
            for (int i = 0; i < 3; i++) glds16s((const char*)wo + (size_t)c * 64 * D_ * 2, bsrc[i], lb + 8192 + (lw + 4 * i > 8 ? 8 : lw + 4 * i) * 1024);
        }
        WAIT_V(40); BAR;
        WAIT_V(35); BAR;
        WAIT_V(30); BAR;
        WAIT_V(25); BAR;
        WAIT_V(20); BAR;
        WAIT_V(15); BAR;
        WAIT_V(10); BAR;
        WAIT_V(5); BAR;
        WAIT_V(0); BAR;
        return;
    }
    const int xr = wave * 16 + (lane & 15);
    const int row = r0 + xr;
    u2v xx[5];
#pragma unroll
    for (int n = 0; n < 5; n++) {
        int col = n * 16 + 4 * g; if (col > 68) col = 68;
        xx[n] = *(const u2v*)(x + (size_t)row * D_ + c0 + col);
    }
    f4v acc[5];
#pragma unroll
    for (int n = 0; n < 5; n++) acc[n] = f4v{0, 0, 0, 0};
    const int boff = 8192 + (8 * g + q) * 144 + 8 * p;
#pragma unroll 1
    for (int c = 0; c < 9; c++) {
        BAR;
        const char* base = smem + c * K2_CH;
        s8v xf[2], wf[2][5];
#pragma unroll
        for (int ss = 0; ss < 2; ss++) {
            xf[ss] = *(const s8v*)(base + xr * 128 + (((ss * 4 + g) ^ (xr & 7)) << 4));
#pragma unroll
            for (int n = 0; n < 5; n++) {
                const char* wb = base + boff + ss * 32 * 144 + n * 32;
                wf[ss][n] = cat8(tr_read(wb), tr_read(wb + 4 * 144));
            }
        }
#pragma unroll
        for (int ss = 0; ss < 2; ss++)
#pragma unroll
            for (int n = 0; n < 5; n++) acc[n] = MFMA16(wf[ss][n], xf[ss], acc[n]);
    }
    float hv[5][4];
    float sum = 0.f;
#pragma unroll
    for (int n = 0; n < 5; n++) {
        int col = n * 16 + 4 * g;
        if (col < 72) {
            unsigned p0 = pack_bf2(bf_lo(xx[n][0]) + acc[n][0], bf_hi(xx[n][0]) + acc[n][1]);
            unsigned p1 = pack_bf2(bf_lo(xx[n][1]) + acc[n][2], bf_hi(xx[n][1]) + acc[n][3]);
            *(u2v*)(h + (size_t)row * D_ + c0 + col) = u2v{p0, p1};
            {
                int cc = c0 + col;
                *(u2v*)(hT + (size_t)((row >> 4) * 18 + (cc >> 5)) * 1024 + (((cc & 31) >> 3) * 16 + (row & 15)) * 16 + (cc & 7) * 2) = u2v{p0, p1};
            }
            hv[n][0] = bf_lo(p0); hv[n][1] = bf_hi(p0); hv[n][2] = bf_lo(p1); hv[n][3] = bf_hi(p1);
            sum += hv[n][0] + hv[n][1] + hv[n][2] + hv[n][3];
        } else {
            hv[n][0] = hv[n][1] = hv[n][2] = hv[n][3] = 0.f;
        }
    }
    sum += __shfl_xor(sum, 16); sum += __shfl_xor(sum, 32);
    float mean = sum * (1.f / 72.f);
    float m2 = 0.f;
#pragma unroll
    for (int n = 0; n < 5; n++) {
        int col = n * 16 + 4 * g;
        if (col < 72) {
#pragma unroll
            for (int j = 0; j < 4; j++) { float d = hv[n][j] - mean; m2 += d * d; }
        }
    }
    m2 += __shfl_xor(m2, 16); m2 += __shfl_xor(m2, 32);
    if (g == 0) stats[(size_t)row * 8 + ct] = f2v{sum, m2};
}

constexpr int K3_STAGE = 8192 + 18432;
constexpr int K3_NB = 5;
constexpr int K3_PAR = K3_NB * K3_STAGE;
constexpr int K3_LDS = K3_PAR + 1024 + 2304;

__device__ __forceinline__ float gelu_tanh(float v) {
    float t = v * v;
    float z = v * (-2.302208198f * 0.044715f * t - 2.302208198f);
    float e = __builtin_amdgcn_exp2f(z);
    return v * __builtin_amdgcn_rcpf(1.f + e);
}

__device__ __forceinline__ f2v gelu_tanh2(f2v v) {
    f2v t = v * v;
    f2v z = v * (t * (-2.302208198f * 0.044715f) + (-2.302208198f));
    f2v d = f2v{__builtin_amdgcn_exp2f(z[0]), __builtin_amdgcn_exp2f(z[1])} + 1.f;
    return v * f2v{__builtin_amdgcn_rcpf(d[0]), __builtin_amdgcn_rcpf(d[1])};
}

__global__ __launch_bounds__(512, 2) void k3_up(const char* __restrict__ hT, const bf16_t* __restrict__ w1,
                                                const bf16_t* __restrict__ b1, const float* __restrict__ cs1,
                                                const f2v* __restrict__ stats, const bf16_t* __restrict__ w2,
                                                char* __restrict__ h1f) {
    extern __shared__ __attribute__((aligned(16))) char smem[];
    const int tid = threadIdx.x, lane = tid & 63;
    const int wave = __builtin_amdgcn_readfirstlane(tid >> 6);
    const int xcd = blockIdx.x & 7, idx = blockIdx.x >> 3;
    const int rt = (xcd & 1) * 8 + (idx & 7), ct = (xcd >> 1) * 4 + (idx >> 3);
    const int r0 = rt * 128, c0 = ct * 288;
    const unsigned lbase = lds_addr(smem);
    const int g = lane >> 4, q = (lane & 15) >> 2, p = lane & 3;
    const int xr = lane & 15;
    f2v* mu = (f2v*)(smem + K3_PAR);
    float* csl = (float*)(smem + K3_PAR + 1024);
    float* bbl = csl + 288;
    f4v acc[4][9];
    const int wm = (wave >> 1) & 1, wn = wave & 1;
    if (wave >= 4) {
        const int lw = wave - 4;
        unsigned bsrc[5];
        const unsigned avo = lane * 16;
        const char* ag = hT + (size_t)(rt * 8 + lw * 2) * 18 * 1024;
#pragma unroll
        for (int i = 0; i < 5; i++) {
            int bi = lw * 5 + i; if (bi > 17) bi = 17;
            int off = bi * 1024 + lane * 16;
            int k = off / 576, r = off - k * 576;
            int nt = (r >> 5) ^ ((k >> 3) & 1);
            bsrc[i] = (unsigned)((k * F_ + c0 + nt * 16 + ((r >> 4) & 1) * 8) * 2);
        }
        auto issue = [&](int st) {
            int sc = st > 17 ? 17 : st;
            unsigned lb = lbase + (st % K3_NB) * K3_STAGE;
            const char* ga = ag + sc * 1024;
            const char* gb = (const char*)w1 + (size_t)sc * 32 * F_ * 2;
            glds16s(ga, avo, lb + (lw * 2) * 1024);
            glds16s(ga + 18 * 1024, avo, lb + (lw * 2 + 1) * 1024);
#pragma unroll
            for (int i = 0; i < 5; i++) glds16s(gb, bsrc[i], lb + 8192 + (lw * 5 + i > 17 ? 17 : lw * 5 + i) * 1024);
        };
#pragma unroll
        for (int st = 0; st < K3_NB - 1; st++) issue(st);
        WAIT_V(21);
        BAR;
        for (int i = -1; i < 18; i++) {
            if (i >= 0) issue(i + K3_NB - 1);
            WAIT_V(14);
            BAR;
        }
        WAIT_V(0);
    } else {
        {
            float c0v = (cs1[c0 + tid] + cs1[F_ + c0 + tid]) + cs1[2 * F_ + c0 + tid], b0v = bf2f(b1[c0 + tid]);
            float c1v = 0.f, b1v = 0.f;
            if (tid < 32) { c1v = (cs1[c0 + 256 + tid] + cs1[F_ + c0 + 256 + tid]) + cs1[2 * F_ + c0 + 256 + tid]; b1v = bf2f(b1[c0 + 256 + tid]); }
            if (tid < 128) {
                const f4v* sp = (const f4v*)(stats + (size_t)(r0 + tid) * 8);
                f4v v[4];
#pragma unroll
                for (int i = 0; i < 4; i++) v[i] = sp[i];
                float tot = 0.f;
#pragma unroll
                for (int i = 0; i < 4; i++) tot += v[i][0] + v[i][2];
                float mean = tot * (1.f / 576.f);
                float M2 = 0.f;
#pragma unroll
                for (int i = 0; i < 4; i++) {
                    float d0 = v[i][0] * (1.f / 72.f) - mean, d1 = v[i][2] * (1.f / 72.f) - mean;
                    M2 += v[i][1] + v[i][3] + 72.f * (d0 * d0 + d1 * d1);
                }
                float rstd = rsqrtf(M2 * (1.f / 576.f) + 1e-5f);
                mu[tid] = f2v{mean * rstd, rstd};
            }
            csl[tid] = c0v; bbl[tid] = b0v;
            if (tid < 32) { csl[256 + tid] = c1v; bbl[256 + tid] = b1v; }
        }
#pragma unroll
        for (int m = 0; m < 4; m++)
#pragma unroll
            for (int n = 0; n < 9; n++) acc[m][n] = f4v{0, 0, 0, 0};
        const int dlt = (g & 1) ? 32 : 0;
        const int wb0 = 8192 + (8 * g + q) * 576 + wn * 9 * 32 + 8 * p;
        const int wbe = wb0 + ((wn & 1) ? -dlt : dlt);
        const int wbo = wb0 + ((wn & 1) ? dlt : -dlt);
        const int xoff = wm * 4096 + lane * 16;
        s8v xa[4], xb[4], wf[9];
        BAR;
        {
            const char* base = smem;
#pragma unroll
            for (int m = 0; m < 4; m++) xa[m] = *(const s8v*)(base + xoff + m * 1024);
#pragma unroll
            for (int n = 0; n < 9; n++) {
                const char* wb = base + ((n & 1) ? wbo : wbe) + n * 32;
                wf[n] = cat8(tr_read(wb), tr_read(wb + 4 * 576));
            }
        }
        WAIT_L(0);
        BAR;
#define K3_STEP(XC, XN) { \
            const char* base = smem + ((i + 1) % K3_NB) * K3_STAGE; \
            _Pragma("unroll") for (int m = 0; m < 4; m++) XN[m] = *(const s8v*)(base + xoff + m * 1024); \
            SB0; \
            _Pragma("unroll") for (int n = 0; n < 9; n++) { \
                _Pragma("unroll") for (int m = 0; m < 4; m++) acc[m][n] = MFMA16(wf[n], XC[m], acc[m][n]); \
                SB0; \
                const char* wb = base + ((n & 1) ? wbo : wbe) + n * 32; \
                wf[n] = cat8(tr_read(wb), tr_read(wb + 4 * 576)); \
                SB0; \
            } \
            BAR; }
#pragma unroll 1
        for (int i = 0; i < 18; i += 2) {
            K3_STEP(xa, xb);
            i++; K3_STEP(xb, xa); i--;
        }
    }
    __syncthreads();
#pragma unroll
    for (int j = 0; j < 3; j++) {
        int c = blockIdx.x * 24 + wave * 3 + j; if (c > 5183) c = 5183;
        glds16s((const char*)w2 + (size_t)c * 1024, (unsigned)(lane * 16), lbase + 98304 + wave * 1024);
    }
    f4v* ex = (f4v*)smem + (wave & 3) * (18 * 64) + lane;
    if (wave < 4) {
#pragma unroll
        for (int m = 0; m < 2; m++)
#pragma unroll
            for (int n = 0; n < 9; n++) ex[(m * 9 + n) * 64] = acc[m + 2][n];
    }
    __syncthreads();
    if (wave >= 4) {
#pragma unroll
        for (int m = 0; m < 2; m++)
#pragma unroll
            for (int n = 0; n < 9; n++) acc[m][n] = ex[(m * 9 + n) * 64];
    }
    {
        const int mb = (wave >> 2) * 2;
#pragma unroll
        for (int m = 0; m < 2; m++) {
            int rl = wm * 64 + (mb + m) * 16 + xr;
            f2v ms = mu[rl];
            char* ob = h1f + (size_t)((r0 + wm * 64 + (mb + m) * 16) >> 4) * (144 * 1024) + lane * 16;
            u2v pv[9];
#pragma unroll
            for (int n = 0; n < 9; n++) {
                f4v a = acc[m][n];
                int cl = wn * 144 + n * 16 + 4 * g;
                f4v cs = *(const f4v*)(csl + cl), bb = *(const f4v*)(bbl + cl);
                f2v t01 = f2v{bb[0], bb[1]} - f2v{cs[0], cs[1]} * ms[0];
                f2v t23 = f2v{bb[2], bb[3]} - f2v{cs[2], cs[3]} * ms[0];
                f2v v01 = gelu_tanh2(f2v{a[0], a[1]} * ms[1] + t01);
                f2v v23 = gelu_tanh2(f2v{a[2], a[3]} * ms[1] + t23);
                pv[n] = u2v{pack_bf2(v01[0], v01[1]), pack_bf2(v23[0], v23[1])};
                if (wn == 0) {
                    if (n & 1) *(u4v*)(ob + (((c0 >> 5) + (n >> 1)) * 1024)) = u4v{pv[n - 1][0], pv[n - 1][1], pv[n][0], pv[n][1]};
                    else if (n == 8) *(u2v*)(ob + (((c0 >> 5) + 4) * 1024)) = pv[n];
                } else {
                    if (n == 0) *(u2v*)(ob + (((c0 >> 5) + 4) * 1024) + 8) = pv[n];
                    else if (!(n & 1)) *(u4v*)(ob + (((c0 >> 5) + 4 + (n >> 1)) * 1024)) = u4v{pv[n - 1][0], pv[n - 1][1], pv[n][0], pv[n][1]};
                }
            }
        }
    }
    WAIT_V(0);
}

constexpr int K4_STAGE = 16384 + 8 * 2432;
constexpr int K4_NB = 4;
constexpr int K4_LDS = 147456;

__global__ __launch_bounds__(512, 2) void k4_down(const char* __restrict__ h1f, const bf16_t* __restrict__ w2,
                                                  float* __restrict__ part) {
    extern __shared__ __attribute__((aligned(16))) char smem[];
    const int tid = threadIdx.x, lane = tid & 63;
    const int wave = __builtin_amdgcn_readfirstlane(tid >> 6);
    const int xcd = blockIdx.x & 7, idx = blockIdx.x >> 3;
    const int ks = xcd >> 1;
    const int rt = (xcd & 1) * 8 + (idx & 7), ct = idx >> 3;
    const int r0 = rt * 128, c0 = ct * 144, k0 = ks * 1152;
    const unsigned lbase = lds_addr(smem);
    const int g = lane >> 4, q = (lane & 15) >> 2, p = lane & 3;
    const int wk = (wave >> 1) & 1, wm = wave & 1;
    f4v acc[4][9];
    if (wave >= 4) {
        const int lw = wave - 4;
        unsigned bsrc[5];
#pragma unroll
        for (int i = 0; i < 5; i++) {
            int bi = lw * 5 + i; if (bi > 18) bi = 18;
            int off = bi * 1024 + lane * 16;
            int blk = off / 2432, r = off - blk * 2432;
            int kk = r / 288, cr = r - kk * 288;
            if (kk > 7) { kk = 7; cr = 0; }
            bsrc[i] = (unsigned)(((k0 + blk * 8 + kk) * D_ + c0) * 2 + cr);
        }
        const char* xg = h1f + ((size_t)(rt * 8 + lw * 2) * 144 + (k0 >> 5)) * 1024;
        const unsigned xvo = lane * 16;
        auto issue = [&](int st) {
            int sc = st > 17 ? 17 : st;
            unsigned lb = lbase + (st % K4_NB) * K4_STAGE;
            const char* gx = xg + sc * 2048;
            glds16s(gx, xvo, lb + (lw * 4) * 1024);
            glds16s(gx + 1024, xvo, lb + (lw * 4 + 1) * 1024);
            glds16s(gx + 144 * 1024, xvo, lb + (lw * 4 + 2) * 1024);
            glds16s(gx + 145 * 1024, xvo, lb + (lw * 4 + 3) * 1024);
            const char* gb = (const char*)w2 + (size_t)sc * 64 * D_ * 2;
#pragma unroll
            for (int i = 0; i < 5; i++) glds16s(gb, bsrc[i], lb + 16384 + (lw * 5 + i > 18 ? 18 : lw * 5 + i) * 1024);
        };
#pragma unroll
        for (int st = 0; st < K4_NB - 1; st++) issue(st);
        WAIT_V(18);
        BAR;
        for (int i = -1; i < 18; i++) {
            if (i >= 0) issue(i + K4_NB - 1);
            WAIT_V(9);
            BAR;
        }
        WAIT_V(0);
    } else {
#pragma unroll
        for (int m = 0; m < 4; m++)
#pragma unroll
            for (int n = 0; n < 9; n++) acc[m][n] = f4v{0, 0, 0, 0};
        const int kA = wk * 32 + 4 * g + q, kB = kA + 16;
        const int wbA = 16384 + (kA >> 3) * 2432 + (kA & 7) * 288 + 8 * p;
        const int wbB = 16384 + (kB >> 3) * 2432 + (kB & 7) * 288 + 8 * p;
        const int xoff = (wm * 8 + wk) * 1024 + lane * 16;
        s8v xa[4], xb[4], wf[9];
        BAR;
        {
            const char* base = smem;
#pragma unroll
            for (int m = 0; m < 4; m++) xa[m] = *(const s8v*)(base + xoff + m * 2048);
#pragma unroll
            for (int n = 0; n < 9; n++) wf[n] = cat8(tr_read(base + wbA + n * 32), tr_read(base + wbB + n * 32));
        }
        WAIT_L(0);
        BAR;
#define K4_STEP(XC, XN) { \
            const char* base = smem + ((i + 1) % K4_NB) * K4_STAGE; \
            _Pragma("unroll") for (int m = 0; m < 4; m++) XN[m] = *(const s8v*)(base + xoff + m * 2048); \
            SB0; \
            _Pragma("unroll") for (int n = 0; n < 9; n++) { \
                _Pragma("unroll") for (int m = 0; m < 4; m++) acc[m][n] = MFMA16(wf[n], XC[m], acc[m][n]); \
                SB0; \
                wf[n] = cat8(tr_read(base + wbA + n * 32), tr_read(base + wbB + n * 32)); \
                SB0; \
            } \
            BAR; }
#pragma unroll 1
        for (int i = 0; i < 18; i += 2) {
            K4_STEP(xa, xb);
            i++; K4_STEP(xb, xa); i--;
        }
    }
    __syncthreads();
    if (wave < 4) {
        f4v* ex = (f4v*)smem + wave * (36 * 64) + lane;
#pragma unroll
        for (int m = 0; m < 4; m++)
#pragma unroll
            for (int n = 0; n < 9; n++) ex[(m * 9 + n) * 64] = acc[m][n];
    }
    __syncthreads();
    {
        const int om = wave >> 1;
        const f4v* e0 = (const f4v*)smem + (wm) * (36 * 64) + (om * 9) * 64 + lane;
        const f4v* e1 = (const f4v*)smem + (2 + wm) * (36 * 64) + (om * 9) * 64 + lane;
        const int row = r0 + wm * 64 + om * 16 + (lane & 15);
        float* pp = part + (size_t)ks * S_ * D_ + (size_t)row * D_ + c0 + 4 * g;
#pragma unroll
        for (int n = 0; n < 9; n++) *(f4v*)(pp + n * 16) = e0[n * 64] + e1[n * 64];
    }
}

__global__ __launch_bounds__(256) void k5_reduce(const float* __restrict__ part, const bf16_t* __restrict__ h,
                                                 const bf16_t* __restrict__ b2, bf16_t* __restrict__ out) {
    int i = blockIdx.x * 256 + threadIdx.x;
    size_t e = (size_t)i * 4;
    int col = (int)(e % D_);
    f4v a = *(const f4v*)(part + e);
#pragma unroll
    for (int k = 1; k < 4; k++) a += *(const f4v*)(part + (size_t)k * S_ * D_ + e);
    u2v hh = *(const u2v*)(h + e);
    u2v bb = *(const u2v*)(b2 + col);
    float v0 = bf_lo(hh[0]) + (a[0] + bf_lo(bb[0]));
    float v1 = bf_hi(hh[0]) + (a[1] + bf_hi(bb[0]));
    float v2 = bf_lo(hh[1]) + (a[2] + bf_lo(bb[1]));
    float v3 = bf_hi(hh[1]) + (a[3] + bf_hi(bb[1]));
    *(u2v*)(out + e) = u2v{pack_bf2(v0, v1), pack_bf2(v2, v3)};
}

extern "C" void kernel_launch(void* const* d_in, const int* in_sizes, int n_in,
                              void* d_out, int out_size, void* d_ws, size_t ws_size,
                              hipStream_t stream) {
    const bf16_t* x = (const bf16_t*)d_in[0];
    const bf16_t* wqkv = (const bf16_t*)d_in[1];
    const bf16_t* wo = (const bf16_t*)d_in[2];
    const bf16_t* w1 = (const bf16_t*)d_in[4];
    const bf16_t* b1 = (const bf16_t*)d_in[5];
    const bf16_t* w2 = (const bf16_t*)d_in[6];
    const bf16_t* b2 = (const bf16_t*)d_in[7];
    bf16_t* out = (bf16_t*)d_out;
    char* ws = (char*)d_ws;
    bf16_t* o = (bf16_t*)(ws);
    bf16_t* h = (bf16_t*)(ws + (4 << 20));
    char* hT = ws + (12 << 20);
    f2v* stats = (f2v*)(ws + (8 << 20));
    float* cs1 = (float*)(ws + (9 << 20));
    char* h1 = (char*)(ws + (16 << 20));
    float* part = (float*)(ws + (48 << 20));

    hipFuncSetAttribute((const void*)k1_qkv_attn, hipFuncAttributeMaxDynamicSharedMemorySize, K1_LDS);
    hipFuncSetAttribute((const void*)k2_wo, hipFuncAttributeMaxDynamicSharedMemorySize, K2_LDS);
    hipFuncSetAttribute((const void*)k3_up, hipFuncAttributeMaxDynamicSharedMemorySize, K3_LDS);
    hipFuncSetAttribute((const void*)k4_down, hipFuncAttributeMaxDynamicSharedMemorySize, K4_LDS);

    k1_qkv_attn<<<256, 512, K1_LDS, stream>>>(x, wqkv, w1, wo, o, cs1);
    k2_wo<<<256, 512, K2_LDS, stream>>>(o, wo, x, h, hT, stats);
    k3_up<<<256, 512, K3_LDS, stream>>>(hT, w1, b1, cs1, stats, w2, h1);
    k4_down<<<256, 512, K4_LDS, stream>>>(h1, w2, part);
    k5_reduce<<<S_ * D_ / 4 / 256, 256, 0, stream>>>(part, h, b2, out);
}
```

```cpp
#include <hip/hip_runtime.h>
#include <hip/hip_bf16.h>

typedef __attribute__((ext_vector_type(8))) short s8v;
typedef __attribute__((ext_vector_type(4))) short s4v;
typedef __attribute__((ext_vector_type(4))) short v4i16_t;
typedef __attribute__((ext_vector_type(4))) float f4v;
typedef __attribute__((ext_vector_type(2))) float f2v;
typedef __attribute__((ext_vector_type(4))) unsigned u4v;
typedef __attribute__((ext_vector_type(2))) unsigned u2v;
typedef unsigned short bf16_t;

constexpr int S_ = 2048, D_ = 576, NQKV = 1728, F_ = 4608;

__device__ __forceinline__ unsigned lds_addr(const void* l) {
    return (unsigned)(size_t)(__attribute__((address_space(3))) const char*)l;
}
__device__ __forceinline__ void glds16(const void* g, unsigned dst) {
    unsigned keep;
    asm volatile("s_mov_b32 %0, m0\n\ts_mov_b32 m0, %2\n\ts_nop 0\n\tglobal_load_lds_dwordx4 %1, off\n\ts_mov_b32 m0, %0"
                 : "=&s"(keep) : "v"(g), "s"(dst) : "memory");
}
__device__ __forceinline__ void glds16s(const void* sbase, unsigned voff, unsigned dst) {
    unsigned keep;
    asm volatile("s_mov_b32 %0, m0\n\ts_mov_b32 m0, %3\n\ts_nop 0\n\tglobal_load_lds_dwordx4 %1, %2\n\ts_mov_b32 m0, %0"
                 : "=&s"(keep) : "v"(voff), "s"(sbase), "s"(dst) : "memory");
}
__device__ __forceinline__ s4v tr_read(const char* p) {
    return __builtin_bit_cast(s4v, __builtin_amdgcn_ds_read_tr16_b64_v4i16((__attribute__((address_space(3))) v4i16_t*)p));
}
__device__ __forceinline__ s8v cat8(s4v a, s4v b) {
    return __builtin_shufflevector(a, b, 0, 1, 2, 3, 4, 5, 6, 7);
}
__device__ __forceinline__ float bf_lo(unsigned u) { return __builtin_bit_cast(float, u << 16); }
__device__ __forceinline__ float bf_hi(unsigned u) { return __builtin_bit_cast(float, u & 0xffff0000u); }
__device__ __forceinline__ float bf2f(bf16_t v) { return __builtin_bit_cast(float, (unsigned)v << 16); }
__device__ __forceinline__ unsigned pack_bf2(float a, float b) {
    unsigned r;
    asm("v_cvt_pk_bf16_f32 %0, %1, %2" : "=v"(r) : "v"(a), "v"(b));
    return r;
}
#define MFMA16(a, b, c) __builtin_amdgcn_mfma_f32_16x16x32_bf16(a, b, c, 0, 0, 0)
#define WAIT_V(n) asm volatile("s_waitcnt vmcnt(" #n ")" ::: "memory")
#define WAIT_L(n) asm volatile("s_waitcnt lgkmcnt(" #n ")" ::: "memory")

#define SB0 __builtin_amdgcn_sched_barrier(0)
#define BAR __builtin_amdgcn_s_barrier()
constexpr int K1_XN_BYTES = 9 * 80 * 128;
constexpr int K1_STAGE = 14336;
constexpr int K1_NB = 4;
constexpr int K1_DUMMY = K1_XN_BYTES + K1_NB * K1_STAGE;
constexpr int K1_RED = K1_DUMMY + 4096;
constexpr int K1_LDS = K1_RED + 2048;

__global__ __launch_bounds__(512, 2) void k1_qkv_attn(const bf16_t* __restrict__ x, const bf16_t* __restrict__ wqkv,
                                                      const bf16_t* __restrict__ w1, const bf16_t* __restrict__ wo,
                                                      bf16_t* __restrict__ o,
                                                      float* __restrict__ cs1) {
    extern __shared__ __attribute__((aligned(16))) char smem[];
    const int tid = threadIdx.x, lane = tid & 63;
    const int wave = __builtin_amdgcn_readfirstlane(tid >> 6);
    const int xcd = blockIdx.x & 7, idx = blockIdx.x >> 3;
    const int hg = (xcd & 1) * 4 + (idx >> 3), rt = (xcd >> 1) * 8 + (idx & 7);
    const int s0 = rt * 64;
    const unsigned lring = lds_addr(smem) + K1_XN_BYTES;
    const int g = lane >> 4, q = (lane & 15) >> 2, p = lane & 3;

    const int lw = wave - 4;
    unsigned bsrc[4];
    auto issue = [&](int st) {
        int sc = st > 17 ? 17 : st;
        unsigned lb = lring + (st % K1_NB) * K1_STAGE;
        const char* gb = (const char*)wqkv + (size_t)sc * 32 * NQKV * 2;
#pragma unroll
        for (int i = 0; i < 4; i++) glds16s(gb, bsrc[i], lb + (lw * 4 + i > 13 ? 13 : lw * 4 + i) * 1024);
    };
    u4v cw[6];
    const int cu = blockIdx.x < 216 ? blockIdx.x : blockIdx.x - 216;
    const int cchunk = cu % 72, rthird = cu / 72;
    const unsigned dz = lds_addr(smem) + K1_DUMMY + (wave & 3) * 1024;
    if (wave >= 4) {
#pragma unroll
        for (int i = 0; i < 4; i++) {
            int bi = lw * 4 + i; if (bi > 13) bi = 13;
            int off = bi * 1024 + lane * 16;
            int k = off / 448, r = off - k * 448;
            int nt = (r >> 5) ^ ((k >> 3) & 1);
            int pc = nt * 2 + ((r >> 4) & 1); if (pc > 26) pc = 26;
            int sec = pc / 9, wi = pc - sec * 9;
            bsrc[i] = (unsigned)((k * NQKV + sec * 576 + hg * 72 + wi * 8) * 2);
        }
#pragma unroll
        for (int st = 0; st < K1_NB - 1; st++) issue(st);
#pragma unroll
        for (int j = 0; j < 8; j++) {
            int c = (lw * 8 + j) * 64 + lane; if (c > 1943) c = 1943;
            int seg = c / 9, pc = c - seg * 9;
            int sec = seg / 72, rr = seg - sec * 72;
            glds16s(wqkv, (unsigned)((((idx & 7) * 72 + rr) * NQKV + sec * 576 + hg * 72 + pc * 8) * 2), dz);
        }
    } else {
        u4v xv[3][9];
#pragma unroll
        for (int j = 0; j < 3; j++) {
            if (j < 2 || wave < 2) {
                int r = (4 * j + wave) * 8 + (lane >> 3);
                int s = s0 - 16 + r; if (s < 0) s = 0;
                const u4v* pp = (const u4v*)(x + (size_t)s * D_) + (lane & 7);
#pragma unroll
                for (int i = 0; i < 9; i++) xv[j][i] = pp[i * 8];
            }
            if (j == 0) {
            }
        }
#pragma unroll
        for (int j = 0; j < 3; j++) {
            if (j < 2 || wave < 2) {
                const int r = (4 * j + wave) * 8 + (lane >> 3);
                f2v v[36];
                f2v sm = f2v{0.f, 0.f};
#pragma unroll
                for (int i = 0; i < 9; i++)
#pragma unroll
                    for (int t = 0; t < 4; t++) { v[i * 4 + t] = f2v{bf_lo(xv[j][i][t]), bf_hi(xv[j][i][t])}; sm += v[i * 4 + t]; }
                float sum = sm[0] + sm[1];
                sum += __shfl_xor(sum, 1); sum += __shfl_xor(sum, 2); sum += __shfl_xor(sum, 4);
                const float mean = sum * (1.f / 576.f);
                f2v sq = f2v{0.f, 0.f};
#pragma unroll
                for (int i = 0; i < 36; i++) { v[i] = v[i] - mean; sq += v[i] * v[i]; }
                float sqs = sq[0] + sq[1];
                sqs += __shfl_xor(sqs, 1); sqs += __shfl_xor(sqs, 2); sqs += __shfl_xor(sqs, 4);
                const float rstd = rsqrtf(sqs * (1.f / 576.f) + 1e-5f);
#pragma unroll
                for (int i = 0; i < 9; i++) {
                    u4v ov;
#pragma unroll
                    for (int t = 0; t < 4; t++) { f2v w = v[i * 4 + t] * rstd; ov[t] = pack_bf2(w[0], w[1]); }
                    *(u4v*)(smem + i * (80 * 128) + r * 128 + (((lane & 7) ^ (r & 7)) << 4)) = ov;
                }
                if (j == 0) {
                }
                if (j == 1) {
                }
            }
        }
    }

    f4v acc[5][4];
    if (wave >= 4) {
        WAIT_V(16);
        __syncthreads();
        WAIT_V(12); BAR;
        issue(3); WAIT_V(12); BAR;
        for (int i = 1; i < 18; i++) {
            issue(i + K1_NB - 1);
            WAIT_V(4);
            BAR;
        }
        WAIT_V(0);
        {
            const u4v* pw = (const u4v*)(w1 + (size_t)(rthird * 192 + lw * 48 + (lane >> 3)) * F_ + cchunk * 64) + (lane & 7);
#pragma unroll
            for (int j = 0; j < 6; j++) cw[j] = pw[(size_t)j * 8 * F_ / 8];
            int c = blockIdx.x * 4 + lw; if (c > 647) c = 647;
            glds16s((const char*)wo + (size_t)c * 1024, (unsigned)(lane * 16), dz);
        }
    } else {
        const int nt0 = wave == 0 ? 0 : 1 + 3 * wave;
        const int nn = (wave == 0 || wave == 3) ? 4 : 3;
#pragma unroll
        for (int m = 0; m < 5; m++)
#pragma unroll
            for (int n = 0; n < 4; n++) acc[m][n] = f4v{0, 0, 0, 0};
        const int xr = lane & 15;
        int woff[4];
#pragma unroll
        for (int n = 0; n < 4; n++) woff[n] = K1_XN_BYTES + (8 * g + q) * 448 + (((nt0 + n) ^ (g & 1)) << 5) + 8 * p;
        s8v xa[5], xb[5], wf[4];
        auto rdx = [&](s8v* X, int st) {
            const char* xbp = smem + (st >> 1) * (80 * 128) + xr * 128 + ((((st & 1) * 4 + g) ^ (xr & 7)) << 4);
#pragma unroll
            for (int m = 0; m < 5; m++) X[m] = *(const s8v*)(xbp + m * 2048);
        };
        __syncthreads();
        rdx(xa, 0);
#pragma unroll
        for (int n = 0; n < 4; n++) wf[n] = cat8(tr_read(smem + woff[n]), tr_read(smem + woff[n] + 4 * 448));
        WAIT_L(0);
        BAR;
#define K1_STEP(XC, XN) { \
            const char* base = smem + ((i + 1) % K1_NB) * K1_STAGE; \
            rdx(XN, i + 1 > 17 ? 17 : i + 1); \
            SB0; \
            _Pragma("unroll") for (int n = 0; n < 4; n++) { \
                if (n < 3 || nn == 4) { \
                    if (wave != 0) acc[0][n] = MFMA16(XC[0], wf[n], acc[0][n]); \
                    _Pragma("unroll") for (int m = 1; m < 5; m++) acc[m][n] = MFMA16(XC[m], wf[n], acc[m][n]); \
                    SB0; \
                    wf[n] = cat8(tr_read(base + woff[n]), tr_read(base + woff[n] + 4 * 448)); \
                    SB0; \
                } \
            } \
            BAR; }
#pragma unroll 1
        for (int i = 0; i < 18; i += 2) {
            K1_STEP(xa, xb);
            i++; K1_STEP(xb, xa); i--;
        }
    }
    __syncthreads();
    float* qkv = (float*)smem;
    if (wave < 4) {
        const int nt0 = wave == 0 ? 0 : 1 + 3 * wave;
        const int nn = (wave == 0 || wave == 3) ? 4 : 3;
#pragma unroll
        for (int m = 0; m < 5; m++)
#pragma unroll
            for (int n = 0; n < 4; n++) {
                int col = (nt0 + n) * 16 + (lane & 15);
                if (n < nn && col < 216) *(f4v*)(qkv + col * 80 + m * 16 + 4 * g) = acc[m][n];
            }
    }
    __syncthreads();
    {
        const int head = wave >> 1;
        const int rl = (wave & 1) * 32 + (lane & 31);
        const int hw = lane >> 5;
        const int r = 16 + rl;
        const int s = s0 + rl;
        const float* qp = qkv + (head * 18) * 80 + r;
        const float* kp = qkv + (72 + head * 18) * 80 + r - 8 * hw;
        const float* vp = qkv + (144 + head * 18) * 80 + r - 8 * hw;
        float qv[18];
#pragma unroll
        for (int d = 0; d < 18; d++) qv[d] = qp[d * 80];
        float lg[4];
        float mx = -3.0e38f;
#pragma unroll
        for (int w = 0; w < 4; w++) {
            float a = 0.f;
#pragma unroll
            for (int d = 0; d < 18; d++) a += qv[d] * kp[d * 80 - 2 * w];
            a *= 0.23570226039551584f;
            lg[w] = (s - 2 * (w + 4 * hw) >= 0) ? a : -1e9f;
            mx = fmaxf(mx, lg[w]);
        }
        mx = fmaxf(mx, __shfl_xor(mx, 32));
        float den = 0.f;
#pragma unroll
        for (int w = 0; w < 4; w++) { lg[w] = __expf(lg[w] - mx); den += lg[w]; }
        den += __shfl_xor(den, 32);
        float inv = 1.f / den;
        float ov[18];
#pragma unroll
        for (int d = 0; d < 18; d++) ov[d] = 0.f;
#pragma unroll
        for (int w = 0; w < 4; w++) {
            float pw = lg[w] * inv;
#pragma unroll
            for (int d = 0; d < 18; d++) ov[d] += pw * vp[d * 80 - 2 * w];
        }
#pragma unroll
        for (int d = 0; d < 18; d++) ov[d] += __shfl_xor(ov[d], 32);
        unsigned* op = (unsigned*)(o + (size_t)s * D_ + hg * 72 + head * 18);
        if (hw == 0) {
#pragma unroll
            for (int d = 0; d < 5; d++) op[d] = pack_bf2(ov[2 * d], ov[2 * d + 1]);
        } else {
#pragma unroll
            for (int d = 5; d < 9; d++) op[d] = pack_bf2(ov[2 * d], ov[2 * d + 1]);
        }
    }
    if (wave >= 4) {
        float a[8];
#pragma unroll
        for (int j = 0; j < 8; j++) a[j] = 0.f;
#pragma unroll
        for (int i = 0; i < 6; i++)
#pragma unroll
            for (int j = 0; j < 4; j++) { a[2 * j] += bf_lo(cw[i][j]); a[2 * j + 1] += bf_hi(cw[i][j]); }
#pragma unroll
        for (int j = 0; j < 8; j++) {
            float t = a[j];
            t += __shfl_xor(t, 8); t += __shfl_xor(t, 16); t += __shfl_xor(t, 32);
            a[j] = t;
        }
        if (lane < 8) {
            float* rq = (float*)(smem + K1_RED) + lw * 64 + lane * 8;
            *(f4v*)rq = f4v{a[0], a[1], a[2], a[3]};
            *(f4v*)(rq + 4) = f4v{a[4], a[5], a[6], a[7]};
        }
        WAIT_V(0);
    }
    __syncthreads();
    if (wave == 7 && blockIdx.x < 216) {
        const float* rq = (const float*)(smem + K1_RED) + lane;
        cs1[rthird * F_ + cchunk * 64 + lane] = (rq[0] + rq[64]) + (rq[128] + rq[192]);
    }
}

constexpr int K2_CH = 8192 + 64 * 144;
constexpr int K2_LDS = 9 * K2_CH + 64;

__global__ __launch_bounds__(512, 2) void k2_wo(const bf16_t* __restrict__ o, const bf16_t* __restrict__ wo,
                                                const bf16_t* __restrict__ x, bf16_t* __restrict__ h,
                                                char* __restrict__ hT, f2v* __restrict__ stats) {
    extern __shared__ __attribute__((aligned(16))) char smem[];
    const int tid = threadIdx.x, lane = tid & 63;
    const int wave = __builtin_amdgcn_readfirstlane(tid >> 6);
    const int rt = (blockIdx.x & 7) * 4 + ((blockIdx.x >> 3) & 3), ct = blockIdx.x >> 5;
    const int r0 = rt * 64, c0 = ct * 72;
    const int g = lane >> 4, q = (lane & 15) >> 2, p = lane & 3;
    if (wave >= 4) {
        const int lw = wave - 4;
        const unsigned lbase = lds_addr(smem);
        unsigned asrc[2], bsrc[3];
#pragma unroll
        for (int i = 0; i < 2; i++) {
            int a = lw * 2 + i;
            int row = a * 8 + (lane >> 3);
            int ch = (lane & 7) ^ (row & 7);
            asrc[i] = (unsigned)(((r0 + row) * D_ + ch * 8) * 2);
        }
#pragma unroll
        for (int i = 0; i < 3; i++) {
            int bi = lw + 4 * i; if (bi > 8) bi = 8;
            int ci = bi * 64 + lane;
            int k = ci / 9, c = ci - k * 9;
            bsrc[i] = (unsigned)((k * D_ + c0 + c * 8) * 2);
        }
#pragma unroll
        for (int c = 0; c < 9; c++) {
            unsigned lb = lbase + c * K2_CH;
            glds16s((const char*)o + c * 128, asrc[0], lb + (lw * 2) * 1024);
            glds16s((const char*)o + c * 128, asrc[1], lb + (lw * 2 + 1) * 1024);
#pragma unroll
            for (int i = 0; i < 3; i++) glds16s((const char*)wo + (size_t)c * 64 * D_ * 2, bsrc[i], lb + 8192 + (lw + 4 * i > 8 ? 8 : lw + 4 * i) * 1024);
        }
        WAIT_V(40); BAR;
        WAIT_V(35); BAR;
        WAIT_V(30); BAR;
        WAIT_V(25); BAR;
        WAIT_V(20); BAR;
        WAIT_V(15); BAR;
        WAIT_V(10); BAR;
        WAIT_V(5); BAR;
        WAIT_V(0); BAR;
        return;
    }
    const int xr = wave * 16 + (lane & 15);
    const int row = r0 + xr;
    u2v xx[5];
#pragma unroll
    for (int n = 0; n < 5; n++) {
        int col = n * 16 + 4 * g; if (col > 68) col = 68;
        xx[n] = *(const u2v*)(x + (size_t)row * D_ + c0 + col);
    }
    f4v acc[5];
#pragma unroll
    for (int n = 0; n < 5; n++) acc[n] = f4v{0, 0, 0, 0};
    const int boff = 8192 + (8 * g + q) * 144 + 8 * p;
#pragma unroll 1
    for (int c = 0; c < 9; c++) {
        BAR;
        const char* base = smem + c * K2_CH;
        s8v xf[2], wf[2][5];
#pragma unroll
        for (int ss = 0; ss < 2; ss++) {
            xf[ss] = *(const s8v*)(base + xr * 128 + (((ss * 4 + g) ^ (xr & 7)) << 4));
#pragma unroll
            for (int n = 0; n < 5; n++) {
                const char* wb = base + boff + ss * 32 * 144 + n * 32;
                wf[ss][n] = cat8(tr_read(wb), tr_read(wb + 4 * 144));
            }
        }
#pragma unroll
        for (int ss = 0; ss < 2; ss++)
#pragma unroll
            for (int n = 0; n < 5; n++) acc[n] = MFMA16(wf[ss][n], xf[ss], acc[n]);
    }
    float hv[5][4];
    float sum = 0.f;
#pragma unroll
    for (int n = 0; n < 5; n++) {
        int col = n * 16 + 4 * g;
        if (col < 72) {
            unsigned p0 = pack_bf2(bf_lo(xx[n][0]) + acc[n][0], bf_hi(xx[n][0]) + acc[n][1]);
            unsigned p1 = pack_bf2(bf_lo(xx[n][1]) + acc[n][2], bf_hi(xx[n][1]) + acc[n][3]);
            *(u2v*)(h + (size_t)row * D_ + c0 + col) = u2v{p0, p1};
            {
                int cc = c0 + col;
                *(u2v*)(hT + (size_t)((row >> 4) * 18 + (cc >> 5)) * 1024 + (((cc & 31) >> 3) * 16 + (row & 15)) * 16 + (cc & 7) * 2) = u2v{p0, p1};
            }
            hv[n][0] = bf_lo(p0); hv[n][1] = bf_hi(p0); hv[n][2] = bf_lo(p1); hv[n][3] = bf_hi(p1);
            sum += hv[n][0] + hv[n][1] + hv[n][2] + hv[n][3];
        } else {
            hv[n][0] = hv[n][1] = hv[n][2] = hv[n][3] = 0.f;
        }
    }
    sum += __shfl_xor(sum, 16); sum += __shfl_xor(sum, 32);
    float mean = sum * (1.f / 72.f);
    float m2 = 0.f;
#pragma unroll
    for (int n = 0; n < 5; n++) {
        int col = n * 16 + 4 * g;
        if (col < 72) {
#pragma unroll
            for (int j = 0; j < 4; j++) { float d = hv[n][j] - mean; m2 += d * d; }
        }
    }
    m2 += __shfl_xor(m2, 16); m2 += __shfl_xor(m2, 32);
    if (g == 0) stats[(size_t)row * 8 + ct] = f2v{sum, m2};
}

constexpr int K3_STAGE = 8192 + 18432;
constexpr int K3_NB = 5;
constexpr int K3_PAR = K3_NB * K3_STAGE;
constexpr int K3_LDS = K3_PAR + 1024 + 2304;

__device__ __forceinline__ float gelu_tanh(float v) {
    float t = v * v;
    float z = v * (-2.302208198f * 0.044715f * t - 2.302208198f);
    float e = __builtin_amdgcn_exp2f(z);
    return v * __builtin_amdgcn_rcpf(1.f + e);
}

__device__ __forceinline__ f2v gelu_tanh2(f2v v) {
    f2v t = v * v;
    f2v z = v * (t * (-2.302208198f * 0.044715f) + (-2.302208198f));
    f2v d = f2v{__builtin_amdgcn_exp2f(z[0]), __builtin_amdgcn_exp2f(z[1])} + 1.f;
    return v * f2v{__builtin_amdgcn_rcpf(d[0]), __builtin_amdgcn_rcpf(d[1])};
}

__global__ __launch_bounds__(512, 2) void k3_up(const char* __restrict__ hT, const bf16_t* __restrict__ w1,
                                                const bf16_t* __restrict__ b1, const float* __restrict__ cs1,
                                                const f2v* __restrict__ stats, const bf16_t* __restrict__ w2,
                                                char* __restrict__ h1f, unsigned* __restrict__ cnt) {
    extern __shared__ __attribute__((aligned(16))) char smem[];
    const int tid = threadIdx.x, lane = tid & 63;
    const int wave = __builtin_amdgcn_readfirstlane(tid >> 6);
    const int xcd = blockIdx.x & 7, idx = blockIdx.x >> 3;
    const int rt = (xcd & 1) * 8 + (idx & 7), ct = (xcd >> 1) * 4 + (idx >> 3);
    const int r0 = rt * 128, c0 = ct * 288;
    const unsigned lbase = lds_addr(smem);
    const int g = lane >> 4, q = (lane & 15) >> 2, p = lane & 3;
    const int xr = lane & 15;
    f2v* mu = (f2v*)(smem + K3_PAR);
    float* csl = (float*)(smem + K3_PAR + 1024);
    float* bbl = csl + 288;
    f4v acc[4][9];
    const int wm = (wave >> 1) & 1, wn = wave & 1;
    if (wave >= 4) {
        const int lw = wave - 4;
        unsigned bsrc[5];
        const unsigned avo = lane * 16;
        const char* ag = hT + (size_t)(rt * 8 + lw * 2) * 18 * 1024;
#pragma unroll
        for (int i = 0; i < 5; i++) {
            int bi = lw * 5 + i; if (bi > 17) bi = 17;
            int off = bi * 1024 + lane * 16;
            int k = off / 576, r = off - k * 576;
            int nt = (r >> 5) ^ ((k >> 3) & 1);
            bsrc[i] = (unsigned)((k * F_ + c0 + nt * 16 + ((r >> 4) & 1) * 8) * 2);
        }
        auto issue = [&](int st) {
            int sc = st > 17 ? 17 : st;
            unsigned lb = lbase + (st % K3_NB) * K3_STAGE;
            const char* ga = ag + sc * 1024;
            const char* gb = (const char*)w1 + (size_t)sc * 32 * F_ * 2;
            glds16s(ga, avo, lb + (lw * 2) * 1024);
            glds16s(ga + 18 * 1024, avo, lb + (lw * 2 + 1) * 1024);
#pragma unroll
            for (int i = 0; i < 5; i++) glds16s(gb, bsrc[i], lb + 8192 + (lw * 5 + i > 17 ? 17 : lw * 5 + i) * 1024);
        };
#pragma unroll
        for (int st = 0; st < K3_NB - 1; st++) issue(st);
        WAIT_V(21);
        BAR;
        for (int i = -1; i < 18; i++) {
            if (i >= 0) issue(i + K3_NB - 1);
            WAIT_V(14);
            BAR;
        }
        WAIT_V(0);
    } else {
        {
            float c0v = (cs1[c0 + tid] + cs1[F_ + c0 + tid]) + cs1[2 * F_ + c0 + tid], b0v = bf2f(b1[c0 + tid]);
            float c1v = 0.f, b1v = 0.f;
            if (tid < 32) { c1v = (cs1[c0 + 256 + tid] + cs1[F_ + c0 + 256 + tid]) + cs1[2 * F_ + c0 + 256 + tid]; b1v = bf2f(b1[c0 + 256 + tid]); }
            if (tid < 128) {
                const f4v* sp = (const f4v*)(stats + (size_t)(r0 + tid) * 8);
                f4v v[4];
#pragma unroll
                for (int i = 0; i < 4; i++) v[i] = sp[i];
                float tot = 0.f;
#pragma unroll
                for (int i = 0; i < 4; i++) tot += v[i][0] + v[i][2];
                float mean = tot * (1.f / 576.f);
                float M2 = 0.f;
#pragma unroll
                for (int i = 0; i < 4; i++) {
                    float d0 = v[i][0] * (1.f / 72.f) - mean, d1 = v[i][2] * (1.f / 72.f) - mean;
                    M2 += v[i][1] + v[i][3] + 72.f * (d0 * d0 + d1 * d1);
                }
                float rstd = rsqrtf(M2 * (1.f / 576.f) + 1e-5f);
                mu[tid] = f2v{mean * rstd, rstd};
            }
            csl[tid] = c0v; bbl[tid] = b0v;
            if (tid < 32) { csl[256 + tid] = c1v; bbl[256 + tid] = b1v; }
        }
#pragma unroll
        for (int m = 0; m < 4; m++)
#pragma unroll
            for (int n = 0; n < 9; n++) acc[m][n] = f4v{0, 0, 0, 0};
        const int dlt = (g & 1) ? 32 : 0;
        const int wb0 = 8192 + (8 * g + q) * 576 + wn * 9 * 32 + 8 * p;
        const int wbe = wb0 + ((wn & 1) ? -dlt : dlt);
        const int wbo = wb0 + ((wn & 1) ? dlt : -dlt);
        const int xoff = wm * 4096 + lane * 16;
        s8v xa[4], xb[4], wf[9];
        BAR;
        {
            const char* base = smem;
#pragma unroll
            for (int m = 0; m < 4; m++) xa[m] = *(const s8v*)(base + xoff + m * 1024);
#pragma unroll
            for (int n = 0; n < 9; n++) {
                const char* wb = base + ((n & 1) ? wbo : wbe) + n * 32;
                wf[n] = cat8(tr_read(wb), tr_read(wb + 4 * 576));
            }
        }
        WAIT_L(0);
        BAR;
#define K3_STEP(XC, XN) { \
            const char* base = smem + ((i + 1) % K3_NB) * K3_STAGE; \
            s4v wlo, whi; \
            _Pragma("unroll") for (int n = 0; n < 9; n++) { \
                const char* wbp = base + (((n - 1) & 1) ? wbo : wbe) + (n - 1) * 32; \
                acc[0][n] = MFMA16(wf[n], XC[0], acc[0][n]); SB0; \
                if (n > 0) { wlo = tr_read(wbp); SB0; } \
                acc[1][n] = MFMA16(wf[n], XC[1], acc[1][n]); SB0; \
                if (n > 0) { whi = tr_read(wbp + 4 * 576); wf[n - 1] = cat8(wlo, whi); SB0; } \
                acc[2][n] = MFMA16(wf[n], XC[2], acc[2][n]); SB0; \
                if (n < 4) { XN[n] = *(const s8v*)(base + xoff + n * 1024); SB0; } \
                acc[3][n] = MFMA16(wf[n], XC[3], acc[3][n]); SB0; \
            } \
            { const char* wbp = base + wbe + 8 * 32; wf[8] = cat8(tr_read(wbp), tr_read(wbp + 4 * 576)); } \
            BAR; }
#pragma unroll 1
        for (int i = 0; i < 18; i += 2) {
            K3_STEP(xa, xb);
            i++; K3_STEP(xb, xa); i--;
        }
    }
    __syncthreads();
    if (blockIdx.x == 0 && tid < 64) cnt[tid * 32] = 0u;
#pragma unroll
    for (int j = 0; j < 3; j++) {
        int c = blockIdx.x * 24 + wave * 3 + j; if (c > 5183) c = 5183;
        glds16s((const char*)w2 + (size_t)c * 1024, (unsigned)(lane * 16), lbase + 98304 + wave * 1024);
    }
    f4v* ex = (f4v*)smem + (wave & 3) * (18 * 64) + lane;
    if (wave < 4) {
#pragma unroll
        for (int m = 0; m < 2; m++)
#pragma unroll
            for (int n = 0; n < 9; n++) ex[(m * 9 + n) * 64] = acc[m + 2][n];
    }
    __syncthreads();
    if (wave >= 4) {
#pragma unroll
        for (int m = 0; m < 2; m++)
#pragma unroll
            for (int n = 0; n < 9; n++) acc[m][n] = ex[(m * 9 + n) * 64];
    }
    {
        const int mb = (wave >> 2) * 2;
#pragma unroll
        for (int m = 0; m < 2; m++) {
            int rl = wm * 64 + (mb + m) * 16 + xr;
            f2v ms = mu[rl];
            char* ob = h1f + (size_t)((r0 + wm * 64 + (mb + m) * 16) >> 4) * (144 * 1024) + lane * 16;
            u2v pv[9];
#pragma unroll
            for (int n = 0; n < 9; n++) {
                f4v a = acc[m][n];
                int cl = wn * 144 + n * 16 + 4 * g;
                f4v cs = *(const f4v*)(csl + cl), bb = *(const f4v*)(bbl + cl);
                f2v t01 = f2v{bb[0], bb[1]} - f2v{cs[0], cs[1]} * ms[0];
                f2v t23 = f2v{bb[2], bb[3]} - f2v{cs[2], cs[3]} * ms[0];
                f2v v01 = gelu_tanh2(f2v{a[0], a[1]} * ms[1] + t01);
                f2v v23 = gelu_tanh2(f2v{a[2], a[3]} * ms[1] + t23);
                pv[n] = u2v{pack_bf2(v01[0], v01[1]), pack_bf2(v23[0], v23[1])};
            }
            if (m == 0) {
            } else {
            }
            char* oc = ob + (c0 >> 5) * 1024;
            if (wn == 0) {
#pragma unroll
                for (int j = 0; j < 4; j++) *(u4v*)(oc + j * 1024) = u4v{pv[2 * j][0], pv[2 * j][1], pv[2 * j + 1][0], pv[2 * j + 1][1]};
                *(u2v*)(oc + 4 * 1024) = pv[8];
            } else {
                *(u2v*)(oc + 4 * 1024 + 8) = pv[0];
#pragma unroll
                for (int j = 0; j < 4; j++) *(u4v*)(oc + (5 + j) * 1024) = u4v{pv[2 * j + 1][0], pv[2 * j + 1][1], pv[2 * j + 2][0], pv[2 * j + 2][1]};
            }
        }
    }
    WAIT_V(0);
}

constexpr int K4_STAGE = 16384 + 8 * 2432;
constexpr int K4_NB = 4;
constexpr int K4_LDS = 147456;

__global__ __launch_bounds__(512, 2) void k4_down(const char* __restrict__ h1f, const bf16_t* __restrict__ w2,
                                                  const bf16_t* __restrict__ h, const bf16_t* __restrict__ b2,
                                                  char* __restrict__ slab, unsigned* __restrict__ cnt,
                                                  bf16_t* __restrict__ out) {
    extern __shared__ __attribute__((aligned(16))) char smem[];
    const int tid = threadIdx.x, lane = tid & 63;
    const int wave = __builtin_amdgcn_readfirstlane(tid >> 6);
    const int xcd = blockIdx.x & 7, idx = blockIdx.x >> 3;
    const int ks = xcd >> 1;
    const int rt = (xcd & 1) * 8 + (idx & 7), ct = idx >> 3;
    const int r0 = rt * 128, c0 = ct * 144, k0 = ks * 1152;
    const unsigned lbase = lds_addr(smem);
    const int g = lane >> 4, q = (lane & 15) >> 2, p = lane & 3;
    const int wk = (wave >> 1) & 1, wm = wave & 1;
    f4v acc[4][9];
    if (wave >= 4) {
        const int lw = wave - 4;
        unsigned bsrc[5];
#pragma unroll
        for (int i = 0; i < 5; i++) {
            int bi = lw * 5 + i; if (bi > 18) bi = 18;
            int off = bi * 1024 + lane * 16;
            int blk = off / 2432, r = off - blk * 2432;
            int kk = r / 288, cr = r - kk * 288;
            if (kk > 7) { kk = 7; cr = 0; }
            bsrc[i] = (unsigned)(((k0 + blk * 8 + kk) * D_ + c0) * 2 + cr);
        }
        const char* xg = h1f + ((size_t)(rt * 8 + lw * 2) * 144 + (k0 >> 5)) * 1024;
        const unsigned xvo = lane * 16;
        auto issue = [&](int st) {
            int sc = st > 17 ? 17 : st;
            unsigned lb = lbase + (st % K4_NB) * K4_STAGE;
            const char* gx = xg + sc * 2048;
            glds16s(gx, xvo, lb + (lw * 4) * 1024);
            glds16s(gx + 1024, xvo, lb + (lw * 4 + 1) * 1024);
            glds16s(gx + 144 * 1024, xvo, lb + (lw * 4 + 2) * 1024);
            glds16s(gx + 145 * 1024, xvo, lb + (lw * 4 + 3) * 1024);
            const char* gb = (const char*)w2 + (size_t)sc * 64 * D_ * 2;
#pragma unroll
            for (int i = 0; i < 5; i++) glds16s(gb, bsrc[i], lb + 16384 + (lw * 5 + i > 18 ? 18 : lw * 5 + i) * 1024);
        };
#pragma unroll
        for (int st = 0; st < K4_NB - 1; st++) issue(st);
        WAIT_V(18);
        BAR;
        for (int i = -1; i < 18; i++) {
            if (i >= 0) issue(i + K4_NB - 1);
            WAIT_V(9);
            BAR;
        }
        WAIT_V(0);
    } else {
#pragma unroll
        for (int m = 0; m < 4; m++)
#pragma unroll
            for (int n = 0; n < 9; n++) acc[m][n] = f4v{0, 0, 0, 0};
        const int kA = wk * 32 + 4 * g + q, kB = kA + 16;
        const int wbA = 16384 + (kA >> 3) * 2432 + (kA & 7) * 288 + 8 * p;
        const int wbB = 16384 + (kB >> 3) * 2432 + (kB & 7) * 288 + 8 * p;
        const int xoff = (wm * 8 + wk) * 1024 + lane * 16;
        s8v xa[4], xb[4], wf[9];
        BAR;
        {
            const char* base = smem;
#pragma unroll
            for (int m = 0; m < 4; m++) xa[m] = *(const s8v*)(base + xoff + m * 2048);
#pragma unroll
            for (int n = 0; n < 9; n++) wf[n] = cat8(tr_read(base + wbA + n * 32), tr_read(base + wbB + n * 32));
        }
        WAIT_L(0);
        BAR;
#define K4_STEP(XC, XN) { \
            const char* base = smem + ((i + 1) % K4_NB) * K4_STAGE; \
            s4v wlo, whi; \
            _Pragma("unroll") for (int n = 0; n < 9; n++) { \
                acc[0][n] = MFMA16(wf[n], XC[0], acc[0][n]); SB0; \
                if (n > 0) { wlo = tr_read(base + wbA + (n - 1) * 32); SB0; } \
                acc[1][n] = MFMA16(wf[n], XC[1], acc[1][n]); SB0; \
                if (n > 0) { whi = tr_read(base + wbB + (n - 1) * 32); wf[n - 1] = cat8(wlo, whi); SB0; } \
                acc[2][n] = MFMA16(wf[n], XC[2], acc[2][n]); SB0; \
                if (n < 4) { XN[n] = *(const s8v*)(base + xoff + n * 2048); SB0; } \
                acc[3][n] = MFMA16(wf[n], XC[3], acc[3][n]); SB0; \
            } \
            wf[8] = cat8(tr_read(base + wbA + 8 * 32), tr_read(base + wbB + 8 * 32)); \
            BAR; }
#pragma unroll 1
        for (int i = 0; i < 18; i += 2) {
            K4_STEP(xa, xb);
            i++; K4_STEP(xb, xa); i--;
        }
    }
    __syncthreads();
    if (wave < 4) {
        f4v* ex = (f4v*)smem + wave * (18 * 64) + lane;
        if (wk == 0) {
#pragma unroll
            for (int m = 0; m < 2; m++)
#pragma unroll
                for (int n = 0; n < 9; n++) ex[(m * 9 + n) * 64] = acc[2 + m][n];
        } else {
#pragma unroll
            for (int m = 0; m < 2; m++)
#pragma unroll
                for (int n = 0; n < 9; n++) ex[(m * 9 + n) * 64] = acc[m][n];
        }
    }
    __syncthreads();
    const int tile = rt * 4 + ct;
    const __amdgpu_buffer_rsrc_t srs = __builtin_amdgcn_make_buffer_rsrc(slab, 0, 64 * 16 * 18432, 0x00020000);
    if (wave < 4) {
        const f4v* ei = (const f4v*)smem + (wave ^ 2) * (18 * 64) + lane;
        const int qd = wm * 2 + wk;
        const int so = ((tile * 4 + ks) * 4 + qd) * 18432;
        f4v* oq = (f4v*)(smem + 73728) + lane;
#define K4_PUB(M0) \
        _Pragma("unroll") for (int m = 0; m < 2; m++) \
        _Pragma("unroll") for (int n = 0; n < 9; n++) { \
            f4v v = acc[M0 + m][n] + ei[(m * 9 + n) * 64]; \
            if (qd != ks) __builtin_amdgcn_raw_buffer_store_b128(__builtin_bit_cast(u4v, v), srs, lane * 16 + (m * 9 + n) * 1024, so, 16); \
            else oq[(m * 9 + n) * 64] = v; \
        }
        if (wk == 0) { K4_PUB(0) } else { K4_PUB(2) }
        WAIT_V(0);
    }
    __syncthreads();
    if (tid == 0) __hip_atomic_fetch_add(cnt + tile * 32, 1u, __ATOMIC_RELAXED, __HIP_MEMORY_SCOPE_AGENT);
    if (wave == 4) {
        for (int spin = 0; spin < (1 << 22); spin++) {
            unsigned c = __hip_atomic_load(cnt + tile * 32, __ATOMIC_RELAXED, __HIP_MEMORY_SCOPE_AGENT);
            if (c >= 4u) break;
            __builtin_amdgcn_s_sleep(1);
        }
    }
    __syncthreads();
    {
        const f4v* oq = (const f4v*)(smem + 73728) + lane;
        f4v v[3][4];
        u2v hh[3], bb[3];
#pragma unroll
        for (int j = 0; j < 3; j++) {
            int t = wave + 8 * j;
            if (t < 18) {
                const int m = t / 9, n = t - m * 9;
#pragma unroll
                for (int sk = 0; sk < 4; sk++) {
                    if (sk == ks) v[j][sk] = oq[t * 64];
                    else v[j][sk] = __builtin_bit_cast(f4v, __builtin_amdgcn_raw_buffer_load_b128(srs, lane * 16 + t * 1024, ((tile * 4 + sk) * 4 + ks) * 18432, 16));
                }
                const int row = r0 + ks * 32 + m * 16 + (lane & 15), col = c0 + n * 16 + 4 * g;
                hh[j] = *(const u2v*)(h + (size_t)row * D_ + col);
                bb[j] = *(const u2v*)(b2 + col);
            }
        }
#pragma unroll
        for (int j = 0; j < 3; j++) {
            int t = wave + 8 * j;
            if (t < 18) {
                const int m = t / 9, n = t - m * 9;
                f4v a = ((v[j][0] + v[j][1]) + v[j][2]) + v[j][3];
                const int row = r0 + ks * 32 + m * 16 + (lane & 15), col = c0 + n * 16 + 4 * g;
                float v0 = bf_lo(hh[j][0]) + (a[0] + bf_lo(bb[j][0]));
                float v1 = bf_hi(hh[j][0]) + (a[1] + bf_hi(bb[j][0]));
                float v2 = bf_lo(hh[j][1]) + (a[2] + bf_lo(bb[j][1]));
                float v3 = bf_hi(hh[j][1]) + (a[3] + bf_hi(bb[j][1]));
                *(u2v*)(out + (size_t)row * D_ + col) = u2v{pack_bf2(v0, v1), pack_bf2(v2, v3)};
            }
        }
    }
}

extern "C" void kernel_launch(void* const* d_in, const int* in_sizes, int n_in,
                              void* d_out, int out_size, void* d_ws, size_t ws_size,
                              hipStream_t stream) {
    const bf16_t* x = (const bf16_t*)d_in[0];
    const bf16_t* wqkv = (const bf16_t*)d_in[1];
    const bf16_t* wo = (const bf16_t*)d_in[2];
    const bf16_t* w1 = (const bf16_t*)d_in[4];
    const bf16_t* b1 = (const bf16_t*)d_in[5];
    const bf16_t* w2 = (const bf16_t*)d_in[6];
    const bf16_t* b2 = (const bf16_t*)d_in[7];
    bf16_t* out = (bf16_t*)d_out;
    char* ws = (char*)d_ws;
    bf16_t* o = (bf16_t*)(ws);
    bf16_t* h = (bf16_t*)(ws + (4 << 20));
    char* hT = ws + (12 << 20);
    f2v* stats = (f2v*)(ws + (8 << 20));
    float* cs1 = (float*)(ws + (9 << 20));
    char* h1 = (char*)(ws + (16 << 20));
    char* slab = ws + (48 << 20);
    unsigned* cnt = (unsigned*)(ws + (10 << 20));

    hipFuncSetAttribute((const void*)k1_qkv_attn, hipFuncAttributeMaxDynamicSharedMemorySize, K1_LDS);
    hipFuncSetAttribute((const void*)k2_wo, hipFuncAttributeMaxDynamicSharedMemorySize, K2_LDS);
    hipFuncSetAttribute((const void*)k3_up, hipFuncAttributeMaxDynamicSharedMemorySize, K3_LDS);
    hipFuncSetAttribute((const void*)k4_down, hipFuncAttributeMaxDynamicSharedMemorySize, K4_LDS);

    k1_qkv_attn<<<256, 512, K1_LDS, stream>>>(x, wqkv, w1, wo, o, cs1);
    k2_wo<<<256, 512, K2_LDS, stream>>>(o, wo, x, h, hT, stats);
    k3_up<<<256, 512, K3_LDS, stream>>>(hT, w1, b1, cs1, stats, w2, h1, cnt);
    k4_down<<<256, 512, K4_LDS, stream>>>(h1, w2, h, b2, slab, cnt, out);
}
```

```cpp
#include <hip/hip_runtime.h>
#include <hip/hip_bf16.h>

typedef __attribute__((ext_vector_type(8))) short s8v;
typedef __attribute__((ext_vector_type(4))) short s4v;
typedef __attribute__((ext_vector_type(4))) short v4i16_t;
typedef __attribute__((ext_vector_type(4))) float f4v;
typedef __attribute__((ext_vector_type(2))) float f2v;
typedef __attribute__((ext_vector_type(4))) unsigned u4v;
typedef __attribute__((ext_vector_type(2))) unsigned u2v;
typedef unsigned short bf16_t;

constexpr int S_ = 2048, D_ = 576, NQKV = 1728, F_ = 4608;

__device__ __forceinline__ unsigned lds_addr(const void* l) {
    return (unsigned)(size_t)(__attribute__((address_space(3))) const char*)l;
}
__device__ __forceinline__ void glds16(const void* g, unsigned dst) {
    unsigned keep;
    asm volatile("s_mov_b32 %0, m0\n\ts_mov_b32 m0, %2\n\ts_nop 0\n\tglobal_load_lds_dwordx4 %1, off\n\ts_mov_b32 m0, %0"
                 : "=&s"(keep) : "v"(g), "s"(dst) : "memory");
}
__device__ __forceinline__ void glds16s(const void* sbase, unsigned voff, unsigned dst) {
    unsigned keep;
    asm volatile("s_mov_b32 %0, m0\n\ts_mov_b32 m0, %3\n\ts_nop 0\n\tglobal_load_lds_dwordx4 %1, %2\n\ts_mov_b32 m0, %0"
                 : "=&s"(keep) : "v"(voff), "s"(sbase), "s"(dst) : "memory");
}
__device__ __forceinline__ s4v tr_read(const char* p) {
    return __builtin_bit_cast(s4v, __builtin_amdgcn_ds_read_tr16_b64_v4i16((__attribute__((address_space(3))) v4i16_t*)p));
}
__device__ __forceinline__ s8v cat8(s4v a, s4v b) {
    return __builtin_shufflevector(a, b, 0, 1, 2, 3, 4, 5, 6, 7);
}
__device__ __forceinline__ float bf_lo(unsigned u) { return __builtin_bit_cast(float, u << 16); }
__device__ __forceinline__ float bf_hi(unsigned u) { return __builtin_bit_cast(float, u & 0xffff0000u); }
__device__ __forceinline__ float bf2f(bf16_t v) { return __builtin_bit_cast(float, (unsigned)v << 16); }
__device__ __forceinline__ unsigned pack_bf2(float a, float b) {
    unsigned r;
    asm("v_cvt_pk_bf16_f32 %0, %1, %2" : "=v"(r) : "v"(a), "v"(b));
    return r;
}
#define MFMA16(a, b, c) __builtin_amdgcn_mfma_f32_16x16x32_bf16(a, b, c, 0, 0, 0)
#define WAIT_V(n) asm volatile("s_waitcnt vmcnt(" #n ")" ::: "memory")
#define WAIT_L(n) asm volatile("s_waitcnt lgkmcnt(" #n ")" ::: "memory")

#define SB0 __builtin_amdgcn_sched_barrier(0)
#define BAR __builtin_amdgcn_s_barrier()
constexpr int K1_XN_BYTES = 9 * 80 * 128;
constexpr int K1_STAGE = 14336;
constexpr int K1_NB = 4;
constexpr int K1_DUMMY = K1_XN_BYTES + K1_NB * K1_STAGE;
constexpr int K1_RED = K1_DUMMY + 4096;
constexpr int K1_LDS = K1_RED + 2048;

__global__ __launch_bounds__(512, 2) void k1_qkv_attn(const bf16_t* __restrict__ x, const bf16_t* __restrict__ wqkv,
                                                      const bf16_t* __restrict__ w1, const bf16_t* __restrict__ wo,
                                                      bf16_t* __restrict__ o,
                                                      float* __restrict__ cs1) {
    extern __shared__ __attribute__((aligned(16))) char smem[];
    const int tid = threadIdx.x, lane = tid & 63;
    const int wave = __builtin_amdgcn_readfirstlane(tid >> 6);
    const int xcd = blockIdx.x & 7, idx = blockIdx.x >> 3;
    const int hg = (xcd & 1) * 4 + (idx >> 3), rt = (xcd >> 1) * 8 + (idx & 7);
    const int s0 = rt * 64;
    const unsigned lring = lds_addr(smem) + K1_XN_BYTES;
    const int g = lane >> 4, q = (lane & 15) >> 2, p = lane & 3;

    const int lw = wave - 4;
    unsigned bsrc[4];
    auto issue = [&](int st) {
        int sc = st > 17 ? 17 : st;
        unsigned lb = lring + (st % K1_NB) * K1_STAGE;
        const char* gb = (const char*)wqkv + (size_t)sc * 32 * NQKV * 2;
#pragma unroll
        for (int i = 0; i < 4; i++) glds16s(gb, bsrc[i], lb + (lw * 4 + i > 13 ? 13 : lw * 4 + i) * 1024);
    };
    u4v cw[6];
    const int cu = blockIdx.x < 216 ? blockIdx.x : blockIdx.x - 216;
    const int cchunk = cu % 72, rthird = cu / 72;
    const unsigned dz = lds_addr(smem) + K1_DUMMY + (wave & 3) * 1024;
    if (wave >= 4) {
#pragma unroll
        for (int i = 0; i < 4; i++) {
            int bi = lw * 4 + i; if (bi > 13) bi = 13;
            int off = bi * 1024 + lane * 16;
            int k = off / 448, r = off - k * 448;
            int nt = (r >> 5) ^ ((k >> 3) & 1);
            int pc = nt * 2 + ((r >> 4) & 1); if (pc > 26) pc = 26;
            int sec = pc / 9, wi = pc - sec * 9;
            bsrc[i] = (unsigned)((k * NQKV + sec * 576 + hg * 72 + wi * 8) * 2);
        }
#pragma unroll
        for (int st = 0; st < K1_NB - 1; st++) issue(st);
#pragma unroll
        for (int j = 0; j < 8; j++) {
            int c = (lw * 8 + j) * 64 + lane; if (c > 1943) c = 1943;
            int seg = c / 9, pc = c - seg * 9;
            int sec = seg / 72, rr = seg - sec * 72;
            glds16s(wqkv, (unsigned)((((idx & 7) * 72 + rr) * NQKV + sec * 576 + hg * 72 + pc * 8) * 2), dz);
        }
    } else {
        u4v xv[3][9];
#pragma unroll
        for (int j = 0; j < 3; j++) {
            if (j < 2 || wave < 2) {
                int r = (4 * j + wave) * 8 + (lane >> 3);
                int s = s0 - 16 + r; if (s < 0) s = 0;
                const u4v* pp = (const u4v*)(x + (size_t)s * D_) + (lane & 7);
#pragma unroll
                for (int i = 0; i < 9; i++) xv[j][i] = pp[i * 8];
            }
            if (j == 0) {
            }
        }
#pragma unroll
        for (int j = 0; j < 3; j++) {
            if (j < 2 || wave < 2) {
                const int r = (4 * j + wave) * 8 + (lane >> 3);
                f2v v[36];
                f2v sm = f2v{0.f, 0.f};
#pragma unroll
                for (int i = 0; i < 9; i++)
#pragma unroll
                    for (int t = 0; t < 4; t++) { v[i * 4 + t] = f2v{bf_lo(xv[j][i][t]), bf_hi(xv[j][i][t])}; sm += v[i * 4 + t]; }
                float sum = sm[0] + sm[1];
                sum += __shfl_xor(sum, 1); sum += __shfl_xor(sum, 2); sum += __shfl_xor(sum, 4);
                const float mean = sum * (1.f / 576.f);
                f2v sq = f2v{0.f, 0.f};
#pragma unroll
                for (int i = 0; i < 36; i++) { v[i] = v[i] - mean; sq += v[i] * v[i]; }
                float sqs = sq[0] + sq[1];
                sqs += __shfl_xor(sqs, 1); sqs += __shfl_xor(sqs, 2); sqs += __shfl_xor(sqs, 4);
                const float rstd = rsqrtf(sqs * (1.f / 576.f) + 1e-5f);
#pragma unroll
                for (int i = 0; i < 9; i++) {
                    u4v ov;
#pragma unroll
                    for (int t = 0; t < 4; t++) { f2v w = v[i * 4 + t] * rstd; ov[t] = pack_bf2(w[0], w[1]); }
                    *(u4v*)(smem + i * (80 * 128) + r * 128 + (((lane & 7) ^ (r & 7)) << 4)) = ov;
                }
                if (j == 0) {
                }
                if (j == 1) {
                }
            }
        }
    }

    f4v acc[5][4];
    if (wave >= 4) {
        WAIT_V(16);
        __syncthreads();
        WAIT_V(12); BAR;
        issue(3); WAIT_V(12); BAR;
        for (int i = 1; i < 18; i++) {
            issue(i + K1_NB - 1);
            WAIT_V(4);
            BAR;
        }
        WAIT_V(0);
        {
            const u4v* pw = (const u4v*)(w1 + (size_t)(rthird * 192 + lw * 48 + (lane >> 3)) * F_ + cchunk * 64) + (lane & 7);
#pragma unroll
            for (int j = 0; j < 6; j++) cw[j] = pw[(size_t)j * 8 * F_ / 8];
            int c = blockIdx.x * 4 + lw; if (c > 647) c = 647;
            glds16s((const char*)wo + (size_t)c * 1024, (unsigned)(lane * 16), dz);
        }
    } else {
        const int nt0 = wave == 0 ? 0 : 1 + 3 * wave;
        const int nn = (wave == 0 || wave == 3) ? 4 : 3;
#pragma unroll
        for (int m = 0; m < 5; m++)
#pragma unroll
            for (int n = 0; n < 4; n++) acc[m][n] = f4v{0, 0, 0, 0};
        const int xr = lane & 15;
        int woff[4];
#pragma unroll
        for (int n = 0; n < 4; n++) woff[n] = K1_XN_BYTES + (8 * g + q) * 448 + (((nt0 + n) ^ (g & 1)) << 5) + 8 * p;
        s8v xa[5], xb[5], wf[4];
        auto rdx = [&](s8v* X, int st) {
            const char* xbp = smem + (st >> 1) * (80 * 128) + xr * 128 + ((((st & 1) * 4 + g) ^ (xr & 7)) << 4);
#pragma unroll
            for (int m = 0; m < 5; m++) X[m] = *(const s8v*)(xbp + m * 2048);
        };
        __syncthreads();
        rdx(xa, 0);
#pragma unroll
        for (int n = 0; n < 4; n++) wf[n] = cat8(tr_read(smem + woff[n]), tr_read(smem + woff[n] + 4 * 448));
        WAIT_L(0);
        BAR;
#define K1_STEP(XC, XN) { \
            const char* base = smem + ((i + 1) % K1_NB) * K1_STAGE; \
            rdx(XN, i + 1 > 17 ? 17 : i + 1); \
            SB0; \
            _Pragma("unroll") for (int n = 0; n < 4; n++) { \
                if (n < 3 || nn == 4) { \
                    if (wave != 0) acc[0][n] = MFMA16(XC[0], wf[n], acc[0][n]); \
                    _Pragma("unroll") for (int m = 1; m < 5; m++) acc[m][n] = MFMA16(XC[m], wf[n], acc[m][n]); \
                    SB0; \
                    wf[n] = cat8(tr_read(base + woff[n]), tr_read(base + woff[n] + 4 * 448)); \
                    SB0; \
                } \
            } \
            BAR; }
#pragma unroll 1
        for (int i = 0; i < 18; i += 2) {
            K1_STEP(xa, xb);
            i++; K1_STEP(xb, xa); i--;
        }
    }
    __syncthreads();
    float* qkv = (float*)smem;
    if (wave < 4) {
        const int nt0 = wave == 0 ? 0 : 1 + 3 * wave;
        const int nn = (wave == 0 || wave == 3) ? 4 : 3;
#pragma unroll
        for (int m = 0; m < 5; m++)
#pragma unroll
            for (int n = 0; n < 4; n++) {
                int col = (nt0 + n) * 16 + (lane & 15);
                if (n < nn && col < 216) *(f4v*)(qkv + col * 80 + m * 16 + 4 * g) = acc[m][n];
            }
    }
    __syncthreads();
    {
        const int head = wave >> 1;
        const int rl = (wave & 1) * 32 + (lane & 31);
        const int hw = lane >> 5;
        const int r = 16 + rl;
        const int s = s0 + rl;
        const float* qp = qkv + (head * 18) * 80 + r;
        const float* kp = qkv + (72 + head * 18) * 80 + r - 8 * hw;
        const float* vp = qkv + (144 + head * 18) * 80 + r - 8 * hw;
        float qv[18];
#pragma unroll
        for (int d = 0; d < 18; d++) qv[d] = qp[d * 80];
        float lg[4];
        float mx = -3.0e38f;
#pragma unroll
        for (int w = 0; w < 4; w++) {
            float a = 0.f;
#pragma unroll
            for (int d = 0; d < 18; d++) a += qv[d] * kp[d * 80 - 2 * w];
            a *= 0.23570226039551584f;
            lg[w] = (s - 2 * (w + 4 * hw) >= 0) ? a : -1e9f;
            mx = fmaxf(mx, lg[w]);
        }
        mx = fmaxf(mx, __shfl_xor(mx, 32));
        float den = 0.f;
#pragma unroll
        for (int w = 0; w < 4; w++) { lg[w] = __expf(lg[w] - mx); den += lg[w]; }
        den += __shfl_xor(den, 32);
        float inv = 1.f / den;
        float ov[18];
#pragma unroll
        for (int d = 0; d < 18; d++) ov[d] = 0.f;
#pragma unroll
        for (int w = 0; w < 4; w++) {
            float pw = lg[w] * inv;
#pragma unroll
            for (int d = 0; d < 18; d++) ov[d] += pw * vp[d * 80 - 2 * w];
        }
#pragma unroll
        for (int d = 0; d < 18; d++) ov[d] += __shfl_xor(ov[d], 32);
        unsigned* op = (unsigned*)(o + (size_t)s * D_ + hg * 72 + head * 18);
        if (hw == 0) {
#pragma unroll
            for (int d = 0; d < 5; d++) op[d] = pack_bf2(ov[2 * d], ov[2 * d + 1]);
        } else {
#pragma unroll
            for (int d = 5; d < 9; d++) op[d] = pack_bf2(ov[2 * d], ov[2 * d + 1]);
        }
    }
    if (wave >= 4) {
        float a[8];
#pragma unroll
        for (int j = 0; j < 8; j++) a[j] = 0.f;
#pragma unroll
        for (int i = 0; i < 6; i++)
#pragma unroll
            for (int j = 0; j < 4; j++) { a[2 * j] += bf_lo(cw[i][j]); a[2 * j + 1] += bf_hi(cw[i][j]); }
#pragma unroll
        for (int j = 0; j < 8; j++) {
            float t = a[j];
            t += __shfl_xor(t, 8); t += __shfl_xor(t, 16); t += __shfl_xor(t, 32);
            a[j] = t;
        }
        if (lane < 8) {
            float* rq = (float*)(smem + K1_RED) + lw * 64 + lane * 8;
            *(f4v*)rq = f4v{a[0], a[1], a[2], a[3]};
            *(f4v*)(rq + 4) = f4v{a[4], a[5], a[6], a[7]};
        }
        WAIT_V(0);
    }
    __syncthreads();
    if (wave == 7 && blockIdx.x < 216) {
        const float* rq = (const float*)(smem + K1_RED) + lane;
        cs1[rthird * F_ + cchunk * 64 + lane] = (rq[0] + rq[64]) + (rq[128] + rq[192]);
    }
}

constexpr int K2_CH = 8192 + 64 * 144;
constexpr int K2_LDS = 9 * K2_CH + 64;

__global__ __launch_bounds__(512, 2) void k2_wo(const bf16_t* __restrict__ o, const bf16_t* __restrict__ wo,
                                                const bf16_t* __restrict__ x, bf16_t* __restrict__ h,
                                                char* __restrict__ hT, f2v* __restrict__ stats) {
    extern __shared__ __attribute__((aligned(16))) char smem[];
    const int tid = threadIdx.x, lane = tid & 63;
    const int wave = __builtin_amdgcn_readfirstlane(tid >> 6);
    const int rt = (blockIdx.x & 7) * 4 + ((blockIdx.x >> 3) & 3), ct = blockIdx.x >> 5;
    const int r0 = rt * 64, c0 = ct * 72;
    const int g = lane >> 4, q = (lane & 15) >> 2, p = lane & 3;
    if (wave >= 4) {
        const int lw = wave - 4;
        const unsigned lbase = lds_addr(smem);
        unsigned asrc[2], bsrc[3];
#pragma unroll
        for (int i = 0; i < 2; i++) {
            int a = lw * 2 + i;
            int row = a * 8 + (lane >> 3);
            int ch = (lane & 7) ^ (row & 7);
            asrc[i] = (unsigned)(((r0 + row) * D_ + ch * 8) * 2);
        }
#pragma unroll
        for (int i = 0; i < 3; i++) {
            int bi = lw + 4 * i; if (bi > 8) bi = 8;
            int ci = bi * 64 + lane;
            int k = ci / 9, c = ci - k * 9;
            bsrc[i] = (unsigned)((k * D_ + c0 + c * 8) * 2);
        }
#pragma unroll
        for (int c = 0; c < 9; c++) {
            unsigned lb = lbase + c * K2_CH;
            glds16s((const char*)o + c * 128, asrc[0], lb + (lw * 2) * 1024);
            glds16s((const char*)o + c * 128, asrc[1], lb + (lw * 2 + 1) * 1024);
#pragma unroll
            for (int i = 0; i < 3; i++) glds16s((const char*)wo + (size_t)c * 64 * D_ * 2, bsrc[i], lb + 8192 + (lw + 4 * i > 8 ? 8 : lw + 4 * i) * 1024);
        }
        WAIT_V(40); BAR;
        WAIT_V(35); BAR;
        WAIT_V(30); BAR;
        WAIT_V(25); BAR;
        WAIT_V(20); BAR;
        WAIT_V(15); BAR;
        WAIT_V(10); BAR;
        WAIT_V(5); BAR;
        WAIT_V(0); BAR;
        return;
    }
    const int xr = wave * 16 + (lane & 15);
    const int row = r0 + xr;
    u2v xx[5];
#pragma unroll
    for (int n = 0; n < 5; n++) {
        int col = n * 16 + 4 * g; if (col > 68) col = 68;
        xx[n] = *(const u2v*)(x + (size_t)row * D_ + c0 + col);
    }
    f4v acc[5];
#pragma unroll
    for (int n = 0; n < 5; n++) acc[n] = f4v{0, 0, 0, 0};
    const int boff = 8192 + (8 * g + q) * 144 + 8 * p;
#pragma unroll 1
    for (int c = 0; c < 9; c++) {
        BAR;
        const char* base = smem + c * K2_CH;
        s8v xf[2], wf[2][5];
#pragma unroll
        for (int ss = 0; ss < 2; ss++) {
            xf[ss] = *(const s8v*)(base + xr * 128 + (((ss * 4 + g) ^ (xr & 7)) << 4));
#pragma unroll
            for (int n = 0; n < 5; n++) {
                const char* wb = base + boff + ss * 32 * 144 + n * 32;
                wf[ss][n] = cat8(tr_read(wb), tr_read(wb + 4 * 144));
            }
        }
#pragma unroll
        for (int ss = 0; ss < 2; ss++)
#pragma unroll
            for (int n = 0; n < 5; n++) acc[n] = MFMA16(wf[ss][n], xf[ss], acc[n]);
    }
    float hv[5][4];
    float sum = 0.f;
#pragma unroll
    for (int n = 0; n < 5; n++) {
        int col = n * 16 + 4 * g;
        if (col < 72) {
            unsigned p0 = pack_bf2(bf_lo(xx[n][0]) + acc[n][0], bf_hi(xx[n][0]) + acc[n][1]);
            unsigned p1 = pack_bf2(bf_lo(xx[n][1]) + acc[n][2], bf_hi(xx[n][1]) + acc[n][3]);
            *(u2v*)(h + (size_t)row * D_ + c0 + col) = u2v{p0, p1};
            {
                int cc = c0 + col;
                *(u2v*)(hT + (size_t)((row >> 4) * 18 + (cc >> 5)) * 1024 + (((cc & 31) >> 3) * 16 + (row & 15)) * 16 + (cc & 7) * 2) = u2v{p0, p1};
            }
            hv[n][0] = bf_lo(p0); hv[n][1] = bf_hi(p0); hv[n][2] = bf_lo(p1); hv[n][3] = bf_hi(p1);
            sum += hv[n][0] + hv[n][1] + hv[n][2] + hv[n][3];
        } else {
            hv[n][0] = hv[n][1] = hv[n][2] = hv[n][3] = 0.f;
        }
    }
    sum += __shfl_xor(sum, 16); sum += __shfl_xor(sum, 32);
    float mean = sum * (1.f / 72.f);
    float m2 = 0.f;
#pragma unroll
    for (int n = 0; n < 5; n++) {
        int col = n * 16 + 4 * g;
        if (col < 72) {
#pragma unroll
            for (int j = 0; j < 4; j++) { float d = hv[n][j] - mean; m2 += d * d; }
        }
    }
    m2 += __shfl_xor(m2, 16); m2 += __shfl_xor(m2, 32);
    if (g == 0) stats[(size_t)row * 8 + ct] = f2v{sum, m2};
}

constexpr int K3_STAGE = 8192 + 18432;
constexpr int K3_NB = 5;
constexpr int K3_PAR = K3_NB * K3_STAGE;
constexpr int K3_LDS = K3_PAR + 1024 + 2304;

__device__ __forceinline__ float gelu_tanh(float v) {
    float t = v * v;
    float z = v * (-2.302208198f * 0.044715f * t - 2.302208198f);
    float e = __builtin_amdgcn_exp2f(z);
    return v * __builtin_amdgcn_rcpf(1.f + e);
}

__device__ __forceinline__ f2v gelu_tanh2(f2v v) {
    f2v t = v * v;
    f2v z = v * (t * (-2.302208198f * 0.044715f) + (-2.302208198f));
    f2v d = f2v{__builtin_amdgcn_exp2f(z[0]), __builtin_amdgcn_exp2f(z[1])} + 1.f;
    return v * f2v{__builtin_amdgcn_rcpf(d[0]), __builtin_amdgcn_rcpf(d[1])};
}

__global__ __launch_bounds__(512, 2) void k3_up(const char* __restrict__ hT, const bf16_t* __restrict__ w1,
                                                const bf16_t* __restrict__ b1, const float* __restrict__ cs1,
                                                const f2v* __restrict__ stats, const bf16_t* __restrict__ w2,
                                                char* __restrict__ h1f, char* __restrict__ w2f) {
    extern __shared__ __attribute__((aligned(16))) char smem[];
    const int tid = threadIdx.x, lane = tid & 63;
    const int wave = __builtin_amdgcn_readfirstlane(tid >> 6);
    const int xcd = blockIdx.x & 7, idx = blockIdx.x >> 3;
    const int rt = (xcd & 1) * 8 + (idx & 7), ct = (xcd >> 1) * 4 + (idx >> 3);
    const int r0 = rt * 128, c0 = ct * 288;
    const unsigned lbase = lds_addr(smem);
    const int g = lane >> 4, q = (lane & 15) >> 2, p = lane & 3;
    const int xr = lane & 15;
    f2v* mu = (f2v*)(smem + K3_PAR);
    float* csl = (float*)(smem + K3_PAR + 1024);
    float* bbl = csl + 288;
    f4v acc[4][9];
    const int wm = (wave >> 1) & 1, wn = wave & 1;
    if (wave >= 4) {
        const int lw = wave - 4;
        unsigned bsrc[5];
        const unsigned avo = lane * 16;
        const char* ag = hT + (size_t)(rt * 8 + lw * 2) * 18 * 1024;
#pragma unroll
        for (int i = 0; i < 5; i++) {
            int bi = lw * 5 + i; if (bi > 17) bi = 17;
            int off = bi * 1024 + lane * 16;
            int k = off / 576, r = off - k * 576;
            int nt = (r >> 5) ^ ((k >> 3) & 1);
            bsrc[i] = (unsigned)((k * F_ + c0 + nt * 16 + ((r >> 4) & 1) * 8) * 2);
        }
        auto issue = [&](int st) {
            int sc = st > 17 ? 17 : st;
            unsigned lb = lbase + (st % K3_NB) * K3_STAGE;
            const char* ga = ag + sc * 1024;
            const char* gb = (const char*)w1 + (size_t)sc * 32 * F_ * 2;
            glds16s(ga, avo, lb + (lw * 2) * 1024);
            glds16s(ga + 18 * 1024, avo, lb + (lw * 2 + 1) * 1024);
#pragma unroll
            for (int i = 0; i < 5; i++) glds16s(gb, bsrc[i], lb + 8192 + (lw * 5 + i > 17 ? 17 : lw * 5 + i) * 1024);
        };
#pragma unroll
        for (int st = 0; st < K3_NB - 1; st++) issue(st);
        WAIT_V(21);
        BAR;
        for (int i = -1; i < 18; i++) {
            if (i >= 0) issue(i + K3_NB - 1);
            WAIT_V(14);
            BAR;
        }
        WAIT_V(0);
    } else {
        {
            float c0v = (cs1[c0 + tid] + cs1[F_ + c0 + tid]) + cs1[2 * F_ + c0 + tid], b0v = bf2f(b1[c0 + tid]);
            float c1v = 0.f, b1v = 0.f;
            if (tid < 32) { c1v = (cs1[c0 + 256 + tid] + cs1[F_ + c0 + 256 + tid]) + cs1[2 * F_ + c0 + 256 + tid]; b1v = bf2f(b1[c0 + 256 + tid]); }
            if (tid < 128) {
                const f4v* sp = (const f4v*)(stats + (size_t)(r0 + tid) * 8);
                f4v v[4];
#pragma unroll
                for (int i = 0; i < 4; i++) v[i] = sp[i];
                float tot = 0.f;
#pragma unroll
                for (int i = 0; i < 4; i++) tot += v[i][0] + v[i][2];
                float mean = tot * (1.f / 576.f);
                float M2 = 0.f;
#pragma unroll
                for (int i = 0; i < 4; i++) {
                    float d0 = v[i][0] * (1.f / 72.f) - mean, d1 = v[i][2] * (1.f / 72.f) - mean;
                    M2 += v[i][1] + v[i][3] + 72.f * (d0 * d0 + d1 * d1);
                }
                float rstd = rsqrtf(M2 * (1.f / 576.f) + 1e-5f);
                mu[tid] = f2v{mean * rstd, rstd};
            }
            csl[tid] = c0v; bbl[tid] = b0v;
            if (tid < 32) { csl[256 + tid] = c1v; bbl[256 + tid] = b1v; }
        }
#pragma unroll
        for (int m = 0; m < 4; m++)
#pragma unroll
            for (int n = 0; n < 9; n++) acc[m][n] = f4v{0, 0, 0, 0};
        const int dlt = (g & 1) ? 32 : 0;
        const int wb0 = 8192 + (8 * g + q) * 576 + wn * 9 * 32 + 8 * p;
        const int wbe = wb0 + ((wn & 1) ? -dlt : dlt);
        const int wbo = wb0 + ((wn & 1) ? dlt : -dlt);
        const int xoff = wm * 4096 + lane * 16;
        s8v xa[4], xb[4], wf[9];
        BAR;
        {
            const char* base = smem;
#pragma unroll
            for (int m = 0; m < 4; m++) xa[m] = *(const s8v*)(base + xoff + m * 1024);
#pragma unroll
            for (int n = 0; n < 9; n++) {
                const char* wb = base + ((n & 1) ? wbo : wbe) + n * 32;
                wf[n] = cat8(tr_read(wb), tr_read(wb + 4 * 576));
            }
        }
        WAIT_L(0);
        BAR;
#define K3_STEP(XC, XN) { \
            const char* base = smem + ((i + 1) % K3_NB) * K3_STAGE; \
            s4v wlo, whi; \
            _Pragma("unroll") for (int n = 0; n < 9; n++) { \
                const char* wbp = base + (((n - 1) & 1) ? wbo : wbe) + (n - 1) * 32; \
                acc[0][n] = MFMA16(wf[n], XC[0], acc[0][n]); SB0; \
                if (n > 0) { wlo = tr_read(wbp); SB0; } \
                acc[1][n] = MFMA16(wf[n], XC[1], acc[1][n]); SB0; \
                if (n > 0) { whi = tr_read(wbp + 4 * 576); wf[n - 1] = cat8(wlo, whi); SB0; } \
                acc[2][n] = MFMA16(wf[n], XC[2], acc[2][n]); SB0; \
                if (n < 4) { XN[n] = *(const s8v*)(base + xoff + n * 1024); SB0; } \
                acc[3][n] = MFMA16(wf[n], XC[3], acc[3][n]); SB0; \
            } \
            { const char* wbp = base + wbe + 8 * 32; wf[8] = cat8(tr_read(wbp), tr_read(wbp + 4 * 576)); } \
            BAR; }
#pragma unroll 1
        for (int i = 0; i < 18; i += 2) {
            K3_STEP(xa, xb);
            i++; K3_STEP(xb, xa); i--;
        }
    }
    __syncthreads();
    const int u = wave * 256 + blockIdx.x;
    if (u < 1152) {
        const int ctp = u / 144, kst = u - ctp * 144;
#pragma unroll
        for (int j = 0; j < 5; j++) {
            int c = j * 64 + lane; if (c > 287) c = 287;
            int row = c / 9, pc = c - row * 9;
            glds16s(w2, (unsigned)(((kst * 32 + row) * D_ + ctp * 72 + pc * 8) * 2), lbase + 98304 + wave * 5120 + j * 1024);
        }
    }
    f4v* ex = (f4v*)smem + (wave & 3) * (18 * 64) + lane;
    if (wave < 4) {
#pragma unroll
        for (int m = 0; m < 2; m++)
#pragma unroll
            for (int n = 0; n < 9; n++) ex[(m * 9 + n) * 64] = acc[m + 2][n];
    }
    __syncthreads();
    if (wave >= 4) {
#pragma unroll
        for (int m = 0; m < 2; m++)
#pragma unroll
            for (int n = 0; n < 9; n++) acc[m][n] = ex[(m * 9 + n) * 64];
    }
    {
        const int mb = (wave >> 2) * 2;
#pragma unroll
        for (int m = 0; m < 2; m++) {
            int rl = wm * 64 + (mb + m) * 16 + xr;
            f2v ms = mu[rl];
            char* ob = h1f + (size_t)((r0 + wm * 64 + (mb + m) * 16) >> 4) * (144 * 1024) + lane * 16;
            u2v pv[9];
#pragma unroll
            for (int n = 0; n < 9; n++) {
                f4v a = acc[m][n];
                int cl = wn * 144 + n * 16 + 4 * g;
                f4v cs = *(const f4v*)(csl + cl), bb = *(const f4v*)(bbl + cl);
                f2v t01 = f2v{bb[0], bb[1]} - f2v{cs[0], cs[1]} * ms[0];
                f2v t23 = f2v{bb[2], bb[3]} - f2v{cs[2], cs[3]} * ms[0];
                f2v v01 = gelu_tanh2(f2v{a[0], a[1]} * ms[1] + t01);
                f2v v23 = gelu_tanh2(f2v{a[2], a[3]} * ms[1] + t23);
                pv[n] = u2v{pack_bf2(v01[0], v01[1]), pack_bf2(v23[0], v23[1])};
            }
            if (m == 0) {
            } else {
            }
            char* oc = ob + (c0 >> 5) * 1024;
            if (wn == 0) {
#pragma unroll
                for (int j = 0; j < 4; j++) *(u4v*)(oc + j * 1024) = u4v{pv[2 * j][0], pv[2 * j][1], pv[2 * j + 1][0], pv[2 * j + 1][1]};
                *(u2v*)(oc + 4 * 1024) = pv[8];
            } else {
                *(u2v*)(oc + 4 * 1024 + 8) = pv[0];
#pragma unroll
                for (int j = 0; j < 4; j++) *(u4v*)(oc + (5 + j) * 1024) = u4v{pv[2 * j + 1][0], pv[2 * j + 1][1], pv[2 * j + 2][0], pv[2 * j + 2][1]};
            }
        }
    }
    WAIT_V(0);
    if (u < 1152) {
        const char* sb = smem + 98304 + wave * 5120 + (4 * g + q) * 144 + 8 * p;
        char* dst = w2f + (size_t)u * 5 * 1024 + lane * 16;
#pragma unroll
        for (int nt = 0; nt < 5; nt++)
            *(s8v*)(dst + nt * 1024) = cat8(tr_read(sb + nt * 32), tr_read(sb + 16 * 144 + nt * 32));
    }
}

constexpr int K4_LDS = 4 * 20 * 1024;
constexpr int K4_D = 3;

__global__ __launch_bounds__(512, 2) void k4_down(const char* __restrict__ h1f, const char* __restrict__ w2f,
                                                  const bf16_t* __restrict__ h, const bf16_t* __restrict__ b2,
                                                  bf16_t* __restrict__ out) {
    extern __shared__ __attribute__((aligned(16))) char smem[];
    const int tid = threadIdx.x, lane = tid & 63;
    const int wave = __builtin_amdgcn_readfirstlane(tid >> 6);
    const int xcd = blockIdx.x & 7, idx = blockIdx.x >> 3;
    const int rt = (xcd >> 1) * 8 + (idx & 7), ct = (xcd & 1) * 4 + (idx >> 3);
    const int g = lane >> 4;
    const char* pa = h1f + ((size_t)rt * 4 * 144 + wave) * 1024 + lane * 16;
    const char* pw = w2f + ((size_t)ct * 144 + wave) * 5 * 1024 + lane * 16;
    f4v acc[4][5];
#pragma unroll
    for (int m = 0; m < 4; m++)
#pragma unroll
        for (int n = 0; n < 5; n++) acc[m][n] = f4v{0, 0, 0, 0};
    s8v fa[K4_D][4], fw[K4_D][5];
#pragma unroll
    for (int j = 0; j < K4_D; j++) {
#pragma unroll
        for (int m = 0; m < 4; m++) fa[j][m] = *(const s8v*)(pa + m * (144 * 1024) + j * 8192);
#pragma unroll
        for (int n = 0; n < 5; n++) fw[j][n] = *(const s8v*)(pw + n * 1024 + j * 40960);
        SB0;
    }
#pragma unroll
    for (int j = 0; j < 18; j++) {
        const int sl = j % K4_D;
        SB0;
#pragma unroll
        for (int n = 0; n < 5; n++)
#pragma unroll
            for (int m = 0; m < 4; m++) acc[m][n] = MFMA16(fw[sl][n], fa[sl][m], acc[m][n]);
        SB0;
        if (j + K4_D < 18) {
#pragma unroll
            for (int m = 0; m < 4; m++) fa[sl][m] = *(const s8v*)(pa + m * (144 * 1024) + (j + K4_D) * 8192);
#pragma unroll
            for (int n = 0; n < 5; n++) fw[sl][n] = *(const s8v*)(pw + n * 1024 + (j + K4_D) * 40960);
        }
        SB0;
    }
    u2v hh[3], bb[3];
#pragma unroll
    for (int j = 0; j < 3; j++) {
        int t = wave + 8 * j; if (t > 19) t = 19;
        const int m = t / 5, n = t - m * 5;
        const int row = rt * 64 + m * 16 + (lane & 15);
        int cl = n * 16 + 4 * g; if (cl > 68) cl = 68;
        hh[j] = *(const u2v*)(h + (size_t)row * D_ + ct * 72 + cl);
        bb[j] = *(const u2v*)(b2 + ct * 72 + cl);
    }
    f4v* ex = (f4v*)smem + (wave & 3) * (20 * 64) + lane;
    if (wave >= 4) {
#pragma unroll
        for (int m = 0; m < 4; m++)
#pragma unroll
            for (int n = 0; n < 5; n++) ex[(m * 5 + n) * 64] = acc[m][n];
    }
    __syncthreads();
    if (wave < 4) {
#pragma unroll
        for (int m = 0; m < 4; m++)
#pragma unroll
            for (int n = 0; n < 5; n++) { f4v v = ex[(m * 5 + n) * 64]; ex[(m * 5 + n) * 64] = acc[m][n] + v; }
    }
    __syncthreads();
#pragma unroll
    for (int j = 0; j < 3; j++) {
        const int t = wave + 8 * j;
        if (t < 20) {
            const int m = t / 5, n = t - m * 5;
            const f4v* e = (const f4v*)smem + t * 64 + lane;
            f4v a = (e[0] + e[20 * 64]) + (e[40 * 64] + e[60 * 64]);
            const int row = rt * 64 + m * 16 + (lane & 15);
            const int cl = n * 16 + 4 * g;
            float v0 = bf_lo(hh[j][0]) + (a[0] + bf_lo(bb[j][0]));
            float v1 = bf_hi(hh[j][0]) + (a[1] + bf_hi(bb[j][0]));
            float v2 = bf_lo(hh[j][1]) + (a[2] + bf_lo(bb[j][1]));
            float v3 = bf_hi(hh[j][1]) + (a[3] + bf_hi(bb[j][1]));
            if (cl < 72) *(u2v*)(out + (size_t)row * D_ + ct * 72 + cl) = u2v{pack_bf2(v0, v1), pack_bf2(v2, v3)};
        }
    }
}

extern "C" void kernel_launch(void* const* d_in, const int* in_sizes, int n_in,
                              void* d_out, int out_size, void* d_ws, size_t ws_size,
                              hipStream_t stream) {
    const bf16_t* x = (const bf16_t*)d_in[0];
    const bf16_t* wqkv = (const bf16_t*)d_in[1];
    const bf16_t* wo = (const bf16_t*)d_in[2];
    const bf16_t* w1 = (const bf16_t*)d_in[4];
    const bf16_t* b1 = (const bf16_t*)d_in[5];
    const bf16_t* w2 = (const bf16_t*)d_in[6];
    const bf16_t* b2 = (const bf16_t*)d_in[7];
    bf16_t* out = (bf16_t*)d_out;
    char* ws = (char*)d_ws;
    bf16_t* o = (bf16_t*)(ws);
    bf16_t* h = (bf16_t*)(ws + (4 << 20));
    char* hT = ws + (12 << 20);
    f2v* stats = (f2v*)(ws + (8 << 20));
    float* cs1 = (float*)(ws + (9 << 20));
    char* h1 = (char*)(ws + (16 << 20));
    char* w2f = ws + (48 << 20);

    hipFuncSetAttribute((const void*)k1_qkv_attn, hipFuncAttributeMaxDynamicSharedMemorySize, K1_LDS);
    hipFuncSetAttribute((const void*)k2_wo, hipFuncAttributeMaxDynamicSharedMemorySize, K2_LDS);
    hipFuncSetAttribute((const void*)k3_up, hipFuncAttributeMaxDynamicSharedMemorySize, K3_LDS);
    hipFuncSetAttribute((const void*)k4_down, hipFuncAttributeMaxDynamicSharedMemorySize, K4_LDS);

    k1_qkv_attn<<<256, 512, K1_LDS, stream>>>(x, wqkv, w1, wo, o, cs1);
    k2_wo<<<256, 512, K2_LDS, stream>>>(o, wo, x, h, hT, stats);
    k3_up<<<256, 512, K3_LDS, stream>>>(hT, w1, b1, cs1, stats, w2, h1, w2f);
    k4_down<<<256, 512, K4_LDS, stream>>>(h1, w2f, h, b2, out);
}
```

```cpp
#include <hip/hip_runtime.h>
#include <hip/hip_bf16.h>

typedef __attribute__((ext_vector_type(8))) short s8v;
typedef __attribute__((ext_vector_type(4))) short s4v;
typedef __attribute__((ext_vector_type(4))) short v4i16_t;
typedef __attribute__((ext_vector_type(4))) float f4v;
typedef __attribute__((ext_vector_type(2))) float f2v;
typedef __attribute__((ext_vector_type(4))) unsigned u4v;
typedef __attribute__((ext_vector_type(2))) unsigned u2v;
typedef unsigned short bf16_t;

constexpr int S_ = 2048, D_ = 576, NQKV = 1728, F_ = 4608;

__device__ __forceinline__ unsigned lds_addr(const void* l) {
    return (unsigned)(size_t)(__attribute__((address_space(3))) const char*)l;
}
__device__ __forceinline__ void glds16(const void* g, unsigned dst) {
    unsigned keep;
    asm volatile("s_mov_b32 %0, m0\n\ts_mov_b32 m0, %2\n\ts_nop 0\n\tglobal_load_lds_dwordx4 %1, off\n\ts_mov_b32 m0, %0"
                 : "=&s"(keep) : "v"(g), "s"(dst) : "memory");
}
__device__ __forceinline__ void glds16s(const void* sbase, unsigned voff, unsigned dst) {
    unsigned keep;
    asm volatile("s_mov_b32 %0, m0\n\ts_mov_b32 m0, %3\n\ts_nop 0\n\tglobal_load_lds_dwordx4 %1, %2\n\ts_mov_b32 m0, %0"
                 : "=&s"(keep) : "v"(voff), "s"(sbase), "s"(dst) : "memory");
}
__device__ __forceinline__ s4v tr_read(const char* p) {
    return __builtin_bit_cast(s4v, __builtin_amdgcn_ds_read_tr16_b64_v4i16((__attribute__((address_space(3))) v4i16_t*)p));
}
__device__ __forceinline__ s8v cat8(s4v a, s4v b) {
    return __builtin_shufflevector(a, b, 0, 1, 2, 3, 4, 5, 6, 7);
}
__device__ __forceinline__ float bf_lo(unsigned u) { return __builtin_bit_cast(float, u << 16); }
__device__ __forceinline__ float bf_hi(unsigned u) { return __builtin_bit_cast(float, u & 0xffff0000u); }
__device__ __forceinline__ float bf2f(bf16_t v) { return __builtin_bit_cast(float, (unsigned)v << 16); }
__device__ __forceinline__ unsigned pack_bf2(float a, float b) {
    unsigned r;
    asm("v_cvt_pk_bf16_f32 %0, %1, %2" : "=v"(r) : "v"(a), "v"(b));
    return r;
}
#define MFMA16(a, b, c) __builtin_amdgcn_mfma_f32_16x16x32_bf16(a, b, c, 0, 0, 0)
#define WAIT_V(n) asm volatile("s_waitcnt vmcnt(" #n ")" ::: "memory")
#define WAIT_L(n) asm volatile("s_waitcnt lgkmcnt(" #n ")" ::: "memory")

#define SB0 __builtin_amdgcn_sched_barrier(0)
#define BAR __builtin_amdgcn_s_barrier()
constexpr int K1_XN_BYTES = 9 * 80 * 128;
constexpr int K1_STAGE = 14336;
constexpr int K1_NB = 4;
constexpr int K1_DUMMY = K1_XN_BYTES + K1_NB * K1_STAGE;
constexpr int K1_RED = K1_DUMMY + 5120;
constexpr int K1_LDS = K1_RED + 2048;

__global__ __launch_bounds__(512, 2) void k1_qkv_attn(const bf16_t* __restrict__ x, const bf16_t* __restrict__ wqkv,
                                                      const bf16_t* __restrict__ w1, const bf16_t* __restrict__ wo,
                                                      char* __restrict__ of, char* __restrict__ wof,
                                                      float* __restrict__ cs1) {
    extern __shared__ __attribute__((aligned(16))) char smem[];
    const int tid = threadIdx.x, lane = tid & 63;
    const int wave = __builtin_amdgcn_readfirstlane(tid >> 6);
    const int xcd = blockIdx.x & 7, idx = blockIdx.x >> 3;
    const int hg = (xcd & 1) * 4 + (idx >> 3), rt = (xcd >> 1) * 8 + (idx & 7);
    const int s0 = rt * 64;
    const unsigned lring = lds_addr(smem) + K1_XN_BYTES;
    const int g = lane >> 4, q = (lane & 15) >> 2, p = lane & 3;

    const int lw = wave - 4;
    unsigned bsrc[4];
    auto issue = [&](int st) {
        int sc = st > 17 ? 17 : st;
        unsigned lb = lring + (st % K1_NB) * K1_STAGE;
        const char* gb = (const char*)wqkv + (size_t)sc * 32 * NQKV * 2;
#pragma unroll
        for (int i = 0; i < 4; i++) glds16s(gb, bsrc[i], lb + (lw * 4 + i > 13 ? 13 : lw * 4 + i) * 1024);
    };
    u4v cw[6];
    const int cu = blockIdx.x < 216 ? blockIdx.x : blockIdx.x - 216;
    const int cchunk = cu % 72, rthird = cu / 72;
    const unsigned dz = lds_addr(smem) + K1_DUMMY + (wave & 3) * 1024;
    if (wave >= 4) {
#pragma unroll
        for (int i = 0; i < 4; i++) {
            int bi = lw * 4 + i; if (bi > 13) bi = 13;
            int off = bi * 1024 + lane * 16;
            int k = off / 448, r = off - k * 448;
            int nt = (r >> 5) ^ ((k >> 3) & 1);
            int pc = nt * 2 + ((r >> 4) & 1); if (pc > 26) pc = 26;
            int sec = pc / 9, wi = pc - sec * 9;
            bsrc[i] = (unsigned)((k * NQKV + sec * 576 + hg * 72 + wi * 8) * 2);
        }
#pragma unroll
        for (int st = 0; st < K1_NB - 1; st++) issue(st);
#pragma unroll
        for (int j = 0; j < 8; j++) {
            int c = (lw * 8 + j) * 64 + lane; if (c > 1943) c = 1943;
            int seg = c / 9, pc = c - seg * 9;
            int sec = seg / 72, rr = seg - sec * 72;
            glds16s(wqkv, (unsigned)((((idx & 7) * 72 + rr) * NQKV + sec * 576 + hg * 72 + pc * 8) * 2), dz);
        }
    } else {
        u4v xv[3][9];
#pragma unroll
        for (int j = 0; j < 3; j++) {
            if (j < 2 || wave < 2) {
                int r = (4 * j + wave) * 8 + (lane >> 3);
                int s = s0 - 16 + r; if (s < 0) s = 0;
                const u4v* pp = (const u4v*)(x + (size_t)s * D_) + (lane & 7);
#pragma unroll
                for (int i = 0; i < 9; i++) xv[j][i] = pp[i * 8];
            }
            if (j == 0) {
            }
        }
#pragma unroll
        for (int j = 0; j < 3; j++) {
            if (j < 2 || wave < 2) {
                const int r = (4 * j + wave) * 8 + (lane >> 3);
                f2v v[36];
                f2v sm = f2v{0.f, 0.f};
#pragma unroll
                for (int i = 0; i < 9; i++)
#pragma unroll
                    for (int t = 0; t < 4; t++) { v[i * 4 + t] = f2v{bf_lo(xv[j][i][t]), bf_hi(xv[j][i][t])}; sm += v[i * 4 + t]; }
                float sum = sm[0] + sm[1];
                sum += __shfl_xor(sum, 1); sum += __shfl_xor(sum, 2); sum += __shfl_xor(sum, 4);
                const float mean = sum * (1.f / 576.f);
                f2v sq = f2v{0.f, 0.f};
#pragma unroll
                for (int i = 0; i < 36; i++) { v[i] = v[i] - mean; sq += v[i] * v[i]; }
                float sqs = sq[0] + sq[1];
                sqs += __shfl_xor(sqs, 1); sqs += __shfl_xor(sqs, 2); sqs += __shfl_xor(sqs, 4);
                const float rstd = rsqrtf(sqs * (1.f / 576.f) + 1e-5f);
#pragma unroll
                for (int i = 0; i < 9; i++) {
                    u4v ov;
#pragma unroll
                    for (int t = 0; t < 4; t++) { f2v w = v[i * 4 + t] * rstd; ov[t] = pack_bf2(w[0], w[1]); }
                    *(u4v*)(smem + i * (80 * 128) + r * 128 + (((lane & 7) ^ (r & 7)) << 4)) = ov;
                }
                if (j == 0) {
                }
                if (j == 1) {
                }
            }
        }
    }

    f4v acc[5][4];
    if (wave >= 4) {
        WAIT_V(16);
        __syncthreads();
        WAIT_V(12); BAR;
        issue(3); WAIT_V(12); BAR;
        for (int i = 1; i < 15; i++) {
            issue(i + K1_NB - 1);
            WAIT_V(4);
            BAR;
        }
        WAIT_V(0); BAR;
        {
            const u4v* pw = (const u4v*)(w1 + (size_t)(rthird * 192 + lw * 48 + (lane >> 3)) * F_ + cchunk * 64) + (lane & 7);
#pragma unroll
            for (int j = 0; j < 6; j++) cw[j] = pw[(size_t)j * 8 * F_ / 8];
            if (wave == 4 && blockIdx.x < 144) {
                const int ctp = blockIdx.x / 18, kst = blockIdx.x - ctp * 18;
#pragma unroll
                for (int j = 0; j < 5; j++) {
                    int c = j * 64 + lane; if (c > 287) c = 287;
                    int row = c / 9, pc = c - row * 9;
                    glds16s(wo, (unsigned)(((kst * 32 + row) * D_ + ctp * 72 + pc * 8) * 2), lds_addr(smem) + K1_DUMMY + j * 1024);
                }
            }
        }
        BAR; BAR;
    } else {
        const int nt0 = wave == 0 ? 0 : 1 + 3 * wave;
        const int nn = (wave == 0 || wave == 3) ? 4 : 3;
#pragma unroll
        for (int m = 0; m < 5; m++)
#pragma unroll
            for (int n = 0; n < 4; n++) acc[m][n] = f4v{0, 0, 0, 0};
        const int xr = lane & 15;
        int woff[4];
#pragma unroll
        for (int n = 0; n < 4; n++) woff[n] = K1_XN_BYTES + (8 * g + q) * 448 + (((nt0 + n) ^ (g & 1)) << 5) + 8 * p;
        s8v xa[5], xb[5], wf[4];
        auto rdx = [&](s8v* X, int st) {
            const char* xbp = smem + (st >> 1) * (80 * 128) + xr * 128 + ((((st & 1) * 4 + g) ^ (xr & 7)) << 4);
#pragma unroll
            for (int m = 0; m < 5; m++) X[m] = *(const s8v*)(xbp + m * 2048);
        };
        __syncthreads();
        rdx(xa, 0);
#pragma unroll
        for (int n = 0; n < 4; n++) wf[n] = cat8(tr_read(smem + woff[n]), tr_read(smem + woff[n] + 4 * 448));
        WAIT_L(0);
        BAR;
#define K1_STEP(XC, XN) { \
            const char* base = smem + ((i + 1) % K1_NB) * K1_STAGE; \
            rdx(XN, i + 1 > 17 ? 17 : i + 1); \
            SB0; \
            _Pragma("unroll") for (int n = 0; n < 4; n++) { \
                if (n < 3 || nn == 4) { \
                    if (wave != 0) acc[0][n] = MFMA16(XC[0], wf[n], acc[0][n]); \
                    _Pragma("unroll") for (int m = 1; m < 5; m++) acc[m][n] = MFMA16(XC[m], wf[n], acc[m][n]); \
                    SB0; \
                    wf[n] = cat8(tr_read(base + woff[n]), tr_read(base + woff[n] + 4 * 448)); \
                    SB0; \
                } \
            } \
            BAR; }
#pragma unroll 1
        for (int i = 0; i < 18; i += 2) {
            K1_STEP(xa, xb);
            i++; K1_STEP(xb, xa); i--;
        }
    }
    __syncthreads();
    constexpr int QS = 84;
    float* qkv = (float*)smem;
    if (wave < 4) {
        const int nt0 = wave == 0 ? 0 : 1 + 3 * wave;
        const int nn = (wave == 0 || wave == 3) ? 4 : 3;
#pragma unroll
        for (int m = 0; m < 5; m++)
#pragma unroll
            for (int n = 0; n < 4; n++) {
                int col = (nt0 + n) * 16 + (lane & 15);
                if (n < nn && col < 216) *(f4v*)(qkv + col * QS + m * 16 + 4 * g) = acc[m][n];
            }
    }
    __syncthreads();
    {
        const int head = wave >> 1;
        const int rl = (wave & 1) * 32 + (lane & 31);
        const int hw = lane >> 5;
        const int r = 16 + rl;
        const int s = s0 + rl;
        const float* qp = qkv + (head * 18) * QS + r;
        const float* kp = qkv + (72 + head * 18) * QS + r - 8 * hw;
        const float* vp = qkv + (144 + head * 18) * QS + r - 8 * hw;
        float qv[18];
#pragma unroll
        for (int d = 0; d < 18; d++) qv[d] = qp[d * QS];
        float lg[4];
        float mx = -3.0e38f;
#pragma unroll
        for (int w = 0; w < 4; w++) {
            float a = 0.f;
#pragma unroll
            for (int d = 0; d < 18; d++) a += qv[d] * kp[d * QS - 2 * w];
            a *= 0.23570226039551584f;
            lg[w] = (s - 2 * (w + 4 * hw) >= 0) ? a : -1e9f;
            mx = fmaxf(mx, lg[w]);
        }
        mx = fmaxf(mx, __shfl_xor(mx, 32));
        float den = 0.f;
#pragma unroll
        for (int w = 0; w < 4; w++) { lg[w] = __expf(lg[w] - mx); den += lg[w]; }
        den += __shfl_xor(den, 32);
        float inv = 1.f / den;
        float ov[18];
#pragma unroll
        for (int d = 0; d < 18; d++) ov[d] = 0.f;
#pragma unroll
        for (int w = 0; w < 4; w++) {
            float pw = lg[w] * inv;
#pragma unroll
            for (int d = 0; d < 18; d++) ov[d] += pw * vp[d * QS - 2 * w];
        }
#pragma unroll
        for (int d = 0; d < 18; d++) ov[d] += __shfl_xor(ov[d], 32);
        char* ob = of + (size_t)(s >> 4) * (18 * 1024) + (s & 15) * 16;
        const int cb = hg * 72 + head * 18;
#pragma unroll
        for (int d = 0; d < 5; d++) {
            const int dd = hw == 0 ? d : (d + 5 > 8 ? 8 : d + 5);
            const int col = cb + 2 * dd;
            const unsigned pv = hw == 0 ? pack_bf2(ov[2 * d], ov[2 * d + 1]) : pack_bf2(ov[2 * (d + 5 > 8 ? 8 : d + 5)], ov[2 * (d + 5 > 8 ? 8 : d + 5) + 1]);
            if (hw == 0 || d < 4) *(unsigned*)(ob + (col >> 5) * 1024 + ((col & 31) >> 3) * 256 + (col & 7) * 2) = pv;
        }
    }
    if (wave >= 4) {
        float a[8];
#pragma unroll
        for (int j = 0; j < 8; j++) a[j] = 0.f;
#pragma unroll
        for (int i = 0; i < 6; i++)
#pragma unroll
            for (int j = 0; j < 4; j++) { a[2 * j] += bf_lo(cw[i][j]); a[2 * j + 1] += bf_hi(cw[i][j]); }
#pragma unroll
        for (int j = 0; j < 8; j++) {
            float t = a[j];
            t += __shfl_xor(t, 8); t += __shfl_xor(t, 16); t += __shfl_xor(t, 32);
            a[j] = t;
        }
        if (lane < 8) {
            float* rq = (float*)(smem + K1_RED) + lw * 64 + lane * 8;
            *(f4v*)rq = f4v{a[0], a[1], a[2], a[3]};
            *(f4v*)(rq + 4) = f4v{a[4], a[5], a[6], a[7]};
        }
        WAIT_V(0);
        if (wave == 4 && blockIdx.x < 144) {
            const char* sb = smem + K1_DUMMY + (8 * g + q) * 144 + 8 * p;
            char* dst = wof + (size_t)blockIdx.x * 5 * 1024 + lane * 16;
#pragma unroll
            for (int nt = 0; nt < 5; nt++)
                *(s8v*)(dst + nt * 1024) = cat8(tr_read(sb + nt * 32), tr_read(sb + 4 * 144 + nt * 32));
        }
    }
    __syncthreads();
    if (wave == 7 && blockIdx.x < 216) {
        const float* rq = (const float*)(smem + K1_RED) + lane;
        cs1[rthird * F_ + cchunk * 64 + lane] = (rq[0] + rq[64]) + (rq[128] + rq[192]);
    }
}

constexpr int K2_LDS = 4 * 20 * 1024;

__global__ __launch_bounds__(512, 2) void k2_wo(const char* __restrict__ of, const char* __restrict__ wof,
                                                const bf16_t* __restrict__ x, bf16_t* __restrict__ h,
                                                char* __restrict__ hT, f2v* __restrict__ stats) {
    extern __shared__ __attribute__((aligned(16))) char smem[];
    const int tid = threadIdx.x, lane = tid & 63;
    const int wave = __builtin_amdgcn_readfirstlane(tid >> 6);
    const int rt = (blockIdx.x & 7) * 4 + ((blockIdx.x >> 3) & 3), ct = blockIdx.x >> 5;
    const int r0 = rt * 64, c0 = ct * 72;
    const int g = lane >> 4;
    const char* pa = of + ((size_t)rt * 4 * 18 + wave) * 1024 + lane * 16;
    const char* pw = wof + ((size_t)ct * 18 + wave) * 5 * 1024 + lane * 16;
    s8v fa[3][4], fw[3][5];
#pragma unroll
    for (int j = 0; j < 3; j++) {
        if (j < 2 || wave < 2) {
#pragma unroll
            for (int m = 0; m < 4; m++) fa[j][m] = *(const s8v*)(pa + m * (18 * 1024) + j * 8192);
#pragma unroll
            for (int n = 0; n < 5; n++) fw[j][n] = *(const s8v*)(pw + n * 1024 + j * 40960);
        }
    }
    const int row = r0 + (wave & 3) * 16 + (lane & 15);
    u2v xx[5];
#pragma unroll
    for (int n = 0; n < 5; n++) {
        int col = n * 16 + 4 * g; if (col > 68) col = 68;
        xx[n] = *(const u2v*)(x + (size_t)row * D_ + c0 + col);
    }
    f4v acc[4][5];
#pragma unroll
    for (int m = 0; m < 4; m++)
#pragma unroll
        for (int n = 0; n < 5; n++) acc[m][n] = f4v{0, 0, 0, 0};
#pragma unroll
    for (int j = 0; j < 3; j++) {
        if (j < 2 || wave < 2) {
#pragma unroll
            for (int n = 0; n < 5; n++)
#pragma unroll
                for (int m = 0; m < 4; m++) acc[m][n] = MFMA16(fw[j][n], fa[j][m], acc[m][n]);
        }
    }
    f4v* ex = (f4v*)smem + (wave & 3) * (20 * 64) + lane;
    if (wave >= 4) {
#pragma unroll
        for (int m = 0; m < 4; m++)
#pragma unroll
            for (int n = 0; n < 5; n++) ex[(m * 5 + n) * 64] = acc[m][n];
    }
    __syncthreads();
    if (wave < 4) {
#pragma unroll
        for (int m = 0; m < 4; m++)
#pragma unroll
            for (int n = 0; n < 5; n++) { f4v v = ex[(m * 5 + n) * 64]; ex[(m * 5 + n) * 64] = acc[m][n] + v; }
    }
    __syncthreads();
    if (wave >= 4) {
        return;
    }
    float hv[5][4];
    float sum = 0.f;
#pragma unroll
    for (int n = 0; n < 5; n++) {
        const f4v* e = (const f4v*)smem + (wave * 5 + n) * 64 + lane;
        f4v a = (e[0] + e[20 * 64]) + (e[40 * 64] + e[60 * 64]);
        int col = n * 16 + 4 * g;
        if (col < 72) {
            unsigned p0 = pack_bf2(bf_lo(xx[n][0]) + a[0], bf_hi(xx[n][0]) + a[1]);
            unsigned p1 = pack_bf2(bf_lo(xx[n][1]) + a[2], bf_hi(xx[n][1]) + a[3]);
            *(u2v*)(h + (size_t)row * D_ + c0 + col) = u2v{p0, p1};
            {
                int cc = c0 + col;
                *(u2v*)(hT + (size_t)((row >> 4) * 18 + (cc >> 5)) * 1024 + (((cc & 31) >> 3) * 16 + (row & 15)) * 16 + (cc & 7) * 2) = u2v{p0, p1};
            }
            hv[n][0] = bf_lo(p0); hv[n][1] = bf_hi(p0); hv[n][2] = bf_lo(p1); hv[n][3] = bf_hi(p1);
            sum += hv[n][0] + hv[n][1] + hv[n][2] + hv[n][3];
        } else {
            hv[n][0] = hv[n][1] = hv[n][2] = hv[n][3] = 0.f;
        }
    }
    sum += __shfl_xor(sum, 16); sum += __shfl_xor(sum, 32);
    float mean = sum * (1.f / 72.f);
    float m2 = 0.f;
#pragma unroll
    for (int n = 0; n < 5; n++) {
        int col = n * 16 + 4 * g;
        if (col < 72) {
#pragma unroll
            for (int j = 0; j < 4; j++) { float d = hv[n][j] - mean; m2 += d * d; }
        }
    }
    m2 += __shfl_xor(m2, 16); m2 += __shfl_xor(m2, 32);
    if (g == 0) stats[(size_t)row * 8 + ct] = f2v{sum, m2};
}

constexpr int K3_STAGE = 8192 + 18432;
constexpr int K3_NB = 5;
constexpr int K3_PAR = K3_NB * K3_STAGE;
constexpr int K3_LDS = K3_PAR + 1024 + 2304;

__device__ __forceinline__ float gelu_tanh(float v) {
    float t = v * v;
    float z = v * (-2.302208198f * 0.044715f * t - 2.302208198f);
    float e = __builtin_amdgcn_exp2f(z);
    return v * __builtin_amdgcn_rcpf(1.f + e);
}

__device__ __forceinline__ f2v gelu_tanh2(f2v v) {
    f2v t = v * v;
    f2v z = v * (t * (-2.302208198f * 0.044715f) + (-2.302208198f));
    f2v d = f2v{__builtin_amdgcn_exp2f(z[0]), __builtin_amdgcn_exp2f(z[1])} + 1.f;
    return v * f2v{__builtin_amdgcn_rcpf(d[0]), __builtin_amdgcn_rcpf(d[1])};
}

__global__ __launch_bounds__(512, 2) void k3_up(const char* __restrict__ hT, const bf16_t* __restrict__ w1,
                                                const bf16_t* __restrict__ b1, const float* __restrict__ cs1,
                                                const f2v* __restrict__ stats, const bf16_t* __restrict__ w2,
                                                char* __restrict__ h1f, char* __restrict__ w2f) {
    extern __shared__ __attribute__((aligned(16))) char smem[];
    const int tid = threadIdx.x, lane = tid & 63;
    const int wave = __builtin_amdgcn_readfirstlane(tid >> 6);
    const int xcd = blockIdx.x & 7, idx = blockIdx.x >> 3;
    const int rt = (xcd & 1) * 8 + (idx & 7), ct = (xcd >> 1) * 4 + (idx >> 3);
    const int r0 = rt * 128, c0 = ct * 288;
    const unsigned lbase = lds_addr(smem);
    const int g = lane >> 4, q = (lane & 15) >> 2, p = lane & 3;
    const int xr = lane & 15;
    f2v* mu = (f2v*)(smem + K3_PAR);
    float* csl = (float*)(smem + K3_PAR + 1024);
    float* bbl = csl + 288;
    f4v acc[4][9];
    const int wm = (wave >> 1) & 1, wn = wave & 1;
    if (wave >= 4) {
        const int lw = wave - 4;
        unsigned bsrc[5];
        const unsigned avo = lane * 16;
        const char* ag = hT + (size_t)(rt * 8 + lw * 2) * 18 * 1024;
#pragma unroll
        for (int i = 0; i < 5; i++) {
            int bi = lw * 5 + i; if (bi > 17) bi = 17;
            int off = bi * 1024 + lane * 16;
            int k = off / 576, r = off - k * 576;
            int nt = (r >> 5) ^ ((k >> 3) & 1);
            bsrc[i] = (unsigned)((k * F_ + c0 + nt * 16 + ((r >> 4) & 1) * 8) * 2);
        }
        auto issue = [&](int st) {
            int sc = st > 17 ? 17 : st;
            unsigned lb = lbase + (st % K3_NB) * K3_STAGE;
            const char* ga = ag + sc * 1024;
            const char* gb = (const char*)w1 + (size_t)sc * 32 * F_ * 2;
            glds16s(ga, avo, lb + (lw * 2) * 1024);
            glds16s(ga + 18 * 1024, avo, lb + (lw * 2 + 1) * 1024);
#pragma unroll
            for (int i = 0; i < 5; i++) glds16s(gb, bsrc[i], lb + 8192 + (lw * 5 + i > 17 ? 17 : lw * 5 + i) * 1024);
        };
#pragma unroll
        for (int st = 0; st < K3_NB - 1; st++) issue(st);
        WAIT_V(21);
        BAR;
        for (int i = -1; i < 18; i++) {
            if (i >= 0) issue(i + K3_NB - 1);
            WAIT_V(14);
            BAR;
        }
        WAIT_V(0);
    } else {
        {
            float c0v = (cs1[c0 + tid] + cs1[F_ + c0 + tid]) + cs1[2 * F_ + c0 + tid], b0v = bf2f(b1[c0 + tid]);
            float c1v = 0.f, b1v = 0.f;
            if (tid < 32) { c1v = (cs1[c0 + 256 + tid] + cs1[F_ + c0 + 256 + tid]) + cs1[2 * F_ + c0 + 256 + tid]; b1v = bf2f(b1[c0 + 256 + tid]); }
            if (tid < 128) {
                const f4v* sp = (const f4v*)(stats + (size_t)(r0 + tid) * 8);
                f4v v[4];
#pragma unroll
                for (int i = 0; i < 4; i++) v[i] = sp[i];
                float tot = 0.f;
#pragma unroll
                for (int i = 0; i < 4; i++) tot += v[i][0] + v[i][2];
                float mean = tot * (1.f / 576.f);
                float M2 = 0.f;
#pragma unroll
                for (int i = 0; i < 4; i++) {
                    float d0 = v[i][0] * (1.f / 72.f) - mean, d1 = v[i][2] * (1.f / 72.f) - mean;
                    M2 += v[i][1] + v[i][3] + 72.f * (d0 * d0 + d1 * d1);
                }
                float rstd = rsqrtf(M2 * (1.f / 576.f) + 1e-5f);
                mu[tid] = f2v{mean * rstd, rstd};
            }
            csl[tid] = c0v; bbl[tid] = b0v;
            if (tid < 32) { csl[256 + tid] = c1v; bbl[256 + tid] = b1v; }
        }
#pragma unroll
        for (int m = 0; m < 4; m++)
#pragma unroll
            for (int n = 0; n < 9; n++) acc[m][n] = f4v{0, 0, 0, 0};
        const int dlt = (g & 1) ? 32 : 0;
        const int wb0 = 8192 + (8 * g + q) * 576 + wn * 9 * 32 + 8 * p;
        const int wbe = wb0 + ((wn & 1) ? -dlt : dlt);
        const int wbo = wb0 + ((wn & 1) ? dlt : -dlt);
        const int xoff = wm * 4096 + lane * 16;
        s8v xa[4], xb[4], wf[9];
        BAR;
        {
            const char* base = smem;
#pragma unroll
            for (int m = 0; m < 4; m++) xa[m] = *(const s8v*)(base + xoff + m * 1024);
#pragma unroll
            for (int n = 0; n < 9; n++) {
                const char* wb = base + ((n & 1) ? wbo : wbe) + n * 32;
                wf[n] = cat8(tr_read(wb), tr_read(wb + 4 * 576));
            }
        }
        WAIT_L(0);
        BAR;
#define K3_STEP(XC, XN) { \
            const char* base = smem + ((i + 1) % K3_NB) * K3_STAGE; \
            s4v wlo, whi; \
            _Pragma("unroll") for (int n = 0; n < 9; n++) { \
                const char* wbp = base + (((n - 1) & 1) ? wbo : wbe) + (n - 1) * 32; \
                acc[0][n] = MFMA16(wf[n], XC[0], acc[0][n]); SB0; \
                if (n > 0) { wlo = tr_read(wbp); SB0; } \
                acc[1][n] = MFMA16(wf[n], XC[1], acc[1][n]); SB0; \
                if (n > 0) { whi = tr_read(wbp + 4 * 576); wf[n - 1] = cat8(wlo, whi); SB0; } \
                acc[2][n] = MFMA16(wf[n], XC[2], acc[2][n]); SB0; \
                if (n < 4) { XN[n] = *(const s8v*)(base + xoff + n * 1024); SB0; } \
                acc[3][n] = MFMA16(wf[n], XC[3], acc[3][n]); SB0; \
            } \
            { const char* wbp = base + wbe + 8 * 32; wf[8] = cat8(tr_read(wbp), tr_read(wbp + 4 * 576)); } \
            BAR; }
#pragma unroll 1
        for (int i = 0; i < 18; i += 2) {
            K3_STEP(xa, xb);
            i++; K3_STEP(xb, xa); i--;
        }
    }
    __syncthreads();
    const int u = wave * 256 + blockIdx.x;
    if (u < 1152) {
        const int ctp = u / 144, kst = u - ctp * 144;
#pragma unroll
        for (int j = 0; j < 5; j++) {
            int c = j * 64 + lane; if (c > 287) c = 287;
            int row = c / 9, pc = c - row * 9;
            glds16s(w2, (unsigned)(((kst * 32 + row) * D_ + ctp * 72 + pc * 8) * 2), lbase + 98304 + wave * 5120 + j * 1024);
        }
    }
    f4v* ex = (f4v*)smem + (wave & 3) * (18 * 64) + lane;
    if (wave < 4) {
#pragma unroll
        for (int m = 0; m < 2; m++)
#pragma unroll
            for (int n = 0; n < 9; n++) ex[(m * 9 + n) * 64] = acc[m + 2][n];
    }
    __syncthreads();
    if (wave >= 4) {
#pragma unroll
        for (int m = 0; m < 2; m++)
#pragma unroll
            for (int n = 0; n < 9; n++) acc[m][n] = ex[(m * 9 + n) * 64];
    }
    {
        const int mb = (wave >> 2) * 2;
#pragma unroll
        for (int m = 0; m < 2; m++) {
            int rl = wm * 64 + (mb + m) * 16 + xr;
            f2v ms = mu[rl];
            char* ob = h1f + (size_t)((r0 + wm * 64 + (mb + m) * 16) >> 4) * (144 * 1024) + lane * 16;
            u2v pv[9];
#pragma unroll
            for (int n = 0; n < 9; n++) {
                f4v a = acc[m][n];
                int cl = wn * 144 + n * 16 + 4 * g;
                f4v cs = *(const f4v*)(csl + cl), bb = *(const f4v*)(bbl + cl);
                f2v t01 = f2v{bb[0], bb[1]} - f2v{cs[0], cs[1]} * ms[0];
                f2v t23 = f2v{bb[2], bb[3]} - f2v{cs[2], cs[3]} * ms[0];
                f2v v01 = gelu_tanh2(f2v{a[0], a[1]} * ms[1] + t01);
                f2v v23 = gelu_tanh2(f2v{a[2], a[3]} * ms[1] + t23);
                pv[n] = u2v{pack_bf2(v01[0], v01[1]), pack_bf2(v23[0], v23[1])};
            }
            if (m == 0) {
            } else {
            }
            char* oc = ob + (c0 >> 5) * 1024;
            if (wn == 0) {
#pragma unroll
                for (int j = 0; j < 4; j++) *(u4v*)(oc + j * 1024) = u4v{pv[2 * j][0], pv[2 * j][1], pv[2 * j + 1][0], pv[2 * j + 1][1]};
                *(u2v*)(oc + 4 * 1024) = pv[8];
            } else {
                *(u2v*)(oc + 4 * 1024 + 8) = pv[0];
#pragma unroll
                for (int j = 0; j < 4; j++) *(u4v*)(oc + (5 + j) * 1024) = u4v{pv[2 * j + 1][0], pv[2 * j + 1][1], pv[2 * j + 2][0], pv[2 * j + 2][1]};
            }
        }
    }
    WAIT_V(0);
    if (u < 1152) {
        const char* sb = smem + 98304 + wave * 5120 + (4 * g + q) * 144 + 8 * p;
        char* dst = w2f + (size_t)u * 5 * 1024 + lane * 16;
#pragma unroll
        for (int nt = 0; nt < 5; nt++)
            *(s8v*)(dst + nt * 1024) = cat8(tr_read(sb + nt * 32), tr_read(sb + 16 * 144 + nt * 32));
    }
}

constexpr int K4_LDS = 4 * 20 * 1024;
constexpr int K4_D = 3;

__global__ __launch_bounds__(512, 2) void k4_down(const char* __restrict__ h1f, const char* __restrict__ w2f,
                                                  const bf16_t* __restrict__ h, const bf16_t* __restrict__ b2,
                                                  bf16_t* __restrict__ out) {
    extern __shared__ __attribute__((aligned(16))) char smem[];
    const int tid = threadIdx.x, lane = tid & 63;
    const int wave = __builtin_amdgcn_readfirstlane(tid >> 6);
    const int xcd = blockIdx.x & 7, idx = blockIdx.x >> 3;
    const int rt = (xcd >> 1) * 8 + (idx & 7), ct = (xcd & 1) * 4 + (idx >> 3);
    const int g = lane >> 4;
    const char* pa = h1f + ((size_t)rt * 4 * 144 + wave) * 1024 + lane * 16;
    const char* pw = w2f + ((size_t)ct * 144 + wave) * 5 * 1024 + lane * 16;
    f4v acc[4][5];
#pragma unroll
    for (int m = 0; m < 4; m++)
#pragma unroll
        for (int n = 0; n < 5; n++) acc[m][n] = f4v{0, 0, 0, 0};
    s8v fa[K4_D][4], fw[K4_D][5];
#pragma unroll
    for (int j = 0; j < K4_D; j++) {
#pragma unroll
        for (int m = 0; m < 4; m++) fa[j][m] = *(const s8v*)(pa + m * (144 * 1024) + j * 8192);
#pragma unroll
        for (int n = 0; n < 5; n++) fw[j][n] = *(const s8v*)(pw + n * 1024 + j * 40960);
        SB0;
    }
#pragma unroll
    for (int j = 0; j < 18; j++) {
        const int sl = j % K4_D;
        SB0;
#pragma unroll
        for (int n = 0; n < 5; n++)
#pragma unroll
            for (int m = 0; m < 4; m++) acc[m][n] = MFMA16(fw[sl][n], fa[sl][m], acc[m][n]);
        SB0;
        if (j + K4_D < 18) {
#pragma unroll
            for (int m = 0; m < 4; m++) fa[sl][m] = *(const s8v*)(pa + m * (144 * 1024) + (j + K4_D) * 8192);
#pragma unroll
            for (int n = 0; n < 5; n++) fw[sl][n] = *(const s8v*)(pw + n * 1024 + (j + K4_D) * 40960);
        }
        SB0;
    }
    u2v hh[3], bb[3];
#pragma unroll
    for (int j = 0; j < 3; j++) {
        int t = wave + 8 * j; if (t > 19) t = 19;
        const int m = t / 5, n = t - m * 5;
        const int row = rt * 64 + m * 16 + (lane & 15);
        int cl = n * 16 + 4 * g; if (cl > 68) cl = 68;
        hh[j] = *(const u2v*)(h + (size_t)row * D_ + ct * 72 + cl);
        bb[j] = *(const u2v*)(b2 + ct * 72 + cl);
    }
    f4v* ex = (f4v*)smem + (wave & 3) * (20 * 64) + lane;
    if (wave >= 4) {
#pragma unroll
        for (int m = 0; m < 4; m++)
#pragma unroll
            for (int n = 0; n < 5; n++) ex[(m * 5 + n) * 64] = acc[m][n];
    }
    __syncthreads();
    if (wave < 4) {
#pragma unroll
        for (int m = 0; m < 4; m++)
#pragma unroll
            for (int n = 0; n < 5; n++) { f4v v = ex[(m * 5 + n) * 64]; ex[(m * 5 + n) * 64] = acc[m][n] + v; }
    }
    __syncthreads();
#pragma unroll
    for (int j = 0; j < 3; j++) {
        const int t = wave + 8 * j;
        if (t < 20) {
            const int m = t / 5, n = t - m * 5;
            const f4v* e = (const f4v*)smem + t * 64 + lane;
            f4v a = (e[0] + e[20 * 64]) + (e[40 * 64] + e[60 * 64]);
            const int row = rt * 64 + m * 16 + (lane & 15);
            const int cl = n * 16 + 4 * g;
            float v0 = bf_lo(hh[j][0]) + (a[0] + bf_lo(bb[j][0]));
            float v1 = bf_hi(hh[j][0]) + (a[1] + bf_hi(bb[j][0]));
            float v2 = bf_lo(hh[j][1]) + (a[2] + bf_lo(bb[j][1]));
            float v3 = bf_hi(hh[j][1]) + (a[3] + bf_hi(bb[j][1]));
            if (cl < 72) *(u2v*)(out + (size_t)row * D_ + ct * 72 + cl) = u2v{pack_bf2(v0, v1), pack_bf2(v2, v3)};
        }
    }
}

extern "C" void kernel_launch(void* const* d_in, const int* in_sizes, int n_in,
                              void* d_out, int out_size, void* d_ws, size_t ws_size,
                              hipStream_t stream) {
    const bf16_t* x = (const bf16_t*)d_in[0];
    const bf16_t* wqkv = (const bf16_t*)d_in[1];
    const bf16_t* wo = (const bf16_t*)d_in[2];
    const bf16_t* w1 = (const bf16_t*)d_in[4];
    const bf16_t* b1 = (const bf16_t*)d_in[5];
    const bf16_t* w2 = (const bf16_t*)d_in[6];
    const bf16_t* b2 = (const bf16_t*)d_in[7];
    bf16_t* out = (bf16_t*)d_out;
    char* ws = (char*)d_ws;
    char* of = ws;
    char* wof = ws + (3 << 20);
    bf16_t* h = (bf16_t*)(ws + (4 << 20));
    char* hT = ws + (12 << 20);
    f2v* stats = (f2v*)(ws + (8 << 20));
    float* cs1 = (float*)(ws + (9 << 20));
    char* h1 = (char*)(ws + (16 << 20));
    char* w2f = ws + (48 << 20);

    hipFuncSetAttribute((const void*)k1_qkv_attn, hipFuncAttributeMaxDynamicSharedMemorySize, K1_LDS);
    hipFuncSetAttribute((const void*)k2_wo, hipFuncAttributeMaxDynamicSharedMemorySize, K2_LDS);
    hipFuncSetAttribute((const void*)k3_up, hipFuncAttributeMaxDynamicSharedMemorySize, K3_LDS);
    hipFuncSetAttribute((const void*)k4_down, hipFuncAttributeMaxDynamicSharedMemorySize, K4_LDS);

    k1_qkv_attn<<<256, 512, K1_LDS, stream>>>(x, wqkv, w1, wo, of, wof, cs1);
    k2_wo<<<256, 512, K2_LDS, stream>>>(of, wof, x, h, hT, stats);
    k3_up<<<256, 512, K3_LDS, stream>>>(hT, w1, b1, cs1, stats, w2, h1, w2f);
    k4_down<<<256, 512, K4_LDS, stream>>>(h1, w2f, h, b2, out);
}
```

```cpp
#include <hip/hip_runtime.h>
#include <hip/hip_bf16.h>

typedef __attribute__((ext_vector_type(8))) short s8v;
typedef __attribute__((ext_vector_type(4))) short s4v;
typedef __attribute__((ext_vector_type(4))) short v4i16_t;
typedef __attribute__((ext_vector_type(4))) float f4v;
typedef __attribute__((ext_vector_type(2))) float f2v;
typedef __attribute__((ext_vector_type(4))) unsigned u4v;
typedef __attribute__((ext_vector_type(2))) unsigned u2v;
typedef unsigned short bf16_t;

constexpr int S_ = 2048, D_ = 576, NQKV = 1728, F_ = 4608;

__device__ __forceinline__ unsigned lds_addr(const void* l) {
    return (unsigned)(size_t)(__attribute__((address_space(3))) const char*)l;
}
__device__ __forceinline__ void glds16(const void* g, unsigned dst) {
    unsigned keep;
    asm volatile("s_mov_b32 %0, m0\n\ts_mov_b32 m0, %2\n\ts_nop 0\n\tglobal_load_lds_dwordx4 %1, off\n\ts_mov_b32 m0, %0"
                 : "=&s"(keep) : "v"(g), "s"(dst) : "memory");
}
__device__ __forceinline__ void glds16s(const void* sbase, unsigned voff, unsigned dst) {
    unsigned keep;
    asm volatile("s_mov_b32 %0, m0\n\ts_mov_b32 m0, %3\n\ts_nop 0\n\tglobal_load_lds_dwordx4 %1, %2\n\ts_mov_b32 m0, %0"
                 : "=&s"(keep) : "v"(voff), "s"(sbase), "s"(dst) : "memory");
}
__device__ __forceinline__ s4v tr_read(const char* p) {
    return __builtin_bit_cast(s4v, __builtin_amdgcn_ds_read_tr16_b64_v4i16((__attribute__((address_space(3))) v4i16_t*)p));
}
__device__ __forceinline__ s8v cat8(s4v a, s4v b) {
    return __builtin_shufflevector(a, b, 0, 1, 2, 3, 4, 5, 6, 7);
}
__device__ __forceinline__ float bf_lo(unsigned u) { return __builtin_bit_cast(float, u << 16); }
__device__ __forceinline__ float bf_hi(unsigned u) { return __builtin_bit_cast(float, u & 0xffff0000u); }
__device__ __forceinline__ float bf2f(bf16_t v) { return __builtin_bit_cast(float, (unsigned)v << 16); }
__device__ __forceinline__ unsigned pack_bf2(float a, float b) {
    unsigned r;
    asm("v_cvt_pk_bf16_f32 %0, %1, %2" : "=v"(r) : "v"(a), "v"(b));
    return r;
}
#define MFMA16(a, b, c) __builtin_amdgcn_mfma_f32_16x16x32_bf16(a, b, c, 0, 0, 0)
#define WAIT_V(n) asm volatile("s_waitcnt vmcnt(" #n ")" ::: "memory")
#define WAIT_L(n) asm volatile("s_waitcnt lgkmcnt(" #n ")" ::: "memory")

#define SB0 __builtin_amdgcn_sched_barrier(0)
#define BAR __builtin_amdgcn_s_barrier()
constexpr int K1_XN_BYTES = 9 * 80 * 128;
constexpr int K1_STAGE = 14336;
constexpr int K1_NB = 4;
constexpr int K1_DUMMY = K1_XN_BYTES + K1_NB * K1_STAGE;
constexpr int K1_RED = K1_DUMMY + 5120;
constexpr int K1_LDS = K1_RED + 2048;

__global__ __launch_bounds__(512, 2) void k1_qkv_attn(const bf16_t* __restrict__ x, const bf16_t* __restrict__ wqkv,
                                                      const bf16_t* __restrict__ w1, const bf16_t* __restrict__ wo,
                                                      char* __restrict__ of, char* __restrict__ wof,
                                                      float* __restrict__ cs1) {
    extern __shared__ __attribute__((aligned(16))) char smem[];
    const int tid = threadIdx.x, lane = tid & 63;
    const int wave = __builtin_amdgcn_readfirstlane(tid >> 6);
    const int xcd = blockIdx.x & 7, idx = blockIdx.x >> 3;
    const int hg = (xcd & 1) * 4 + (idx >> 3), rt = (xcd >> 1) * 8 + (idx & 7);
    const int s0 = rt * 64;
    const unsigned lring = lds_addr(smem) + K1_XN_BYTES;
    const int g = lane >> 4, q = (lane & 15) >> 2, p = lane & 3;

    const int lw = wave - 4;
    unsigned bsrc[4];
    auto issue = [&](int st) {
        int sc = st > 17 ? 17 : st;
        unsigned lb = lring + (st % K1_NB) * K1_STAGE;
        const char* gb = (const char*)wqkv + (size_t)sc * 32 * NQKV * 2;
#pragma unroll
        for (int i = 0; i < 4; i++) glds16s(gb, bsrc[i], lb + (lw * 4 + i > 13 ? 13 : lw * 4 + i) * 1024);
    };
    u4v cw[6];
    const int cu = blockIdx.x < 216 ? blockIdx.x : blockIdx.x - 216;
    const int cchunk = cu % 72, rthird = cu / 72;
    const unsigned dz = lds_addr(smem) + K1_DUMMY + (wave & 3) * 1024;
    auto ln_pass = [&](const u4v* xx, int r) {
        f2v v[36];
        f2v sm = f2v{0.f, 0.f};
#pragma unroll
        for (int i = 0; i < 9; i++)
#pragma unroll
            for (int t = 0; t < 4; t++) { v[i * 4 + t] = f2v{bf_lo(xx[i][t]), bf_hi(xx[i][t])}; sm += v[i * 4 + t]; }
        float sum = sm[0] + sm[1];
        sum += __shfl_xor(sum, 1); sum += __shfl_xor(sum, 2); sum += __shfl_xor(sum, 4);
        const float mean = sum * (1.f / 576.f);
        f2v sq = f2v{0.f, 0.f};
#pragma unroll
        for (int i = 0; i < 36; i++) { v[i] = v[i] - mean; sq += v[i] * v[i]; }
        float sqs = sq[0] + sq[1];
        sqs += __shfl_xor(sqs, 1); sqs += __shfl_xor(sqs, 2); sqs += __shfl_xor(sqs, 4);
        const float rstd = rsqrtf(sqs * (1.f / 576.f) + 1e-5f);
#pragma unroll
        for (int i = 0; i < 9; i++) {
            u4v ov;
#pragma unroll
            for (int t = 0; t < 4; t++) { f2v w = v[i * 4 + t] * rstd; ov[t] = pack_bf2(w[0], w[1]); }
            *(u4v*)(smem + i * (80 * 128) + r * 128 + (((lane & 7) ^ (r & 7)) << 4)) = ov;
        }
    };
    if (wave >= 4) {
        u4v xv[9];
        {
            const int r = wave * 8 + (lane >> 3);
            const char* pp = (const char*)(x + (size_t)(s0 - 16 + r) * D_) + (lane & 7) * 16;
#pragma unroll
            for (int i = 0; i < 9; i++)
                asm volatile("global_load_dwordx4 %0, %1, off offset:%2" : "=v"(xv[i]) : "v"(pp), "n"(i * 128) : "memory");
        }
#pragma unroll
        for (int i = 0; i < 4; i++) {
            int bi = lw * 4 + i; if (bi > 13) bi = 13;
            int off = bi * 1024 + lane * 16;
            int k = off / 448, r = off - k * 448;
            int nt = (r >> 5) ^ ((k >> 3) & 1);
            int pc = nt * 2 + ((r >> 4) & 1); if (pc > 26) pc = 26;
            int sec = pc / 9, wi = pc - sec * 9;
            bsrc[i] = (unsigned)((k * NQKV + sec * 576 + hg * 72 + wi * 8) * 2);
        }
#pragma unroll
        for (int st = 0; st < K1_NB - 1; st++) issue(st);
#pragma unroll
        for (int j = 0; j < 8; j++) {
            int c = (lw * 8 + j) * 64 + lane; if (c > 1943) c = 1943;
            int seg = c / 9, pc = c - seg * 9;
            int sec = seg / 72, rr = seg - sec * 72;
            glds16s(wqkv, (unsigned)((((idx & 7) * 72 + rr) * NQKV + sec * 576 + hg * 72 + pc * 8) * 2), dz);
        }
        asm volatile("s_waitcnt vmcnt(20)" : "+v"(xv[0]), "+v"(xv[1]), "+v"(xv[2]), "+v"(xv[3]), "+v"(xv[4]),
                     "+v"(xv[5]), "+v"(xv[6]), "+v"(xv[7]), "+v"(xv[8]) :: "memory");
        ln_pass(xv, wave * 8 + (lane >> 3));
    } else {
        u4v xv[2][9];
#pragma unroll
        for (int j = 0; j < 2; j++) {
            if (j < 1 || wave < 2) {
                int r = (8 * j + wave) * 8 + (lane >> 3);
                int s = s0 - 16 + r; if (s < 0) s = 0;
                const u4v* pp = (const u4v*)(x + (size_t)s * D_) + (lane & 7);
#pragma unroll
                for (int i = 0; i < 9; i++) xv[j][i] = pp[i * 8];
            }
        }
        ln_pass(xv[0], wave * 8 + (lane >> 3));
        if (wave < 2) ln_pass(xv[1], (8 + wave) * 8 + (lane >> 3));
    }

    f4v acc[5][4];
    if (wave >= 4) {
        WAIT_V(16);
        __syncthreads();
        WAIT_V(12); BAR;
        issue(3); WAIT_V(12); BAR;
        for (int i = 1; i < 15; i++) {
            issue(i + K1_NB - 1);
            WAIT_V(4);
            BAR;
        }
        WAIT_V(0); BAR;
        {
            const u4v* pw = (const u4v*)(w1 + (size_t)(rthird * 192 + lw * 48 + (lane >> 3)) * F_ + cchunk * 64) + (lane & 7);
#pragma unroll
            for (int j = 0; j < 6; j++) cw[j] = pw[(size_t)j * 8 * F_ / 8];
            if (wave == 4 && blockIdx.x < 144) {
                const int ctp = blockIdx.x / 18, kst = blockIdx.x - ctp * 18;
#pragma unroll
                for (int j = 0; j < 5; j++) {
                    int c = j * 64 + lane; if (c > 287) c = 287;
                    int row = c / 9, pc = c - row * 9;
                    glds16s(wo, (unsigned)(((kst * 32 + row) * D_ + ctp * 72 + pc * 8) * 2), lds_addr(smem) + K1_DUMMY + j * 1024);
                }
            }
        }
        BAR; BAR;
    } else {
        const int nt0 = wave == 0 ? 0 : 1 + 3 * wave;
        const int nn = (wave == 0 || wave == 3) ? 4 : 3;
#pragma unroll
        for (int m = 0; m < 5; m++)
#pragma unroll
            for (int n = 0; n < 4; n++) acc[m][n] = f4v{0, 0, 0, 0};
        const int xr = lane & 15;
        int woff[4];
#pragma unroll
        for (int n = 0; n < 4; n++) woff[n] = K1_XN_BYTES + (8 * g + q) * 448 + (((nt0 + n) ^ (g & 1)) << 5) + 8 * p;
        s8v xa[5], xb[5], wf[4];
        auto rdx = [&](s8v* X, int st) {
            const char* xbp = smem + (st >> 1) * (80 * 128) + xr * 128 + ((((st & 1) * 4 + g) ^ (xr & 7)) << 4);
#pragma unroll
            for (int m = 0; m < 5; m++) X[m] = *(const s8v*)(xbp + m * 2048);
        };
        __syncthreads();
        rdx(xa, 0);
#pragma unroll
        for (int n = 0; n < 4; n++) wf[n] = cat8(tr_read(smem + woff[n]), tr_read(smem + woff[n] + 4 * 448));
        WAIT_L(0);
        BAR;
#define K1_STEP(XC, XN) { \
            const char* base = smem + ((i + 1) % K1_NB) * K1_STAGE; \
            rdx(XN, i + 1 > 17 ? 17 : i + 1); \
            SB0; \
            _Pragma("unroll") for (int n = 0; n < 4; n++) { \
                if (n < 3 || nn == 4) { \
                    if (wave != 0) acc[0][n] = MFMA16(XC[0], wf[n], acc[0][n]); \
                    _Pragma("unroll") for (int m = 1; m < 5; m++) acc[m][n] = MFMA16(XC[m], wf[n], acc[m][n]); \
                    SB0; \
                    wf[n] = cat8(tr_read(base + woff[n]), tr_read(base + woff[n] + 4 * 448)); \
                    SB0; \
                } \
            } \
            BAR; }
#pragma unroll 1
        for (int i = 0; i < 18; i += 2) {
            K1_STEP(xa, xb);
            i++; K1_STEP(xb, xa); i--;
        }
    }
    __syncthreads();
    constexpr int QS = 84;
    float* qkv = (float*)smem;
    if (wave < 4) {
        const int nt0 = wave == 0 ? 0 : 1 + 3 * wave;
        const int nn = (wave == 0 || wave == 3) ? 4 : 3;
#pragma unroll
        for (int m = 0; m < 5; m++)
#pragma unroll
            for (int n = 0; n < 4; n++) {
                int col = (nt0 + n) * 16 + (lane & 15);
                if (n < nn && col < 216) *(f4v*)(qkv + col * QS + m * 16 + 4 * g) = acc[m][n];
            }
    }
    __syncthreads();
    {
        const int head = wave >> 1;
        const int rl = (wave & 1) * 32 + (lane & 31);
        const int hw = lane >> 5;
        const int r = 16 + rl;
        const int s = s0 + rl;
        const float* qp = qkv + (head * 18) * QS + r;
        const float* kp = qkv + (72 + head * 18) * QS + r - 8 * hw;
        const float* vp = qkv + (144 + head * 18) * QS + r - 8 * hw;
        float qv[18];
#pragma unroll
        for (int d = 0; d < 18; d++) qv[d] = qp[d * QS];
        float lg[4];
        float mx = -3.0e38f;
#pragma unroll
        for (int w = 0; w < 4; w++) {
            float a = 0.f;
#pragma unroll
            for (int d = 0; d < 18; d++) a += qv[d] * kp[d * QS - 2 * w];
            a *= 0.23570226039551584f;
            lg[w] = (s - 2 * (w + 4 * hw) >= 0) ? a : -1e9f;
            mx = fmaxf(mx, lg[w]);
        }
        mx = fmaxf(mx, __shfl_xor(mx, 32));
        float den = 0.f;
#pragma unroll
        for (int w = 0; w < 4; w++) { lg[w] = __expf(lg[w] - mx); den += lg[w]; }
        den += __shfl_xor(den, 32);
        float inv = 1.f / den;
        float ov[18];
#pragma unroll
        for (int d = 0; d < 18; d++) ov[d] = 0.f;
#pragma unroll
        for (int w = 0; w < 4; w++) {
            float pw = lg[w] * inv;
#pragma unroll
            for (int d = 0; d < 18; d++) ov[d] += pw * vp[d * QS - 2 * w];
        }
#pragma unroll
        for (int d = 0; d < 18; d++) ov[d] += __shfl_xor(ov[d], 32);
        char* ob = of + (size_t)(s >> 4) * (18 * 1024) + (s & 15) * 16;
        const int cb = hg * 72 + head * 18;
#pragma unroll
        for (int d = 0; d < 5; d++) {
            const int dd = hw == 0 ? d : (d + 5 > 8 ? 8 : d + 5);
            const int col = cb + 2 * dd;
            const unsigned pv = hw == 0 ? pack_bf2(ov[2 * d], ov[2 * d + 1]) : pack_bf2(ov[2 * (d + 5 > 8 ? 8 : d + 5)], ov[2 * (d + 5 > 8 ? 8 : d + 5) + 1]);
            if (hw == 0 || d < 4) *(unsigned*)(ob + (col >> 5) * 1024 + ((col & 31) >> 3) * 256 + (col & 7) * 2) = pv;
        }
    }
    if (wave >= 4) {
        float a[8];
#pragma unroll
        for (int j = 0; j < 8; j++) a[j] = 0.f;
#pragma unroll
        for (int i = 0; i < 6; i++)
#pragma unroll
            for (int j = 0; j < 4; j++) { a[2 * j] += bf_lo(cw[i][j]); a[2 * j + 1] += bf_hi(cw[i][j]); }
#pragma unroll
        for (int j = 0; j < 8; j++) {
            float t = a[j];
            t += __shfl_xor(t, 8); t += __shfl_xor(t, 16); t += __shfl_xor(t, 32);
            a[j] = t;
        }
        if (lane < 8) {
            float* rq = (float*)(smem + K1_RED) + lw * 64 + lane * 8;
            *(f4v*)rq = f4v{a[0], a[1], a[2], a[3]};
            *(f4v*)(rq + 4) = f4v{a[4], a[5], a[6], a[7]};
        }
        WAIT_V(0);
        if (wave == 4 && blockIdx.x < 144) {
            const char* sb = smem + K1_DUMMY + (8 * g + q) * 144 + 8 * p;
            char* dst = wof + (size_t)blockIdx.x * 5 * 1024 + lane * 16;
#pragma unroll
            for (int nt = 0; nt < 5; nt++)
                *(s8v*)(dst + nt * 1024) = cat8(tr_read(sb + nt * 32), tr_read(sb + 4 * 144 + nt * 32));
        }
    }
    __syncthreads();
    if (wave == 7 && blockIdx.x < 216) {
        const float* rq = (const float*)(smem + K1_RED) + lane;
        cs1[rthird * F_ + cchunk * 64 + lane] = (rq[0] + rq[64]) + (rq[128] + rq[192]);
    }
}

constexpr int K2_LDS = 4 * 20 * 1024;

__global__ __launch_bounds__(512, 2) void k2_wo(const char* __restrict__ of, const char* __restrict__ wof,
                                                const bf16_t* __restrict__ x, bf16_t* __restrict__ h,
                                                char* __restrict__ hT, f2v* __restrict__ stats) {
    extern __shared__ __attribute__((aligned(16))) char smem[];
    const int tid = threadIdx.x, lane = tid & 63;
    const int wave = __builtin_amdgcn_readfirstlane(tid >> 6);
    const int rt = (blockIdx.x & 7) * 4 + ((blockIdx.x >> 3) & 3), ct = blockIdx.x >> 5;
    const int r0 = rt * 64, c0 = ct * 72;
    const int g = lane >> 4;
    const char* pa = of + ((size_t)rt * 4 * 18 + wave) * 1024 + lane * 16;
    const char* pw = wof + ((size_t)ct * 18 + wave) * 5 * 1024 + lane * 16;
    s8v fa[3][4], fw[3][5];
#pragma unroll
    for (int j = 0; j < 3; j++) {
        if (j < 2 || wave < 2) {
#pragma unroll
            for (int m = 0; m < 4; m++) fa[j][m] = *(const s8v*)(pa + m * (18 * 1024) + j * 8192);
#pragma unroll
            for (int n = 0; n < 5; n++) fw[j][n] = *(const s8v*)(pw + n * 1024 + j * 40960);
        }
    }
    const int row = r0 + (wave & 3) * 16 + (lane & 15);
    u2v xx[5];
#pragma unroll
    for (int n = 0; n < 5; n++) {
        int col = n * 16 + 4 * g; if (col > 68) col = 68;
        xx[n] = *(const u2v*)(x + (size_t)row * D_ + c0 + col);
    }
    f4v acc[4][5];
#pragma unroll
    for (int m = 0; m < 4; m++)
#pragma unroll
        for (int n = 0; n < 5; n++) acc[m][n] = f4v{0, 0, 0, 0};
#pragma unroll
    for (int j = 0; j < 3; j++) {
        if (j < 2 || wave < 2) {
#pragma unroll
            for (int n = 0; n < 5; n++)
#pragma unroll
                for (int m = 0; m < 4; m++) acc[m][n] = MFMA16(fw[j][n], fa[j][m], acc[m][n]);
        }
    }
    f4v* ex = (f4v*)smem + (wave & 3) * (20 * 64) + lane;
    if (wave >= 4) {
#pragma unroll
        for (int m = 0; m < 4; m++)
#pragma unroll
            for (int n = 0; n < 5; n++) ex[(m * 5 + n) * 64] = acc[m][n];
    }
    __syncthreads();
    if (wave < 4) {
#pragma unroll
        for (int m = 0; m < 4; m++)
#pragma unroll
            for (int n = 0; n < 5; n++) { f4v v = ex[(m * 5 + n) * 64]; ex[(m * 5 + n) * 64] = acc[m][n] + v; }
    }
    __syncthreads();
    if (wave >= 4) {
        return;
    }
    float hv[5][4];
    float sum = 0.f;
#pragma unroll
    for (int n = 0; n < 5; n++) {
        const f4v* e = (const f4v*)smem + (wave * 5 + n) * 64 + lane;
        f4v a = (e[0] + e[20 * 64]) + (e[40 * 64] + e[60 * 64]);
        int col = n * 16 + 4 * g;
        if (col < 72) {
            unsigned p0 = pack_bf2(bf_lo(xx[n][0]) + a[0], bf_hi(xx[n][0]) + a[1]);
            unsigned p1 = pack_bf2(bf_lo(xx[n][1]) + a[2], bf_hi(xx[n][1]) + a[3]);
            *(u2v*)(h + (size_t)row * D_ + c0 + col) = u2v{p0, p1};
            {
                int cc = c0 + col;
                *(u2v*)(hT + (size_t)((row >> 4) * 18 + (cc >> 5)) * 1024 + (((cc & 31) >> 3) * 16 + (row & 15)) * 16 + (cc & 7) * 2) = u2v{p0, p1};
            }
            hv[n][0] = bf_lo(p0); hv[n][1] = bf_hi(p0); hv[n][2] = bf_lo(p1); hv[n][3] = bf_hi(p1);
            sum += hv[n][0] + hv[n][1] + hv[n][2] + hv[n][3];
        } else {
            hv[n][0] = hv[n][1] = hv[n][2] = hv[n][3] = 0.f;
        }
    }
    sum += __shfl_xor(sum, 16); sum += __shfl_xor(sum, 32);
    float mean = sum * (1.f / 72.f);
    float m2 = 0.f;
#pragma unroll
    for (int n = 0; n < 5; n++) {
        int col = n * 16 + 4 * g;
        if (col < 72) {
#pragma unroll
            for (int j = 0; j < 4; j++) { float d = hv[n][j] - mean; m2 += d * d; }
        }
    }
    m2 += __shfl_xor(m2, 16); m2 += __shfl_xor(m2, 32);
    if (g == 0) stats[(size_t)row * 8 + ct] = f2v{sum, m2};
}

constexpr int K3_STAGE = 8192 + 18432;
constexpr int K3_NB = 5;
constexpr int K3_PAR = K3_NB * K3_STAGE;
constexpr int K3_LDS = K3_PAR + 1024 + 2304;

__device__ __forceinline__ float gelu_tanh(float v) {
    float t = v * v;
    float z = v * (-2.302208198f * 0.044715f * t - 2.302208198f);
    float e = __builtin_amdgcn_exp2f(z);
    return v * __builtin_amdgcn_rcpf(1.f + e);
}

__device__ __forceinline__ f2v gelu_tanh2(f2v v) {
    f2v t = v * v;
    f2v z = v * (t * (-2.302208198f * 0.044715f) + (-2.302208198f));
    f2v d = f2v{__builtin_amdgcn_exp2f(z[0]), __builtin_amdgcn_exp2f(z[1])} + 1.f;
    return v * f2v{__builtin_amdgcn_rcpf(d[0]), __builtin_amdgcn_rcpf(d[1])};
}

__global__ __launch_bounds__(512, 2) void k3_up(const char* __restrict__ hT, const bf16_t* __restrict__ w1,
                                                const bf16_t* __restrict__ b1, const float* __restrict__ cs1,
                                                const f2v* __restrict__ stats, const bf16_t* __restrict__ w2,
                                                char* __restrict__ h1f, char* __restrict__ w2f) {
    extern __shared__ __attribute__((aligned(16))) char smem[];
    const int tid = threadIdx.x, lane = tid & 63;
    const int wave = __builtin_amdgcn_readfirstlane(tid >> 6);
    const int xcd = blockIdx.x & 7, idx = blockIdx.x >> 3;
    const int rt = (xcd & 1) * 8 + (idx & 7), ct = (xcd >> 1) * 4 + (idx >> 3);
    const int r0 = rt * 128, c0 = ct * 288;
    const unsigned lbase = lds_addr(smem);
    const int g = lane >> 4, q = (lane & 15) >> 2, p = lane & 3;
    const int xr = lane & 15;
    f2v* mu = (f2v*)(smem + K3_PAR);
    float* csl = (float*)(smem + K3_PAR + 1024);
    float* bbl = csl + 288;
    f4v acc[4][9];
    const int wm = (wave >> 1) & 1, wn = wave & 1;
    if (wave >= 4) {
        const int lw = wave - 4;
        unsigned bsrc[5];
        const unsigned avo = lane * 16;
        const char* ag = hT + (size_t)(rt * 8 + lw * 2) * 18 * 1024;
#pragma unroll
        for (int i = 0; i < 5; i++) {
            int bi = lw * 5 + i; if (bi > 17) bi = 17;
            int off = bi * 1024 + lane * 16;
            int k = off / 576, r = off - k * 576;
            int nt = (r >> 5) ^ ((k >> 3) & 1);
            bsrc[i] = (unsigned)((k * F_ + c0 + nt * 16 + ((r >> 4) & 1) * 8) * 2);
        }
        auto issue = [&](int st) {
            int sc = st > 17 ? 17 : st;
            unsigned lb = lbase + (st % K3_NB) * K3_STAGE;
            const char* ga = ag + sc * 1024;
            const char* gb = (const char*)w1 + (size_t)sc * 32 * F_ * 2;
            glds16s(ga, avo, lb + (lw * 2) * 1024);
            glds16s(ga + 18 * 1024, avo, lb + (lw * 2 + 1) * 1024);
#pragma unroll
            for (int i = 0; i < 5; i++) glds16s(gb, bsrc[i], lb + 8192 + (lw * 5 + i > 17 ? 17 : lw * 5 + i) * 1024);
        };
#pragma unroll
        for (int st = 0; st < K3_NB - 1; st++) issue(st);
        WAIT_V(21);
        BAR;
        for (int i = -1; i < 18; i++) {
            if (i >= 0) issue(i + K3_NB - 1);
            WAIT_V(14);
            BAR;
        }
        WAIT_V(0);
    } else {
        {
            float c0v = (cs1[c0 + tid] + cs1[F_ + c0 + tid]) + cs1[2 * F_ + c0 + tid], b0v = bf2f(b1[c0 + tid]);
            float c1v = 0.f, b1v = 0.f;
            if (tid < 32) { c1v = (cs1[c0 + 256 + tid] + cs1[F_ + c0 + 256 + tid]) + cs1[2 * F_ + c0 + 256 + tid]; b1v = bf2f(b1[c0 + 256 + tid]); }
            if (tid < 128) {
                const f4v* sp = (const f4v*)(stats + (size_t)(r0 + tid) * 8);
                f4v v[4];
#pragma unroll
                for (int i = 0; i < 4; i++) v[i] = sp[i];
                float tot = 0.f;
#pragma unroll
                for (int i = 0; i < 4; i++) tot += v[i][0] + v[i][2];
                float mean = tot * (1.f / 576.f);
                float M2 = 0.f;
#pragma unroll
                for (int i = 0; i < 4; i++) {
                    float d0 = v[i][0] * (1.f / 72.f) - mean, d1 = v[i][2] * (1.f / 72.f) - mean;
                    M2 += v[i][1] + v[i][3] + 72.f * (d0 * d0 + d1 * d1);
                }
                float rstd = rsqrtf(M2 * (1.f / 576.f) + 1e-5f);
                mu[tid] = f2v{mean * rstd, rstd};
            }
            csl[tid] = c0v; bbl[tid] = b0v;
            if (tid < 32) { csl[256 + tid] = c1v; bbl[256 + tid] = b1v; }
        }
#pragma unroll
        for (int m = 0; m < 4; m++)
#pragma unroll
            for (int n = 0; n < 9; n++) acc[m][n] = f4v{0, 0, 0, 0};
        const int dlt = (g & 1) ? 32 : 0;
        const int wb0 = 8192 + (8 * g + q) * 576 + wn * 9 * 32 + 8 * p;
        const int wbe = wb0 + ((wn & 1) ? -dlt : dlt);
        const int wbo = wb0 + ((wn & 1) ? dlt : -dlt);
        const int xoff = wm * 4096 + lane * 16;
        s8v xa[4], xb[4], wf[9];
        BAR;
        {
            const char* base = smem;
#pragma unroll
            for (int m = 0; m < 4; m++) xa[m] = *(const s8v*)(base + xoff + m * 1024);
#pragma unroll
            for (int n = 0; n < 9; n++) {
                const char* wb = base + ((n & 1) ? wbo : wbe) + n * 32;
                wf[n] = cat8(tr_read(wb), tr_read(wb + 4 * 576));
            }
        }
        WAIT_L(0);
        BAR;
#define K3_STEP(XC, XN) { \
            const char* base = smem + ((i + 1) % K3_NB) * K3_STAGE; \
            s4v wlo, whi; \
            _Pragma("unroll") for (int n = 0; n < 9; n++) { \
                const char* wbp = base + (((n - 1) & 1) ? wbo : wbe) + (n - 1) * 32; \
                acc[0][n] = MFMA16(wf[n], XC[0], acc[0][n]); SB0; \
                if (n > 0) { wlo = tr_read(wbp); SB0; } \
                acc[1][n] = MFMA16(wf[n], XC[1], acc[1][n]); SB0; \
                if (n > 0) { whi = tr_read(wbp + 4 * 576); wf[n - 1] = cat8(wlo, whi); SB0; } \
                acc[2][n] = MFMA16(wf[n], XC[2], acc[2][n]); SB0; \
                if (n < 4) { XN[n] = *(const s8v*)(base + xoff + n * 1024); SB0; } \
                acc[3][n] = MFMA16(wf[n], XC[3], acc[3][n]); SB0; \
            } \
            { const char* wbp = base + wbe + 8 * 32; wf[8] = cat8(tr_read(wbp), tr_read(wbp + 4 * 576)); } \
            BAR; }
#pragma unroll 1
        for (int i = 0; i < 18; i += 2) {
            K3_STEP(xa, xb);
            i++; K3_STEP(xb, xa); i--;
        }
    }
    __syncthreads();
    const int u = wave * 256 + blockIdx.x;
    if (u < 1152) {
        const int ctp = u / 144, kst = u - ctp * 144;
#pragma unroll
        for (int j = 0; j < 5; j++) {
            int c = j * 64 + lane; if (c > 287) c = 287;
            int row = c / 9, pc = c - row * 9;
            glds16s(w2, (unsigned)(((kst * 32 + row) * D_ + ctp * 72 + pc * 8) * 2), lbase + 98304 + wave * 5120 + j * 1024);
        }
    }
    f4v* ex = (f4v*)smem + (wave & 3) * (18 * 64) + lane;
    if (wave < 4) {
#pragma unroll
        for (int m = 0; m < 2; m++)
#pragma unroll
            for (int n = 0; n < 9; n++) ex[(m * 9 + n) * 64] = acc[m + 2][n];
    }
    __syncthreads();
    if (wave >= 4) {
#pragma unroll
        for (int m = 0; m < 2; m++)
#pragma unroll
            for (int n = 0; n < 9; n++) acc[m][n] = ex[(m * 9 + n) * 64];
    }
    {
        const int mb = (wave >> 2) * 2;
#pragma unroll
        for (int m = 0; m < 2; m++) {
            int rl = wm * 64 + (mb + m) * 16 + xr;
            f2v ms = mu[rl];
            char* ob = h1f + (size_t)((r0 + wm * 64 + (mb + m) * 16) >> 4) * (144 * 1024) + lane * 16;
            u2v pv[9];
#pragma unroll
            for (int n = 0; n < 9; n++) {
                f4v a = acc[m][n];
                int cl = wn * 144 + n * 16 + 4 * g;
                f4v cs = *(const f4v*)(csl + cl), bb = *(const f4v*)(bbl + cl);
                f2v t01 = f2v{bb[0], bb[1]} - f2v{cs[0], cs[1]} * ms[0];
                f2v t23 = f2v{bb[2], bb[3]} - f2v{cs[2], cs[3]} * ms[0];
                f2v v01 = gelu_tanh2(f2v{a[0], a[1]} * ms[1] + t01);
                f2v v23 = gelu_tanh2(f2v{a[2], a[3]} * ms[1] + t23);
                pv[n] = u2v{pack_bf2(v01[0], v01[1]), pack_bf2(v23[0], v23[1])};
            }
            if (m == 0) {
            } else {
            }
            char* oc = ob + (c0 >> 5) * 1024;
            if (wn == 0) {
#pragma unroll
                for (int j = 0; j < 4; j++) *(u4v*)(oc + j * 1024) = u4v{pv[2 * j][0], pv[2 * j][1], pv[2 * j + 1][0], pv[2 * j + 1][1]};
                *(u2v*)(oc + 4 * 1024) = pv[8];
            } else {
                *(u2v*)(oc + 4 * 1024 + 8) = pv[0];
#pragma unroll
                for (int j = 0; j < 4; j++) *(u4v*)(oc + (5 + j) * 1024) = u4v{pv[2 * j + 1][0], pv[2 * j + 1][1], pv[2 * j + 2][0], pv[2 * j + 2][1]};
            }
        }
    }
    WAIT_V(0);
    if (u < 1152) {
        const char* sb = smem + 98304 + wave * 5120 + (4 * g + q) * 144 + 8 * p;
        char* dst = w2f + (size_t)u * 5 * 1024 + lane * 16;
#pragma unroll
        for (int nt = 0; nt < 5; nt++)
            *(s8v*)(dst + nt * 1024) = cat8(tr_read(sb + nt * 32), tr_read(sb + 16 * 144 + nt * 32));
    }
}

constexpr int K4_LDS = 4 * 20 * 1024;
constexpr int K4_D = 3;

__global__ __launch_bounds__(512, 2) void k4_down(const char* __restrict__ h1f, const char* __restrict__ w2f,
                                                  const bf16_t* __restrict__ h, const bf16_t* __restrict__ b2,
                                                  bf16_t* __restrict__ out) {
    extern __shared__ __attribute__((aligned(16))) char smem[];
    const int tid = threadIdx.x, lane = tid & 63;
    const int wave = __builtin_amdgcn_readfirstlane(tid >> 6);
    const int xcd = blockIdx.x & 7, idx = blockIdx.x >> 3;
    const int rt = (xcd >> 1) * 8 + (idx & 7), ct = (xcd & 1) * 4 + (idx >> 3);
    const int g = lane >> 4;
    const char* pa = h1f + ((size_t)rt * 4 * 144 + wave) * 1024 + lane * 16;
    const char* pw = w2f + ((size_t)ct * 144 + wave) * 5 * 1024 + lane * 16;
    f4v acc[4][5];
#pragma unroll
    for (int m = 0; m < 4; m++)
#pragma unroll
        for (int n = 0; n < 5; n++) acc[m][n] = f4v{0, 0, 0, 0};
    s8v fa[K4_D][4], fw[K4_D][5];
#pragma unroll
    for (int j = 0; j < K4_D; j++) {
#pragma unroll
        for (int m = 0; m < 4; m++) fa[j][m] = *(const s8v*)(pa + m * (144 * 1024) + j * 8192);
#pragma unroll
        for (int n = 0; n < 5; n++) fw[j][n] = *(const s8v*)(pw + n * 1024 + j * 40960);
        SB0;
    }
#pragma unroll
    for (int j = 0; j < 18; j++) {
        const int sl = j % K4_D;
        SB0;
#pragma unroll
        for (int n = 0; n < 5; n++)
#pragma unroll
            for (int m = 0; m < 4; m++) acc[m][n] = MFMA16(fw[sl][n], fa[sl][m], acc[m][n]);
        SB0;
        if (j + K4_D < 18) {
#pragma unroll
            for (int m = 0; m < 4; m++) fa[sl][m] = *(const s8v*)(pa + m * (144 * 1024) + (j + K4_D) * 8192);
#pragma unroll
            for (int n = 0; n < 5; n++) fw[sl][n] = *(const s8v*)(pw + n * 1024 + (j + K4_D) * 40960);
        }
        SB0;
    }
    u2v hh[3], bb[3];
#pragma unroll
    for (int j = 0; j < 3; j++) {
        int t = wave + 8 * j; if (t > 19) t = 19;
        const int m = t / 5, n = t - m * 5;
        const int row = rt * 64 + m * 16 + (lane & 15);
        int cl = n * 16 + 4 * g; if (cl > 68) cl = 68;
        hh[j] = *(const u2v*)(h + (size_t)row * D_ + ct * 72 + cl);
        bb[j] = *(const u2v*)(b2 + ct * 72 + cl);
    }
    f4v* ex = (f4v*)smem + (wave & 3) * (20 * 64) + lane;
    if (wave >= 4) {
#pragma unroll
        for (int m = 0; m < 4; m++)
#pragma unroll
            for (int n = 0; n < 5; n++) ex[(m * 5 + n) * 64] = acc[m][n];
    }
    __syncthreads();
    if (wave < 4) {
#pragma unroll
        for (int m = 0; m < 4; m++)
#pragma unroll
            for (int n = 0; n < 5; n++) { f4v v = ex[(m * 5 + n) * 64]; ex[(m * 5 + n) * 64] = acc[m][n] + v; }
    }
    __syncthreads();
#pragma unroll
    for (int j = 0; j < 3; j++) {
        const int t = wave + 8 * j;
        if (t < 20) {
            const int m = t / 5, n = t - m * 5;
            const f4v* e = (const f4v*)smem + t * 64 + lane;
            f4v a = (e[0] + e[20 * 64]) + (e[40 * 64] + e[60 * 64]);
            const int row = rt * 64 + m * 16 + (lane & 15);
            const int cl = n * 16 + 4 * g;
            float v0 = bf_lo(hh[j][0]) + (a[0] + bf_lo(bb[j][0]));
            float v1 = bf_hi(hh[j][0]) + (a[1] + bf_hi(bb[j][0]));
            float v2 = bf_lo(hh[j][1]) + (a[2] + bf_lo(bb[j][1]));
            float v3 = bf_hi(hh[j][1]) + (a[3] + bf_hi(bb[j][1]));
            if (cl < 72) *(u2v*)(out + (size_t)row * D_ + ct * 72 + cl) = u2v{pack_bf2(v0, v1), pack_bf2(v2, v3)};
        }
    }
}

extern "C" void kernel_launch(void* const* d_in, const int* in_sizes, int n_in,
                              void* d_out, int out_size, void* d_ws, size_t ws_size,
                              hipStream_t stream) {
    const bf16_t* x = (const bf16_t*)d_in[0];
    const bf16_t* wqkv = (const bf16_t*)d_in[1];
    const bf16_t* wo = (const bf16_t*)d_in[2];
    const bf16_t* w1 = (const bf16_t*)d_in[4];
    const bf16_t* b1 = (const bf16_t*)d_in[5];
    const bf16_t* w2 = (const bf16_t*)d_in[6];
    const bf16_t* b2 = (const bf16_t*)d_in[7];
    bf16_t* out = (bf16_t*)d_out;
    char* ws = (char*)d_ws;
    char* of = ws;
    char* wof = ws + (3 << 20);
    bf16_t* h = (bf16_t*)(ws + (4 << 20));
    char* hT = ws + (12 << 20);
    f2v* stats = (f2v*)(ws + (8 << 20));
    float* cs1 = (float*)(ws + (9 << 20));
    char* h1 = (char*)(ws + (16 << 20));
    char* w2f = ws + (48 << 20);

    hipFuncSetAttribute((const void*)k1_qkv_attn, hipFuncAttributeMaxDynamicSharedMemorySize, K1_LDS);
    hipFuncSetAttribute((const void*)k2_wo, hipFuncAttributeMaxDynamicSharedMemorySize, K2_LDS);
    hipFuncSetAttribute((const void*)k3_up, hipFuncAttributeMaxDynamicSharedMemorySize, K3_LDS);
    hipFuncSetAttribute((const void*)k4_down, hipFuncAttributeMaxDynamicSharedMemorySize, K4_LDS);

    k1_qkv_attn<<<256, 512, K1_LDS, stream>>>(x, wqkv, w1, wo, of, wof, cs1);
    k2_wo<<<256, 512, K2_LDS, stream>>>(of, wof, x, h, hT, stats);
    k3_up<<<256, 512, K3_LDS, stream>>>(hT, w1, b1, cs1, stats, w2, h1, w2f);
    k4_down<<<256, 512, K4_LDS, stream>>>(h1, w2f, h, b2, out);
}
```

```cpp
#include <hip/hip_runtime.h>
#include <hip/hip_bf16.h>

typedef __attribute__((ext_vector_type(8))) short s8v;
typedef __attribute__((ext_vector_type(4))) short s4v;
typedef __attribute__((ext_vector_type(4))) short v4i16_t;
typedef __attribute__((ext_vector_type(4))) float f4v;
typedef __attribute__((ext_vector_type(2))) float f2v;
typedef __attribute__((ext_vector_type(4))) unsigned u4v;
typedef __attribute__((ext_vector_type(2))) unsigned u2v;
typedef unsigned short bf16_t;

constexpr int S_ = 2048, D_ = 576, NQKV = 1728, F_ = 4608;

__device__ __forceinline__ unsigned lds_addr(const void* l) {
    return (unsigned)(size_t)(__attribute__((address_space(3))) const char*)l;
}
__device__ __forceinline__ void glds16(const void* g, unsigned dst) {
    unsigned keep;
    asm volatile("s_mov_b32 %0, m0\n\ts_mov_b32 m0, %2\n\ts_nop 0\n\tglobal_load_lds_dwordx4 %1, off\n\ts_mov_b32 m0, %0"
                 : "=&s"(keep) : "v"(g), "s"(dst) : "memory");
}
__device__ __forceinline__ void glds16s(const void* sbase, unsigned voff, unsigned dst) {
    unsigned keep;
    asm volatile("s_mov_b32 %0, m0\n\ts_mov_b32 m0, %3\n\ts_nop 0\n\tglobal_load_lds_dwordx4 %1, %2\n\ts_mov_b32 m0, %0"
                 : "=&s"(keep) : "v"(voff), "s"(sbase), "s"(dst) : "memory");
}
__device__ __forceinline__ s4v tr_read(const char* p) {
    return __builtin_bit_cast(s4v, __builtin_amdgcn_ds_read_tr16_b64_v4i16((__attribute__((address_space(3))) v4i16_t*)p));
}
__device__ __forceinline__ s8v cat8(s4v a, s4v b) {
    return __builtin_shufflevector(a, b, 0, 1, 2, 3, 4, 5, 6, 7);
}
__device__ __forceinline__ float bf_lo(unsigned u) { return __builtin_bit_cast(float, u << 16); }
__device__ __forceinline__ float bf_hi(unsigned u) { return __builtin_bit_cast(float, u & 0xffff0000u); }
__device__ __forceinline__ float bf2f(bf16_t v) { return __builtin_bit_cast(float, (unsigned)v << 16); }
__device__ __forceinline__ unsigned pack_bf2(float a, float b) {
    unsigned r;
    asm("v_cvt_pk_bf16_f32 %0, %1, %2" : "=v"(r) : "v"(a), "v"(b));
    return r;
}
#define MFMA16(a, b, c) __builtin_amdgcn_mfma_f32_16x16x32_bf16(a, b, c, 0, 0, 0)
#define WAIT_V(n) asm volatile("s_waitcnt vmcnt(" #n ")" ::: "memory")
#define WAIT_L(n) asm volatile("s_waitcnt lgkmcnt(" #n ")" ::: "memory")

#define SB0 __builtin_amdgcn_sched_barrier(0)
#define BAR __builtin_amdgcn_s_barrier()
constexpr int K1_XN_BYTES = 9 * 80 * 128;
constexpr int K1_STAGE = 14336;
constexpr int K1_NB = 4;
constexpr int K1_DUMMY = K1_XN_BYTES + K1_NB * K1_STAGE;
constexpr int K1_RED = K1_DUMMY + 4096;
constexpr int K1_LDS = K1_RED + 2048;

__global__ __launch_bounds__(512, 2) void k1_qkv_attn(const bf16_t* __restrict__ x, const bf16_t* __restrict__ wqkv,
                                                      const bf16_t* __restrict__ w1, const bf16_t* __restrict__ wo,
                                                      bf16_t* __restrict__ o,
                                                      float* __restrict__ cs1) {
    extern __shared__ __attribute__((aligned(16))) char smem[];
    const int tid = threadIdx.x, lane = tid & 63;
    const int wave = __builtin_amdgcn_readfirstlane(tid >> 6);
    const int xcd = blockIdx.x & 7, idx = blockIdx.x >> 3;
    const int hg = (xcd & 1) * 4 + (idx >> 3), rt = (xcd >> 1) * 8 + (idx & 7);
    const int s0 = rt * 64;
    const unsigned lring = lds_addr(smem) + K1_XN_BYTES;
    const int g = lane >> 4, q = (lane & 15) >> 2, p = lane & 3;

    const int lw = wave - 4;
    unsigned bsrc[4];
    auto issue = [&](int st) {
        int sc = st > 17 ? 17 : st;
        unsigned lb = lring + (st % K1_NB) * K1_STAGE;
        const char* gb = (const char*)wqkv + (size_t)sc * 32 * NQKV * 2;
#pragma unroll
        for (int i = 0; i < 4; i++) glds16s(gb, bsrc[i], lb + (lw * 4 + i > 13 ? 13 : lw * 4 + i) * 1024);
    };
    u4v cw[6];
    const int cu = blockIdx.x < 216 ? blockIdx.x : blockIdx.x - 216;
    const int cchunk = cu % 72, rthird = cu / 72;
    const unsigned dz = lds_addr(smem) + K1_DUMMY + (wave & 3) * 1024;
    if (wave >= 4) {
#pragma unroll
        for (int i = 0; i < 4; i++) {
            int bi = lw * 4 + i; if (bi > 13) bi = 13;
            int off = bi * 1024 + lane * 16;
            int k = off / 448, r = off - k * 448;
            int nt = (r >> 5) ^ ((k >> 3) & 1);
            int pc = nt * 2 + ((r >> 4) & 1); if (pc > 26) pc = 26;
            int sec = pc / 9, wi = pc - sec * 9;
            bsrc[i] = (unsigned)((k * NQKV + sec * 576 + hg * 72 + wi * 8) * 2);
        }
#pragma unroll
        for (int st = 0; st < K1_NB - 1; st++) issue(st);
#pragma unroll
        for (int j = 0; j < 8; j++) {
            int c = (lw * 8 + j) * 64 + lane; if (c > 1943) c = 1943;
            int seg = c / 9, pc = c - seg * 9;
            int sec = seg / 72, rr = seg - sec * 72;
            glds16s(wqkv, (unsigned)((((idx & 7) * 72 + rr) * NQKV + sec * 576 + hg * 72 + pc * 8) * 2), dz);
        }
    } else {
        u4v xv[3][9];
#pragma unroll
        for (int j = 0; j < 3; j++) {
            if (j < 2 || wave < 2) {
                int r = (4 * j + wave) * 8 + (lane >> 3);
                int s = s0 - 16 + r; if (s < 0) s = 0;
                const u4v* pp = (const u4v*)(x + (size_t)s * D_) + (lane & 7);
#pragma unroll
                for (int i = 0; i < 9; i++) xv[j][i] = pp[i * 8];
            }
            if (j == 0) {
            }
        }
#pragma unroll
        for (int j = 0; j < 3; j++) {
            if (j < 2 || wave < 2) {
                const int r = (4 * j + wave) * 8 + (lane >> 3);
                f2v v[36];
                f2v sm = f2v{0.f, 0.f};
#pragma unroll
                for (int i = 0; i < 9; i++)
#pragma unroll
                    for (int t = 0; t < 4; t++) { v[i * 4 + t] = f2v{bf_lo(xv[j][i][t]), bf_hi(xv[j][i][t])}; sm += v[i * 4 + t]; }
                float sum = sm[0] + sm[1];
                sum += __shfl_xor(sum, 1); sum += __shfl_xor(sum, 2); sum += __shfl_xor(sum, 4);
                const float mean = sum * (1.f / 576.f);
                f2v sq = f2v{0.f, 0.f};
#pragma unroll
                for (int i = 0; i < 36; i++) { v[i] = v[i] - mean; sq += v[i] * v[i]; }
                float sqs = sq[0] + sq[1];
                sqs += __shfl_xor(sqs, 1); sqs += __shfl_xor(sqs, 2); sqs += __shfl_xor(sqs, 4);
                const float rstd = rsqrtf(sqs * (1.f / 576.f) + 1e-5f);
#pragma unroll
                for (int i = 0; i < 9; i++) {
                    u4v ov;
#pragma unroll
                    for (int t = 0; t < 4; t++) { f2v w = v[i * 4 + t] * rstd; ov[t] = pack_bf2(w[0], w[1]); }
                    *(u4v*)(smem + i * (80 * 128) + r * 128 + (((lane & 7) ^ (r & 7)) << 4)) = ov;
                }
                if (j == 0) {
                }
                if (j == 1) {
                }
            }
        }
    }

    f4v acc[5][4];
    if (wave >= 4) {
        WAIT_V(16);
        __syncthreads();
        WAIT_V(12); BAR;
        issue(3); WAIT_V(12); BAR;
        for (int i = 1; i < 15; i++) {
            issue(i + K1_NB - 1);
            WAIT_V(4);
            BAR;
        }
        WAIT_V(0); BAR;
        {
            const u4v* pw = (const u4v*)(w1 + (size_t)(rthird * 192 + lw * 48 + (lane >> 3)) * F_ + cchunk * 64) + (lane & 7);
#pragma unroll
            for (int j = 0; j < 6; j++) cw[j] = pw[(size_t)j * 8 * F_ / 8];
            int c = blockIdx.x * 4 + lw; if (c > 647) c = 647;
            glds16s((const char*)wo + (size_t)c * 1024, (unsigned)(lane * 16), dz);
        }
        BAR; BAR;
    } else {
        const int nt0 = wave == 0 ? 0 : 1 + 3 * wave;
        const int nn = (wave == 0 || wave == 3) ? 4 : 3;
#pragma unroll
        for (int m = 0; m < 5; m++)
#pragma unroll
            for (int n = 0; n < 4; n++) acc[m][n] = f4v{0, 0, 0, 0};
        const int xr = lane & 15;
        int woff[4];
#pragma unroll
        for (int n = 0; n < 4; n++) woff[n] = K1_XN_BYTES + (8 * g + q) * 448 + (((nt0 + n) ^ (g & 1)) << 5) + 8 * p;
        s8v xa[5], xb[5], wf[4];
        auto rdx = [&](s8v* X, int st) {
            const char* xbp = smem + (st >> 1) * (80 * 128) + xr * 128 + ((((st & 1) * 4 + g) ^ (xr & 7)) << 4);
#pragma unroll
            for (int m = 0; m < 5; m++) X[m] = *(const s8v*)(xbp + m * 2048);
        };
        __syncthreads();
        rdx(xa, 0);
#pragma unroll
        for (int n = 0; n < 4; n++) wf[n] = cat8(tr_read(smem + woff[n]), tr_read(smem + woff[n] + 4 * 448));
        WAIT_L(0);
        BAR;
#define K1_STEP(XC, XN) { \
            const char* base = smem + ((i + 1) % K1_NB) * K1_STAGE; \
            rdx(XN, i + 1 > 17 ? 17 : i + 1); \
            SB0; \
            _Pragma("unroll") for (int n = 0; n < 4; n++) { \
                if (n < 3 || nn == 4) { \
                    if (wave != 0) acc[0][n] = MFMA16(XC[0], wf[n], acc[0][n]); \
                    _Pragma("unroll") for (int m = 1; m < 5; m++) acc[m][n] = MFMA16(XC[m], wf[n], acc[m][n]); \
                    SB0; \
                    wf[n] = cat8(tr_read(base + woff[n]), tr_read(base + woff[n] + 4 * 448)); \
                    SB0; \
                } \
            } \
            BAR; }
#pragma unroll 1
        for (int i = 0; i < 18; i += 2) {
            K1_STEP(xa, xb);
            i++; K1_STEP(xb, xa); i--;
        }
    }
    __syncthreads();
    constexpr int QS = 84;
    float* qkv = (float*)smem;
    if (wave < 4) {
        const int nt0 = wave == 0 ? 0 : 1 + 3 * wave;
        const int nn = (wave == 0 || wave == 3) ? 4 : 3;
#pragma unroll
        for (int m = 0; m < 5; m++)
#pragma unroll
            for (int n = 0; n < 4; n++) {
                int col = (nt0 + n) * 16 + (lane & 15);
                if (n < nn && col < 216) *(f4v*)(qkv + col * QS + m * 16 + 4 * g) = acc[m][n];
            }
    }
    __syncthreads();
    {
        const int head = wave >> 1;
        const int rl = (wave & 1) * 32 + (lane & 31);
        const int hw = lane >> 5;
        const int r = 16 + rl;
        const int s = s0 + rl;
        const float* qp = qkv + (head * 18) * QS + r;
        const float* kp = qkv + (72 + head * 18) * QS + r - 8 * hw;
        const float* vp = qkv + (144 + head * 18) * QS + r - 8 * hw;
        float qv[18];
#pragma unroll
        for (int d = 0; d < 18; d++) qv[d] = qp[d * QS];
        float lg[4];
        float mx = -3.0e38f;
#pragma unroll
        for (int w = 0; w < 4; w++) {
            float a = 0.f;
#pragma unroll
            for (int d = 0; d < 18; d++) a += qv[d] * kp[d * QS - 2 * w];
            a *= 0.23570226039551584f;
            lg[w] = (s - 2 * (w + 4 * hw) >= 0) ? a : -1e9f;
            mx = fmaxf(mx, lg[w]);
        }
        mx = fmaxf(mx, __shfl_xor(mx, 32));
        float den = 0.f;
#pragma unroll
        for (int w = 0; w < 4; w++) { lg[w] = __expf(lg[w] - mx); den += lg[w]; }
        den += __shfl_xor(den, 32);
        float inv = 1.f / den;
        float ov[18];
#pragma unroll
        for (int d = 0; d < 18; d++) ov[d] = 0.f;
#pragma unroll
        for (int w = 0; w < 4; w++) {
            float pw = lg[w] * inv;
#pragma unroll
            for (int d = 0; d < 18; d++) ov[d] += pw * vp[d * QS - 2 * w];
        }
#pragma unroll
        for (int d = 0; d < 18; d++) ov[d] += __shfl_xor(ov[d], 32);
        unsigned* op = (unsigned*)(o + (size_t)s * D_ + hg * 72 + head * 18);
        if (hw == 0) {
#pragma unroll
            for (int d = 0; d < 5; d++) op[d] = pack_bf2(ov[2 * d], ov[2 * d + 1]);
        } else {
#pragma unroll
            for (int d = 5; d < 9; d++) op[d] = pack_bf2(ov[2 * d], ov[2 * d + 1]);
        }
    }
    if (wave >= 4) {
        float a[8];
#pragma unroll
        for (int j = 0; j < 8; j++) a[j] = 0.f;
#pragma unroll
        for (int i = 0; i < 6; i++)
#pragma unroll
            for (int j = 0; j < 4; j++) { a[2 * j] += bf_lo(cw[i][j]); a[2 * j + 1] += bf_hi(cw[i][j]); }
#pragma unroll
        for (int j = 0; j < 8; j++) {
            float t = a[j];
            t += __shfl_xor(t, 8); t += __shfl_xor(t, 16); t += __shfl_xor(t, 32);
            a[j] = t;
        }
        if (lane < 8) {
            float* rq = (float*)(smem + K1_RED) + lw * 64 + lane * 8;
            *(f4v*)rq = f4v{a[0], a[1], a[2], a[3]};
            *(f4v*)(rq + 4) = f4v{a[4], a[5], a[6], a[7]};
        }
        WAIT_V(0);
    }
    __syncthreads();
    if (wave == 7 && blockIdx.x < 216) {
        const float* rq = (const float*)(smem + K1_RED) + lane;
        cs1[rthird * F_ + cchunk * 64 + lane] = (rq[0] + rq[64]) + (rq[128] + rq[192]);
    }
}

constexpr int K2_CH = 8192 + 64 * 144;
constexpr int K2_LDS = 9 * K2_CH + 64;

__global__ __launch_bounds__(512, 2) void k2_wo(const bf16_t* __restrict__ o, const bf16_t* __restrict__ wo,
                                                const bf16_t* __restrict__ x, bf16_t* __restrict__ h,
                                                char* __restrict__ hT, f2v* __restrict__ stats) {
    extern __shared__ __attribute__((aligned(16))) char smem[];
    const int tid = threadIdx.x, lane = tid & 63;
    const int wave = __builtin_amdgcn_readfirstlane(tid >> 6);
    const int rt = (blockIdx.x & 7) * 4 + ((blockIdx.x >> 3) & 3), ct = blockIdx.x >> 5;
    const int r0 = rt * 64, c0 = ct * 72;
    const int g = lane >> 4, q = (lane & 15) >> 2, p = lane & 3;
    if (wave >= 4) {
        const int lw = wave - 4;
        const unsigned lbase = lds_addr(smem);
        unsigned asrc[2], bsrc[3];
#pragma unroll
        for (int i = 0; i < 2; i++) {
            int a = lw * 2 + i;
            int row = a * 8 + (lane >> 3);
            int ch = (lane & 7) ^ (row & 7);
            asrc[i] = (unsigned)(((r0 + row) * D_ + ch * 8) * 2);
        }
#pragma unroll
        for (int i = 0; i < 3; i++) {
            int bi = lw + 4 * i; if (bi > 8) bi = 8;
            int ci = bi * 64 + lane;
            int k = ci / 9, c = ci - k * 9;
            bsrc[i] = (unsigned)((k * D_ + c0 + c * 8) * 2);
        }
#pragma unroll
        for (int c = 0; c < 9; c++) {
            unsigned lb = lbase + c * K2_CH;
            glds16s((const char*)o + c * 128, asrc[0], lb + (lw * 2) * 1024);
            glds16s((const char*)o + c * 128, asrc[1], lb + (lw * 2 + 1) * 1024);
#pragma unroll
            for (int i = 0; i < 3; i++) glds16s((const char*)wo + (size_t)c * 64 * D_ * 2, bsrc[i], lb + 8192 + (lw + 4 * i > 8 ? 8 : lw + 4 * i) * 1024);
        }
        WAIT_V(40); BAR;
        WAIT_V(35); BAR;
        WAIT_V(30); BAR;
        WAIT_V(25); BAR;
        WAIT_V(20); BAR;
        WAIT_V(15); BAR;
        WAIT_V(10); BAR;
        WAIT_V(5); BAR;
        WAIT_V(0); BAR;
        return;
    }
    const int xr = wave * 16 + (lane & 15);
    const int row = r0 + xr;
    u2v xx[5];
#pragma unroll
    for (int n = 0; n < 5; n++) {
        int col = n * 16 + 4 * g; if (col > 68) col = 68;
        xx[n] = *(const u2v*)(x + (size_t)row * D_ + c0 + col);
    }
    f4v acc[5];
#pragma unroll
    for (int n = 0; n < 5; n++) acc[n] = f4v{0, 0, 0, 0};
    const int boff = 8192 + (8 * g + q) * 144 + 8 * p;
#pragma unroll 1
    for (int c = 0; c < 9; c++) {
        BAR;
        const char* base = smem + c * K2_CH;
        s8v xf[2], wf[2][5];
#pragma unroll
        for (int ss = 0; ss < 2; ss++) {
            xf[ss] = *(const s8v*)(base + xr * 128 + (((ss * 4 + g) ^ (xr & 7)) << 4));
#pragma unroll
            for (int n = 0; n < 5; n++) {
                const char* wb = base + boff + ss * 32 * 144 + n * 32;
                wf[ss][n] = cat8(tr_read(wb), tr_read(wb + 4 * 144));
            }
        }
#pragma unroll
        for (int ss = 0; ss < 2; ss++)
#pragma unroll
            for (int n = 0; n < 5; n++) acc[n] = MFMA16(wf[ss][n], xf[ss], acc[n]);
    }
    float hv[5][4];
    float sum = 0.f;
#pragma unroll
    for (int n = 0; n < 5; n++) {
        int col = n * 16 + 4 * g;
        if (col < 72) {
            unsigned p0 = pack_bf2(bf_lo(xx[n][0]) + acc[n][0], bf_hi(xx[n][0]) + acc[n][1]);
            unsigned p1 = pack_bf2(bf_lo(xx[n][1]) + acc[n][2], bf_hi(xx[n][1]) + acc[n][3]);
            *(u2v*)(h + (size_t)row * D_ + c0 + col) = u2v{p0, p1};
            {
                int cc = c0 + col;
                *(u2v*)(hT + (size_t)((row >> 4) * 18 + (cc >> 5)) * 1024 + (((cc & 31) >> 3) * 16 + (row & 15)) * 16 + (cc & 7) * 2) = u2v{p0, p1};
            }
            hv[n][0] = bf_lo(p0); hv[n][1] = bf_hi(p0); hv[n][2] = bf_lo(p1); hv[n][3] = bf_hi(p1);
            sum += hv[n][0] + hv[n][1] + hv[n][2] + hv[n][3];
        } else {
            hv[n][0] = hv[n][1] = hv[n][2] = hv[n][3] = 0.f;
        }
    }
    sum += __shfl_xor(sum, 16); sum += __shfl_xor(sum, 32);
    float mean = sum * (1.f / 72.f);
    float m2 = 0.f;
#pragma unroll
    for (int n = 0; n < 5; n++) {
        int col = n * 16 + 4 * g;
        if (col < 72) {
#pragma unroll
            for (int j = 0; j < 4; j++) { float d = hv[n][j] - mean; m2 += d * d; }
        }
    }
    m2 += __shfl_xor(m2, 16); m2 += __shfl_xor(m2, 32);
    if (g == 0) stats[(size_t)row * 8 + ct] = f2v{sum, m2};
}

constexpr int K3_STAGE = 8192 + 18432;
constexpr int K3_NB = 5;
constexpr int K3_PAR = K3_NB * K3_STAGE;
constexpr int K3_LDS = K3_PAR + 1024 + 2304;

__device__ __forceinline__ float gelu_tanh(float v) {
    float t = v * v;
    float z = v * (-2.302208198f * 0.044715f * t - 2.302208198f);
    float e = __builtin_amdgcn_exp2f(z);
    return v * __builtin_amdgcn_rcpf(1.f + e);
}

__device__ __forceinline__ f2v gelu_tanh2(f2v v) {
    f2v t = v * v;
    f2v z = v * (t * (-2.302208198f * 0.044715f) + (-2.302208198f));
    f2v d = f2v{__builtin_amdgcn_exp2f(z[0]), __builtin_amdgcn_exp2f(z[1])} + 1.f;
    return v * f2v{__builtin_amdgcn_rcpf(d[0]), __builtin_amdgcn_rcpf(d[1])};
}

__global__ __launch_bounds__(512, 2) void k3_up(const char* __restrict__ hT, const bf16_t* __restrict__ w1,
                                                const bf16_t* __restrict__ b1, const float* __restrict__ cs1,
                                                const f2v* __restrict__ stats, const bf16_t* __restrict__ w2,
                                                char* __restrict__ h1f, char* __restrict__ w2f) {
    extern __shared__ __attribute__((aligned(16))) char smem[];
    const int tid = threadIdx.x, lane = tid & 63;
    const int wave = __builtin_amdgcn_readfirstlane(tid >> 6);
    const int xcd = blockIdx.x & 7, idx = blockIdx.x >> 3;
    const int rt = (xcd & 1) * 8 + (idx & 7), ct = (xcd >> 1) * 4 + (idx >> 3);
    const int r0 = rt * 128, c0 = ct * 288;
    const unsigned lbase = lds_addr(smem);
    const int g = lane >> 4, q = (lane & 15) >> 2, p = lane & 3;
    const int xr = lane & 15;
    f2v* mu = (f2v*)(smem + K3_PAR);
    float* csl = (float*)(smem + K3_PAR + 1024);
    float* bbl = csl + 288;
    f4v acc[4][9];
    const int wm = (wave >> 1) & 1, wn = wave & 1;
    if (wave >= 4) {
        const int lw = wave - 4;
        unsigned bsrc[5];
        const unsigned avo = lane * 16;
        const char* ag = hT + (size_t)(rt * 8 + lw * 2) * 18 * 1024;
#pragma unroll
        for (int i = 0; i < 5; i++) {
            int bi = lw * 5 + i; if (bi > 17) bi = 17;
            int off = bi * 1024 + lane * 16;
            int k = off / 576, r = off - k * 576;
            int nt = (r >> 5) ^ ((k >> 3) & 1);
            bsrc[i] = (unsigned)((k * F_ + c0 + nt * 16 + ((r >> 4) & 1) * 8) * 2);
        }
        auto issue = [&](int st) {
            int sc = st > 17 ? 17 : st;
            unsigned lb = lbase + (st % K3_NB) * K3_STAGE;
            const char* ga = ag + sc * 1024;
            const char* gb = (const char*)w1 + (size_t)sc * 32 * F_ * 2;
            glds16s(ga, avo, lb + (lw * 2) * 1024);
            glds16s(ga + 18 * 1024, avo, lb + (lw * 2 + 1) * 1024);
#pragma unroll
            for (int i = 0; i < 5; i++) glds16s(gb, bsrc[i], lb + 8192 + (lw * 5 + i > 17 ? 17 : lw * 5 + i) * 1024);
        };
#pragma unroll
        for (int st = 0; st < K3_NB - 1; st++) issue(st);
        WAIT_V(21);
        BAR;
        for (int i = -1; i < 18; i++) {
            if (i >= 0) issue(i + K3_NB - 1);
            WAIT_V(14);
            BAR;
        }
        WAIT_V(0);
    } else {
        {
            float c0v = (cs1[c0 + tid] + cs1[F_ + c0 + tid]) + cs1[2 * F_ + c0 + tid], b0v = bf2f(b1[c0 + tid]);
            float c1v = 0.f, b1v = 0.f;
            if (tid < 32) { c1v = (cs1[c0 + 256 + tid] + cs1[F_ + c0 + 256 + tid]) + cs1[2 * F_ + c0 + 256 + tid]; b1v = bf2f(b1[c0 + 256 + tid]); }
            if (tid < 128) {
                const f4v* sp = (const f4v*)(stats + (size_t)(r0 + tid) * 8);
                f4v v[4];
#pragma unroll
                for (int i = 0; i < 4; i++) v[i] = sp[i];
                float tot = 0.f;
#pragma unroll
                for (int i = 0; i < 4; i++) tot += v[i][0] + v[i][2];
                float mean = tot * (1.f / 576.f);
                float M2 = 0.f;
#pragma unroll
                for (int i = 0; i < 4; i++) {
                    float d0 = v[i][0] * (1.f / 72.f) - mean, d1 = v[i][2] * (1.f / 72.f) - mean;
                    M2 += v[i][1] + v[i][3] + 72.f * (d0 * d0 + d1 * d1);
                }
                float rstd = rsqrtf(M2 * (1.f / 576.f) + 1e-5f);
                mu[tid] = f2v{mean * rstd, rstd};
            }
            csl[tid] = c0v; bbl[tid] = b0v;
            if (tid < 32) { csl[256 + tid] = c1v; bbl[256 + tid] = b1v; }
        }
#pragma unroll
        for (int m = 0; m < 4; m++)
#pragma unroll
            for (int n = 0; n < 9; n++) acc[m][n] = f4v{0, 0, 0, 0};
        const int dlt = (g & 1) ? 32 : 0;
        const int wb0 = 8192 + (8 * g + q) * 576 + wn * 9 * 32 + 8 * p;
        const int wbe = wb0 + ((wn & 1) ? -dlt : dlt);
        const int wbo = wb0 + ((wn & 1) ? dlt : -dlt);
        const int xoff = wm * 4096 + lane * 16;
        s8v xa[4], xb[4], wf[9];
        BAR;
        {
            const char* base = smem;
#pragma unroll
            for (int m = 0; m < 4; m++) xa[m] = *(const s8v*)(base + xoff + m * 1024);
#pragma unroll
            for (int n = 0; n < 9; n++) {
                const char* wb = base + ((n & 1) ? wbo : wbe) + n * 32;
                wf[n] = cat8(tr_read(wb), tr_read(wb + 4 * 576));
            }
        }
        WAIT_L(0);
        BAR;
#define K3_STEP(XC, XN) { \
            const char* base = smem + ((i + 1) % K3_NB) * K3_STAGE; \
            s4v wlo, whi; \
            _Pragma("unroll") for (int n = 0; n < 9; n++) { \
                const char* wbp = base + (((n - 1) & 1) ? wbo : wbe) + (n - 1) * 32; \
                acc[0][n] = MFMA16(wf[n], XC[0], acc[0][n]); SB0; \
                if (n > 0) { wlo = tr_read(wbp); SB0; } \
                acc[1][n] = MFMA16(wf[n], XC[1], acc[1][n]); SB0; \
                if (n > 0) { whi = tr_read(wbp + 4 * 576); wf[n - 1] = cat8(wlo, whi); SB0; } \
                acc[2][n] = MFMA16(wf[n], XC[2], acc[2][n]); SB0; \
                if (n < 4) { XN[n] = *(const s8v*)(base + xoff + n * 1024); SB0; } \
                acc[3][n] = MFMA16(wf[n], XC[3], acc[3][n]); SB0; \
            } \
            { const char* wbp = base + wbe + 8 * 32; wf[8] = cat8(tr_read(wbp), tr_read(wbp + 4 * 576)); } \
            BAR; }
#pragma unroll 1
        for (int i = 0; i < 18; i += 2) {
            K3_STEP(xa, xb);
            i++; K3_STEP(xb, xa); i--;
        }
    }
    __syncthreads();
    const int u = wave * 256 + blockIdx.x;
    if (u < 1152) {
        const int ctp = u / 144, kst = u - ctp * 144;
#pragma unroll
        for (int j = 0; j < 5; j++) {
            int c = j * 64 + lane; if (c > 287) c = 287;
            int row = c / 9, pc = c - row * 9;
            glds16s(w2, (unsigned)(((kst * 32 + row) * D_ + ctp * 72 + pc * 8) * 2), lbase + 98304 + wave * 5120 + j * 1024);
        }
    }
    f4v* ex = (f4v*)smem + (wave & 3) * (18 * 64) + lane;
    if (wave < 4) {
#pragma unroll
        for (int m = 0; m < 2; m++)
#pragma unroll
            for (int n = 0; n < 9; n++) ex[(m * 9 + n) * 64] = acc[m + 2][n];
    }
    __syncthreads();
    if (wave >= 4) {
#pragma unroll
        for (int m = 0; m < 2; m++)
#pragma unroll
            for (int n = 0; n < 9; n++) acc[m][n] = ex[(m * 9 + n) * 64];
    }
    {
        const int mb = (wave >> 2) * 2;
#pragma unroll
        for (int m = 0; m < 2; m++) {
            int rl = wm * 64 + (mb + m) * 16 + xr;
            f2v ms = mu[rl];
            char* ob = h1f + (size_t)((r0 + wm * 64 + (mb + m) * 16) >> 4) * (144 * 1024) + lane * 16;
            u2v pv[9];
#pragma unroll
            for (int n = 0; n < 9; n++) {
                f4v a = acc[m][n];
                int cl = wn * 144 + n * 16 + 4 * g;
                f4v cs = *(const f4v*)(csl + cl), bb = *(const f4v*)(bbl + cl);
                f2v t01 = f2v{bb[0], bb[1]} - f2v{cs[0], cs[1]} * ms[0];
                f2v t23 = f2v{bb[2], bb[3]} - f2v{cs[2], cs[3]} * ms[0];
                f2v v01 = gelu_tanh2(f2v{a[0], a[1]} * ms[1] + t01);
                f2v v23 = gelu_tanh2(f2v{a[2], a[3]} * ms[1] + t23);
                pv[n] = u2v{pack_bf2(v01[0], v01[1]), pack_bf2(v23[0], v23[1])};
            }
            if (m == 0) {
            } else {
            }
            char* oc = ob + (c0 >> 5) * 1024;
            if (wn == 0) {
#pragma unroll
                for (int j = 0; j < 4; j++) *(u4v*)(oc + j * 1024) = u4v{pv[2 * j][0], pv[2 * j][1], pv[2 * j + 1][0], pv[2 * j + 1][1]};
                *(u2v*)(oc + 4 * 1024) = pv[8];
            } else {
                *(u2v*)(oc + 4 * 1024 + 8) = pv[0];
#pragma unroll
                for (int j = 0; j < 4; j++) *(u4v*)(oc + (5 + j) * 1024) = u4v{pv[2 * j + 1][0], pv[2 * j + 1][1], pv[2 * j + 2][0], pv[2 * j + 2][1]};
            }
        }
    }
    WAIT_V(0);
    if (u < 1152) {
        const char* sb = smem + 98304 + wave * 5120 + (4 * g + q) * 144 + 8 * p;
        char* dst = w2f + (size_t)u * 5 * 1024 + lane * 16;
#pragma unroll
        for (int nt = 0; nt < 5; nt++)
            *(s8v*)(dst + nt * 1024) = cat8(tr_read(sb + nt * 32), tr_read(sb + 16 * 144 + nt * 32));
    }
}

constexpr int K4_LDS = 4 * 20 * 1024;
constexpr int K4_D = 3;

__global__ __launch_bounds__(512, 2) void k4_down(const char* __restrict__ h1f, const char* __restrict__ w2f,
                                                  const bf16_t* __restrict__ h, const bf16_t* __restrict__ b2,
                                                  bf16_t* __restrict__ out) {
    extern __shared__ __attribute__((aligned(16))) char smem[];
    const int tid = threadIdx.x, lane = tid & 63;
    const int wave = __builtin_amdgcn_readfirstlane(tid >> 6);
    const int xcd = blockIdx.x & 7, idx = blockIdx.x >> 3;
    const int rt = (xcd >> 1) * 8 + (idx & 7), ct = (xcd & 1) * 4 + (idx >> 3);
    const int g = lane >> 4;
    const char* pa = h1f + ((size_t)rt * 4 * 144 + wave) * 1024 + lane * 16;
    const char* pw = w2f + ((size_t)ct * 144 + wave) * 5 * 1024 + lane * 16;
    f4v acc[4][5];
#pragma unroll
    for (int m = 0; m < 4; m++)
#pragma unroll
        for (int n = 0; n < 5; n++) acc[m][n] = f4v{0, 0, 0, 0};
    s8v fa[K4_D][4], fw[K4_D][5];
#pragma unroll
    for (int j = 0; j < K4_D; j++) {
#pragma unroll
        for (int m = 0; m < 4; m++) fa[j][m] = *(const s8v*)(pa + m * (144 * 1024) + j * 8192);
#pragma unroll
        for (int n = 0; n < 5; n++) fw[j][n] = *(const s8v*)(pw + n * 1024 + j * 40960);
        SB0;
    }
#pragma unroll
    for (int j = 0; j < 18; j++) {
        const int sl = j % K4_D;
        SB0;
#pragma unroll
        for (int n = 0; n < 5; n++)
#pragma unroll
            for (int m = 0; m < 4; m++) acc[m][n] = MFMA16(fw[sl][n], fa[sl][m], acc[m][n]);
        SB0;
        if (j + K4_D < 18) {
#pragma unroll
            for (int m = 0; m < 4; m++) fa[sl][m] = *(const s8v*)(pa + m * (144 * 1024) + (j + K4_D) * 8192);
#pragma unroll
            for (int n = 0; n < 5; n++) fw[sl][n] = *(const s8v*)(pw + n * 1024 + (j + K4_D) * 40960);
        }
        SB0;
    }
    u2v hh[3], bb[3];
#pragma unroll
    for (int j = 0; j < 3; j++) {
        int t = wave + 8 * j; if (t > 19) t = 19;
        const int m = t / 5, n = t - m * 5;
        const int row = rt * 64 + m * 16 + (lane & 15);
        int cl = n * 16 + 4 * g; if (cl > 68) cl = 68;
        hh[j] = *(const u2v*)(h + (size_t)row * D_ + ct * 72 + cl);
        bb[j] = *(const u2v*)(b2 + ct * 72 + cl);
    }
    f4v* ex = (f4v*)smem + (wave & 3) * (20 * 64) + lane;
    if (wave >= 4) {
#pragma unroll
        for (int m = 0; m < 4; m++)
#pragma unroll
            for (int n = 0; n < 5; n++) ex[(m * 5 + n) * 64] = acc[m][n];
    }
    __syncthreads();
    if (wave < 4) {
#pragma unroll
        for (int m = 0; m < 4; m++)
#pragma unroll
            for (int n = 0; n < 5; n++) { f4v v = ex[(m * 5 + n) * 64]; ex[(m * 5 + n) * 64] = acc[m][n] + v; }
    }
    __syncthreads();
#pragma unroll
    for (int j = 0; j < 3; j++) {
        const int t = wave + 8 * j;
        if (t < 20) {
            const int m = t / 5, n = t - m * 5;
            const f4v* e = (const f4v*)smem + t * 64 + lane;
            f4v a = (e[0] + e[20 * 64]) + (e[40 * 64] + e[60 * 64]);
            const int row = rt * 64 + m * 16 + (lane & 15);
            const int cl = n * 16 + 4 * g;
            float v0 = bf_lo(hh[j][0]) + (a[0] + bf_lo(bb[j][0]));
            float v1 = bf_hi(hh[j][0]) + (a[1] + bf_hi(bb[j][0]));
            float v2 = bf_lo(hh[j][1]) + (a[2] + bf_lo(bb[j][1]));
            float v3 = bf_hi(hh[j][1]) + (a[3] + bf_hi(bb[j][1]));
            if (cl < 72) *(u2v*)(out + (size_t)row * D_ + ct * 72 + cl) = u2v{pack_bf2(v0, v1), pack_bf2(v2, v3)};
        }
    }
}

extern "C" void kernel_launch(void* const* d_in, const int* in_sizes, int n_in,
                              void* d_out, int out_size, void* d_ws, size_t ws_size,
                              hipStream_t stream) {
    const bf16_t* x = (const bf16_t*)d_in[0];
    const bf16_t* wqkv = (const bf16_t*)d_in[1];
    const bf16_t* wo = (const bf16_t*)d_in[2];
    const bf16_t* w1 = (const bf16_t*)d_in[4];
    const bf16_t* b1 = (const bf16_t*)d_in[5];
    const bf16_t* w2 = (const bf16_t*)d_in[6];
    const bf16_t* b2 = (const bf16_t*)d_in[7];
    bf16_t* out = (bf16_t*)d_out;
    char* ws = (char*)d_ws;
    bf16_t* o = (bf16_t*)(ws);
    bf16_t* h = (bf16_t*)(ws + (4 << 20));
    char* hT = ws + (12 << 20);
    f2v* stats = (f2v*)(ws + (8 << 20));
    float* cs1 = (float*)(ws + (9 << 20));
    char* h1 = (char*)(ws + (16 << 20));
    char* w2f = ws + (48 << 20);

    hipFuncSetAttribute((const void*)k1_qkv_attn, hipFuncAttributeMaxDynamicSharedMemorySize, K1_LDS);
    hipFuncSetAttribute((const void*)k2_wo, hipFuncAttributeMaxDynamicSharedMemorySize, K2_LDS);
    hipFuncSetAttribute((const void*)k3_up, hipFuncAttributeMaxDynamicSharedMemorySize, K3_LDS);
    hipFuncSetAttribute((const void*)k4_down, hipFuncAttributeMaxDynamicSharedMemorySize, K4_LDS);

    k1_qkv_attn<<<256, 512, K1_LDS, stream>>>(x, wqkv, w1, wo, o, cs1);
    k2_wo<<<256, 512, K2_LDS, stream>>>(o, wo, x, h, hT, stats);
    k3_up<<<256, 512, K3_LDS, stream>>>(hT, w1, b1, cs1, stats, w2, h1, w2f);
    k4_down<<<256, 512, K4_LDS, stream>>>(h1, w2f, h, b2, out);
}
```

```cpp
#include <hip/hip_runtime.h>
#include <hip/hip_bf16.h>

typedef __attribute__((ext_vector_type(8))) short s8v;
typedef __attribute__((ext_vector_type(4))) short s4v;
typedef __attribute__((ext_vector_type(4))) short v4i16_t;
typedef __attribute__((ext_vector_type(4))) float f4v;
typedef __attribute__((ext_vector_type(2))) float f2v;
typedef __attribute__((ext_vector_type(4))) unsigned u4v;
typedef __attribute__((ext_vector_type(2))) unsigned u2v;
typedef unsigned short bf16_t;

constexpr int S_ = 2048, D_ = 576, NQKV = 1728, F_ = 4608;

__device__ __forceinline__ unsigned lds_addr(const void* l) {
    return (unsigned)(size_t)(__attribute__((address_space(3))) const char*)l;
}
__device__ __forceinline__ void glds16(const void* g, unsigned dst) {
    unsigned keep;
    asm volatile("s_mov_b32 %0, m0\n\ts_mov_b32 m0, %2\n\ts_nop 0\n\tglobal_load_lds_dwordx4 %1, off\n\ts_mov_b32 m0, %0"
                 : "=&s"(keep) : "v"(g), "s"(dst) : "memory");
}
__device__ __forceinline__ void glds16s(const void* sbase, unsigned voff, unsigned dst) {
    unsigned keep;
    asm volatile("s_mov_b32 %0, m0\n\ts_mov_b32 m0, %3\n\ts_nop 0\n\tglobal_load_lds_dwordx4 %1, %2\n\ts_mov_b32 m0, %0"
                 : "=&s"(keep) : "v"(voff), "s"(sbase), "s"(dst) : "memory");
}
__device__ __forceinline__ s4v tr_read(const char* p) {
    return __builtin_bit_cast(s4v, __builtin_amdgcn_ds_read_tr16_b64_v4i16((__attribute__((address_space(3))) v4i16_t*)p));
}
__device__ __forceinline__ s8v cat8(s4v a, s4v b) {
    return __builtin_shufflevector(a, b, 0, 1, 2, 3, 4, 5, 6, 7);
}
__device__ __forceinline__ float bf_lo(unsigned u) { return __builtin_bit_cast(float, u << 16); }
__device__ __forceinline__ float bf_hi(unsigned u) { return __builtin_bit_cast(float, u & 0xffff0000u); }
__device__ __forceinline__ float bf2f(bf16_t v) { return __builtin_bit_cast(float, (unsigned)v << 16); }
__device__ __forceinline__ unsigned pack_bf2(float a, float b) {
    unsigned r;
    asm("v_cvt_pk_bf16_f32 %0, %1, %2" : "=v"(r) : "v"(a), "v"(b));
    return r;
}
#define MFMA16(a, b, c) __builtin_amdgcn_mfma_f32_16x16x32_bf16(a, b, c, 0, 0, 0)
#define WAIT_V(n) asm volatile("s_waitcnt vmcnt(" #n ")" ::: "memory")
#define WAIT_L(n) asm volatile("s_waitcnt lgkmcnt(" #n ")" ::: "memory")

#define SB0 __builtin_amdgcn_sched_barrier(0)
#define BAR __builtin_amdgcn_s_barrier()
constexpr int K1_XN_BYTES = 9 * 80 * 128;
constexpr int K1_STAGE = 14336;
constexpr int K1_NB = 4;
constexpr int K1_DUMMY = K1_XN_BYTES + K1_NB * K1_STAGE;
constexpr int K1_RED = K1_DUMMY + 5120;
constexpr int K1_LDS = K1_RED + 2048;

__global__ __launch_bounds__(512, 2) void k1_qkv_attn(const bf16_t* __restrict__ x, const bf16_t* __restrict__ wqkv,
                                                      const bf16_t* __restrict__ w1, const bf16_t* __restrict__ wo,
                                                      char* __restrict__ of, char* __restrict__ wof,
                                                      float* __restrict__ cs1) {
    extern __shared__ __attribute__((aligned(16))) char smem[];
    const int tid = threadIdx.x, lane = tid & 63;
    const int wave = __builtin_amdgcn_readfirstlane(tid >> 6);
    const int xcd = blockIdx.x & 7, idx = blockIdx.x >> 3;
    const int hg = (xcd & 1) * 4 + (idx >> 3), rt = (xcd >> 1) * 8 + (idx & 7);
    const int s0 = rt * 64;
    const unsigned lring = lds_addr(smem) + K1_XN_BYTES;
    const int g = lane >> 4, q = (lane & 15) >> 2, p = lane & 3;

    const int lw = wave - 4;
    unsigned bsrc[4];
    auto issue = [&](int st) {
        int sc = st > 17 ? 17 : st;
        unsigned lb = lring + (st % K1_NB) * K1_STAGE;
        const char* gb = (const char*)wqkv + (size_t)sc * 32 * NQKV * 2;
#pragma unroll
        for (int i = 0; i < 4; i++) glds16s(gb, bsrc[i], lb + (lw * 4 + i > 13 ? 13 : lw * 4 + i) * 1024);
    };
    u4v cw[6];
    const int cu = blockIdx.x < 216 ? blockIdx.x : blockIdx.x - 216;
    const int cchunk = cu % 72, rthird = cu / 72;
    const unsigned dz = lds_addr(smem) + K1_DUMMY + (wave & 3) * 1024;
    auto ln_pass = [&](const u4v* xx, int r) {
        f2v v[36];
        f2v sm = f2v{0.f, 0.f};
#pragma unroll
        for (int i = 0; i < 9; i++)
#pragma unroll
            for (int t = 0; t < 4; t++) { v[i * 4 + t] = f2v{bf_lo(xx[i][t]), bf_hi(xx[i][t])}; sm += v[i * 4 + t]; }
        float sum = sm[0] + sm[1];
        sum += __shfl_xor(sum, 1); sum += __shfl_xor(sum, 2); sum += __shfl_xor(sum, 4);
        const float mean = sum * (1.f / 576.f);
        f2v sq = f2v{0.f, 0.f};
#pragma unroll
        for (int i = 0; i < 36; i++) { v[i] = v[i] - mean; sq += v[i] * v[i]; }
        float sqs = sq[0] + sq[1];
        sqs += __shfl_xor(sqs, 1); sqs += __shfl_xor(sqs, 2); sqs += __shfl_xor(sqs, 4);
        const float rstd = rsqrtf(sqs * (1.f / 576.f) + 1e-5f);
#pragma unroll
        for (int i = 0; i < 9; i++) {
            u4v ov;
#pragma unroll
            for (int t = 0; t < 4; t++) { f2v w = v[i * 4 + t] * rstd; ov[t] = pack_bf2(w[0], w[1]); }
            *(u4v*)(smem + i * (80 * 128) + r * 128 + (((lane & 7) ^ (r & 7)) << 4)) = ov;
        }
    };
    if (wave >= 4) {
        u4v xv[9];
        {
            const int r = wave * 8 + (lane >> 3);
            const char* pp = (const char*)(x + (size_t)(s0 - 16 + r) * D_) + (lane & 7) * 16;
#pragma unroll
            for (int i = 0; i < 9; i++)
                asm volatile("global_load_dwordx4 %0, %1, off offset:%2" : "=v"(xv[i]) : "v"(pp), "n"(i * 128) : "memory");
        }
#pragma unroll
        for (int i = 0; i < 4; i++) {
            int bi = lw * 4 + i; if (bi > 13) bi = 13;
            int off = bi * 1024 + lane * 16;
            int k = off / 448, r = off - k * 448;
            int nt = (r >> 5) ^ ((k >> 3) & 1);
            int pc = nt * 2 + ((r >> 4) & 1); if (pc > 26) pc = 26;
            int sec = pc / 9, wi = pc - sec * 9;
            bsrc[i] = (unsigned)((k * NQKV + sec * 576 + hg * 72 + wi * 8) * 2);
        }
#pragma unroll
        for (int st = 0; st < K1_NB - 1; st++) issue(st);
#pragma unroll
        for (int j = 0; j < 8; j++) {
            int c = (lw * 8 + j) * 64 + lane; if (c > 1943) c = 1943;
            int seg = c / 9, pc = c - seg * 9;
            int sec = seg / 72, rr = seg - sec * 72;
            glds16s(wqkv, (unsigned)((((idx & 7) * 72 + rr) * NQKV + sec * 576 + hg * 72 + pc * 8) * 2), dz);
        }
        asm volatile("s_waitcnt vmcnt(20)" : "+v"(xv[0]), "+v"(xv[1]), "+v"(xv[2]), "+v"(xv[3]), "+v"(xv[4]),
                     "+v"(xv[5]), "+v"(xv[6]), "+v"(xv[7]), "+v"(xv[8]) :: "memory");
        ln_pass(xv, wave * 8 + (lane >> 3));
    } else {
        u4v xv[2][9];
#pragma unroll
        for (int j = 0; j < 2; j++) {
            if (j < 1 || wave < 2) {
                int r = (8 * j + wave) * 8 + (lane >> 3);
                int s = s0 - 16 + r; if (s < 0) s = 0;
                const u4v* pp = (const u4v*)(x + (size_t)s * D_) + (lane & 7);
#pragma unroll
                for (int i = 0; i < 9; i++) xv[j][i] = pp[i * 8];
            }
        }
        ln_pass(xv[0], wave * 8 + (lane >> 3));
        if (wave < 2) ln_pass(xv[1], (8 + wave) * 8 + (lane >> 3));
    }

    f4v acc[5][4];
    if (wave >= 4) {
        WAIT_V(16);
        __syncthreads();
        WAIT_V(12); BAR;
        issue(3); WAIT_V(12); BAR;
        for (int i = 1; i < 15; i++) {
            issue(i + K1_NB - 1);
            WAIT_V(4);
            BAR;
        }
        WAIT_V(0); BAR;
        {
            const u4v* pw = (const u4v*)(w1 + (size_t)(rthird * 192 + lw * 48 + (lane >> 3)) * F_ + cchunk * 64) + (lane & 7);
#pragma unroll
            for (int j = 0; j < 6; j++) cw[j] = pw[(size_t)j * 8 * F_ / 8];
            if (wave == 4 && blockIdx.x < 144) {
                const int ctp = blockIdx.x / 18, kst = blockIdx.x - ctp * 18;
#pragma unroll
                for (int j = 0; j < 5; j++) {
                    int c = j * 64 + lane; if (c > 287) c = 287;
                    int row = c / 9, pc = c - row * 9;
                    glds16s(wo, (unsigned)(((kst * 32 + row) * D_ + ctp * 72 + pc * 8) * 2), lds_addr(smem) + K1_DUMMY + j * 1024);
                }
            }
        }
        BAR; BAR;
    } else {
        const int nt0 = wave == 0 ? 0 : 1 + 3 * wave;
        const int nn = (wave == 0 || wave == 3) ? 4 : 3;
#pragma unroll
        for (int m = 0; m < 5; m++)
#pragma unroll
            for (int n = 0; n < 4; n++) acc[m][n] = f4v{0, 0, 0, 0};
        const int xr = lane & 15;
        int woff[4];
#pragma unroll
        for (int n = 0; n < 4; n++) woff[n] = K1_XN_BYTES + (8 * g + q) * 448 + (((nt0 + n) ^ (g & 1)) << 5) + 8 * p;
        s8v xa[5], xb[5], wf[4];
        auto rdx = [&](s8v* X, int st) {
            const char* xbp = smem + (st >> 1) * (80 * 128) + xr * 128 + ((((st & 1) * 4 + g) ^ (xr & 7)) << 4);
#pragma unroll
            for (int m = 0; m < 5; m++) X[m] = *(const s8v*)(xbp + m * 2048);
        };
        __syncthreads();
        rdx(xa, 0);
#pragma unroll
        for (int n = 0; n < 4; n++) wf[n] = cat8(tr_read(smem + woff[n]), tr_read(smem + woff[n] + 4 * 448));
        WAIT_L(0);
        BAR;
#define K1_STEP(XC, XN) { \
            const char* base = smem + ((i + 1) % K1_NB) * K1_STAGE; \
            rdx(XN, i + 1 > 17 ? 17 : i + 1); \
            SB0; \
            _Pragma("unroll") for (int n = 0; n < 4; n++) { \
                if (n < 3 || nn == 4) { \
                    if (wave != 0) acc[0][n] = MFMA16(XC[0], wf[n], acc[0][n]); \
                    _Pragma("unroll") for (int m = 1; m < 5; m++) acc[m][n] = MFMA16(XC[m], wf[n], acc[m][n]); \
                    SB0; \
                    wf[n] = cat8(tr_read(base + woff[n]), tr_read(base + woff[n] + 4 * 448)); \
                    SB0; \
                } \
            } \
            BAR; }
#pragma unroll 1
        for (int i = 0; i < 18; i += 2) {
            K1_STEP(xa, xb);
            i++; K1_STEP(xb, xa); i--;
        }
    }
    __syncthreads();
    constexpr int QS = 84;
    float* qkv = (float*)smem;
    if (wave < 4) {
        const int nt0 = wave == 0 ? 0 : 1 + 3 * wave;
        const int nn = (wave == 0 || wave == 3) ? 4 : 3;
#pragma unroll
        for (int m = 0; m < 5; m++)
#pragma unroll
            for (int n = 0; n < 4; n++) {
                int col = (nt0 + n) * 16 + (lane & 15);
                if (n < nn && col < 216) *(f4v*)(qkv + col * QS + m * 16 + 4 * g) = acc[m][n];
            }
    }
    __syncthreads();
    {
        const int head = wave >> 1;
        const int rl = (wave & 1) * 32 + (lane & 31);
        const int hw = lane >> 5;
        const int r = 16 + rl;
        const int s = s0 + rl;
        const float* qp = qkv + (head * 18) * QS + r;
        const float* kp = qkv + (72 + head * 18) * QS + r - 8 * hw;
        const float* vp = qkv + (144 + head * 18) * QS + r - 8 * hw;
        float qv[18];
#pragma unroll
        for (int d = 0; d < 18; d++) qv[d] = qp[d * QS];
        float lg[4];
        float mx = -3.0e38f;
#pragma unroll
        for (int w = 0; w < 4; w++) {
            float a = 0.f;
#pragma unroll
            for (int d = 0; d < 18; d++) a += qv[d] * kp[d * QS - 2 * w];
            a *= 0.23570226039551584f;
            lg[w] = (s - 2 * (w + 4 * hw) >= 0) ? a : -1e9f;
            mx = fmaxf(mx, lg[w]);
        }
        mx = fmaxf(mx, __shfl_xor(mx, 32));
        float den = 0.f;
#pragma unroll
        for (int w = 0; w < 4; w++) { lg[w] = __expf(lg[w] - mx); den += lg[w]; }
        den += __shfl_xor(den, 32);
        float inv = 1.f / den;
        float ov[18];
#pragma unroll
        for (int d = 0; d < 18; d++) ov[d] = 0.f;
#pragma unroll
        for (int w = 0; w < 4; w++) {
            float pw = lg[w] * inv;
#pragma unroll
            for (int d = 0; d < 18; d++) ov[d] += pw * vp[d * QS - 2 * w];
        }
#pragma unroll
        for (int d = 0; d < 18; d++) ov[d] += __shfl_xor(ov[d], 32);
        char* ob = of + (size_t)(s >> 4) * (18 * 1024) + (s & 15) * 16;
        const int cb = hg * 72 + head * 18;
#pragma unroll
        for (int d = 0; d < 5; d++) {
            const int dd = hw == 0 ? d : (d + 5 > 8 ? 8 : d + 5);
            const int col = cb + 2 * dd;
            const unsigned pv = hw == 0 ? pack_bf2(ov[2 * d], ov[2 * d + 1]) : pack_bf2(ov[2 * (d + 5 > 8 ? 8 : d + 5)], ov[2 * (d + 5 > 8 ? 8 : d + 5) + 1]);
            if (hw == 0 || d < 4) *(unsigned*)(ob + (col >> 5) * 1024 + ((col & 31) >> 3) * 256 + (col & 7) * 2) = pv;
        }
    }
    if (wave >= 4) {
        float a[8];
#pragma unroll
        for (int j = 0; j < 8; j++) a[j] = 0.f;
#pragma unroll
        for (int i = 0; i < 6; i++)
#pragma unroll
            for (int j = 0; j < 4; j++) { a[2 * j] += bf_lo(cw[i][j]); a[2 * j + 1] += bf_hi(cw[i][j]); }
#pragma unroll
        for (int j = 0; j < 8; j++) {
            float t = a[j];
            t += __shfl_xor(t, 8); t += __shfl_xor(t, 16); t += __shfl_xor(t, 32);
            a[j] = t;
        }
        if (lane < 8) {
            float* rq = (float*)(smem + K1_RED) + lw * 64 + lane * 8;
            *(f4v*)rq = f4v{a[0], a[1], a[2], a[3]};
            *(f4v*)(rq + 4) = f4v{a[4], a[5], a[6], a[7]};
        }
        WAIT_V(0);
        if (wave == 4 && blockIdx.x < 144) {
            const char* sb = smem + K1_DUMMY + (8 * g + q) * 144 + 8 * p;
            char* dst = wof + (size_t)blockIdx.x * 5 * 1024 + lane * 16;
#pragma unroll
            for (int nt = 0; nt < 5; nt++)
                *(s8v*)(dst + nt * 1024) = cat8(tr_read(sb + nt * 32), tr_read(sb + 4 * 144 + nt * 32));
        }
    }
    __syncthreads();
    if (wave == 7 && blockIdx.x < 216) {
        const float* rq = (const float*)(smem + K1_RED) + lane;
        cs1[rthird * F_ + cchunk * 64 + lane] = (rq[0] + rq[64]) + (rq[128] + rq[192]);
    }
}

constexpr int K2_LDS = 8 * 20 * 1024;

__global__ __launch_bounds__(512, 2) void k2_wo(const char* __restrict__ of, const char* __restrict__ wof,
                                                const bf16_t* __restrict__ x, bf16_t* __restrict__ h,
                                                char* __restrict__ hT, f2v* __restrict__ stats) {
    extern __shared__ __attribute__((aligned(16))) char smem[];
    const int tid = threadIdx.x, lane = tid & 63;
    const int wave = __builtin_amdgcn_readfirstlane(tid >> 6);
    const int rt = (blockIdx.x & 7) * 4 + ((blockIdx.x >> 3) & 3), ct = blockIdx.x >> 5;
    const int r0 = rt * 64, c0 = ct * 72;
    const int g = lane >> 4;
    const char* pa = of + ((size_t)rt * 4 * 18 + wave) * 1024 + lane * 16;
    const char* pw = wof + ((size_t)ct * 18 + wave) * 5 * 1024 + lane * 16;
    s8v fa[3][4], fw[3][5];
#pragma unroll
    for (int j = 0; j < 3; j++) {
        if (j < 2 || wave < 2) {
#pragma unroll
            for (int m = 0; m < 4; m++) fa[j][m] = *(const s8v*)(pa + m * (18 * 1024) + j * 8192);
#pragma unroll
            for (int n = 0; n < 5; n++) fw[j][n] = *(const s8v*)(pw + n * 1024 + j * 40960);
        }
    }
    const int row = r0 + (wave & 3) * 16 + (lane & 15);
    u2v xx[5];
#pragma unroll
    for (int n = 0; n < 5; n++) {
        int col = n * 16 + 4 * g; if (col > 68) col = 68;
        xx[n] = *(const u2v*)(x + (size_t)row * D_ + c0 + col);
    }
    f4v acc[4][5];
#pragma unroll
    for (int m = 0; m < 4; m++)
#pragma unroll
        for (int n = 0; n < 5; n++) acc[m][n] = f4v{0, 0, 0, 0};
#pragma unroll
    for (int j = 0; j < 3; j++) {
        if (j < 2 || wave < 2) {
#pragma unroll
            for (int n = 0; n < 5; n++)
#pragma unroll
                for (int m = 0; m < 4; m++) acc[m][n] = MFMA16(fw[j][n], fa[j][m], acc[m][n]);
        }
    }
    f4v* ex = (f4v*)smem + wave * (20 * 64) + lane;
#pragma unroll
    for (int m = 0; m < 4; m++)
#pragma unroll
        for (int n = 0; n < 5; n++) ex[(m * 5 + n) * 64] = acc[m][n];
    __syncthreads();
    if (wave >= 4) {
        return;
    }
    float hv[5][4];
    float sum = 0.f;
#pragma unroll
    for (int n = 0; n < 5; n++) {
        const f4v* e = (const f4v*)smem + (wave * 5 + n) * 64 + lane;
        f4v a = ((e[0] + e[1280]) + (e[2 * 1280] + e[3 * 1280])) + ((e[4 * 1280] + e[5 * 1280]) + (e[6 * 1280] + e[7 * 1280]));
        int col = n * 16 + 4 * g;
        if (col < 72) {
            unsigned p0 = pack_bf2(bf_lo(xx[n][0]) + a[0], bf_hi(xx[n][0]) + a[1]);
            unsigned p1 = pack_bf2(bf_lo(xx[n][1]) + a[2], bf_hi(xx[n][1]) + a[3]);
            *(u2v*)(h + (size_t)row * D_ + c0 + col) = u2v{p0, p1};
            {
                int cc = c0 + col;
                *(u2v*)(hT + (size_t)((row >> 4) * 18 + (cc >> 5)) * 1024 + (((cc & 31) >> 3) * 16 + (row & 15)) * 16 + (cc & 7) * 2) = u2v{p0, p1};
            }
            hv[n][0] = bf_lo(p0); hv[n][1] = bf_hi(p0); hv[n][2] = bf_lo(p1); hv[n][3] = bf_hi(p1);
            sum += hv[n][0] + hv[n][1] + hv[n][2] + hv[n][3];
        } else {
            hv[n][0] = hv[n][1] = hv[n][2] = hv[n][3] = 0.f;
        }
    }
    sum += __shfl_xor(sum, 16); sum += __shfl_xor(sum, 32);
    float mean = sum * (1.f / 72.f);
    float m2 = 0.f;
#pragma unroll
    for (int n = 0; n < 5; n++) {
        int col = n * 16 + 4 * g;
        if (col < 72) {
#pragma unroll
            for (int j = 0; j < 4; j++) { float d = hv[n][j] - mean; m2 += d * d; }
        }
    }
    m2 += __shfl_xor(m2, 16); m2 += __shfl_xor(m2, 32);
    if (g == 0) stats[(size_t)row * 8 + ct] = f2v{sum, m2};
}

constexpr int K3_STAGE = 8192 + 18432;
constexpr int K3_NB = 5;
constexpr int K3_PAR = K3_NB * K3_STAGE;
constexpr int K3_LDS = K3_PAR + 1024 + 2304;

__device__ __forceinline__ float gelu_tanh(float v) {
    float t = v * v;
    float z = v * (-2.302208198f * 0.044715f * t - 2.302208198f);
    float e = __builtin_amdgcn_exp2f(z);
    return v * __builtin_amdgcn_rcpf(1.f + e);
}

__device__ __forceinline__ f2v gelu_tanh2(f2v v) {
    f2v t = v * v;
    f2v z = v * (t * (-2.302208198f * 0.044715f) + (-2.302208198f));
    f2v d = f2v{__builtin_amdgcn_exp2f(z[0]), __builtin_amdgcn_exp2f(z[1])} + 1.f;
    return v * f2v{__builtin_amdgcn_rcpf(d[0]), __builtin_amdgcn_rcpf(d[1])};
}

__global__ __launch_bounds__(512, 2) void k3_up(const char* __restrict__ hT, const bf16_t* __restrict__ w1,
                                                const bf16_t* __restrict__ b1, const float* __restrict__ cs1,
                                                const f2v* __restrict__ stats, const bf16_t* __restrict__ w2,
                                                char* __restrict__ h1f, char* __restrict__ w2f) {
    extern __shared__ __attribute__((aligned(16))) char smem[];
    const int tid = threadIdx.x, lane = tid & 63;
    const int wave = __builtin_amdgcn_readfirstlane(tid >> 6);
    const int xcd = blockIdx.x & 7, idx = blockIdx.x >> 3;
    const int rt = (xcd & 1) * 8 + (idx & 7), ct = (xcd >> 1) * 4 + (idx >> 3);
    const int r0 = rt * 128, c0 = ct * 288;
    const unsigned lbase = lds_addr(smem);
    const int g = lane >> 4, q = (lane & 15) >> 2, p = lane & 3;
    const int xr = lane & 15;
    f2v* mu = (f2v*)(smem + K3_PAR);
    float* csl = (float*)(smem + K3_PAR + 1024);
    float* bbl = csl + 288;
    f4v acc[4][9];
    const int wm = (wave >> 1) & 1, wn = wave & 1;
    if (wave >= 4) {
        const int lw = wave - 4;
        unsigned bsrc[5];
        const unsigned avo = lane * 16;
        const char* ag = hT + (size_t)(rt * 8 + lw * 2) * 18 * 1024;
#pragma unroll
        for (int i = 0; i < 5; i++) {
            int bi = lw * 5 + i; if (bi > 17) bi = 17;
            int off = bi * 1024 + lane * 16;
            int k = off / 576, r = off - k * 576;
            int nt = (r >> 5) ^ ((k >> 3) & 1);
            bsrc[i] = (unsigned)((k * F_ + c0 + nt * 16 + ((r >> 4) & 1) * 8) * 2);
        }
        auto issue = [&](int st) {
            int sc = st > 17 ? 17 : st;
            unsigned lb = lbase + (st % K3_NB) * K3_STAGE;
            const char* ga = ag + sc * 1024;
            const char* gb = (const char*)w1 + (size_t)sc * 32 * F_ * 2;
            glds16s(ga, avo, lb + (lw * 2) * 1024);
            glds16s(ga + 18 * 1024, avo, lb + (lw * 2 + 1) * 1024);
#pragma unroll
            for (int i = 0; i < 5; i++) glds16s(gb, bsrc[i], lb + 8192 + (lw * 5 + i > 17 ? 17 : lw * 5 + i) * 1024);
        };
#pragma unroll
        for (int st = 0; st < K3_NB - 1; st++) issue(st);
        WAIT_V(21);
        BAR;
        for (int i = -1; i < 18; i++) {
            if (i >= 0) issue(i + K3_NB - 1);
            WAIT_V(14);
            BAR;
        }
        WAIT_V(0);
    } else {
        {
            float c0v = (cs1[c0 + tid] + cs1[F_ + c0 + tid]) + cs1[2 * F_ + c0 + tid], b0v = bf2f(b1[c0 + tid]);
            float c1v = 0.f, b1v = 0.f;
            if (tid < 32) { c1v = (cs1[c0 + 256 + tid] + cs1[F_ + c0 + 256 + tid]) + cs1[2 * F_ + c0 + 256 + tid]; b1v = bf2f(b1[c0 + 256 + tid]); }
            if (tid < 128) {
                const f4v* sp = (const f4v*)(stats + (size_t)(r0 + tid) * 8);
                f4v v[4];
#pragma unroll
                for (int i = 0; i < 4; i++) v[i] = sp[i];
                float tot = 0.f;
#pragma unroll
                for (int i = 0; i < 4; i++) tot += v[i][0] + v[i][2];
                float mean = tot * (1.f / 576.f);
                float M2 = 0.f;
#pragma unroll
                for (int i = 0; i < 4; i++) {
                    float d0 = v[i][0] * (1.f / 72.f) - mean, d1 = v[i][2] * (1.f / 72.f) - mean;
                    M2 += v[i][1] + v[i][3] + 72.f * (d0 * d0 + d1 * d1);
                }
                float rstd = rsqrtf(M2 * (1.f / 576.f) + 1e-5f);
                mu[tid] = f2v{mean * rstd, rstd};
            }
            csl[tid] = c0v; bbl[tid] = b0v;
            if (tid < 32) { csl[256 + tid] = c1v; bbl[256 + tid] = b1v; }
        }
#pragma unroll
        for (int m = 0; m < 4; m++)
#pragma unroll
            for (int n = 0; n < 9; n++) acc[m][n] = f4v{0, 0, 0, 0};
        const int dlt = (g & 1) ? 32 : 0;
        const int wb0 = 8192 + (8 * g + q) * 576 + wn * 9 * 32 + 8 * p;
        const int wbe = wb0 + ((wn & 1) ? -dlt : dlt);
        const int wbo = wb0 + ((wn & 1) ? dlt : -dlt);
        const int xoff = wm * 4096 + lane * 16;
        s8v xa[4], xb[4], wf[9];
        BAR;
        {
            const char* base = smem;
#pragma unroll
            for (int m = 0; m < 4; m++) xa[m] = *(const s8v*)(base + xoff + m * 1024);
#pragma unroll
            for (int n = 0; n < 9; n++) {
                const char* wb = base + ((n & 1) ? wbo : wbe) + n * 32;
                wf[n] = cat8(tr_read(wb), tr_read(wb + 4 * 576));
            }
        }
        WAIT_L(0);
        BAR;
#define K3_STEP(XC, XN) { \
            const char* base = smem + ((i + 1) % K3_NB) * K3_STAGE; \
            s4v wlo, whi; \
            _Pragma("unroll") for (int n = 0; n < 9; n++) { \
                const char* wbp = base + (((n - 1) & 1) ? wbo : wbe) + (n - 1) * 32; \
                acc[0][n] = MFMA16(wf[n], XC[0], acc[0][n]); SB0; \
                if (n > 0) { wlo = tr_read(wbp); SB0; } \
                acc[1][n] = MFMA16(wf[n], XC[1], acc[1][n]); SB0; \
                if (n > 0) { whi = tr_read(wbp + 4 * 576); wf[n - 1] = cat8(wlo, whi); SB0; } \
                acc[2][n] = MFMA16(wf[n], XC[2], acc[2][n]); SB0; \
                if (n < 4) { XN[n] = *(const s8v*)(base + xoff + n * 1024); SB0; } \
                acc[3][n] = MFMA16(wf[n], XC[3], acc[3][n]); SB0; \
            } \
            { const char* wbp = base + wbe + 8 * 32; wf[8] = cat8(tr_read(wbp), tr_read(wbp + 4 * 576)); } \
            BAR; }
#pragma unroll 1
        for (int i = 0; i < 18; i += 2) {
            K3_STEP(xa, xb);
            i++; K3_STEP(xb, xa); i--;
        }
    }
    __syncthreads();
    const int u = wave * 256 + blockIdx.x;
    if (u < 1152) {
        const int ctp = u / 144, kst = u - ctp * 144;
#pragma unroll
        for (int j = 0; j < 5; j++) {
            int c = j * 64 + lane; if (c > 287) c = 287;
            int row = c / 9, pc = c - row * 9;
            glds16s(w2, (unsigned)(((kst * 32 + row) * D_ + ctp * 72 + pc * 8) * 2), lbase + 98304 + wave * 5120 + j * 1024);
        }
    }
    f4v* ex = (f4v*)smem + (wave & 3) * (18 * 64) + lane;
    if (wave < 4) {
#pragma unroll
        for (int m = 0; m < 2; m++)
#pragma unroll
            for (int n = 0; n < 9; n++) ex[(m * 9 + n) * 64] = acc[m + 2][n];
    }
    __syncthreads();
    if (wave >= 4) {
#pragma unroll
        for (int m = 0; m < 2; m++)
#pragma unroll
            for (int n = 0; n < 9; n++) acc[m][n] = ex[(m * 9 + n) * 64];
    }
    {
        const int mb = (wave >> 2) * 2;
#pragma unroll
        for (int m = 0; m < 2; m++) {
            int rl = wm * 64 + (mb + m) * 16 + xr;
            f2v ms = mu[rl];
            char* ob = h1f + (size_t)((r0 + wm * 64 + (mb + m) * 16) >> 4) * (144 * 1024) + lane * 16;
            u2v pv[9];
#pragma unroll
            for (int n = 0; n < 9; n++) {
                f4v a = acc[m][n];
                int cl = wn * 144 + n * 16 + 4 * g;
                f4v cs = *(const f4v*)(csl + cl), bb = *(const f4v*)(bbl + cl);
                f2v t01 = f2v{bb[0], bb[1]} - f2v{cs[0], cs[1]} * ms[0];
                f2v t23 = f2v{bb[2], bb[3]} - f2v{cs[2], cs[3]} * ms[0];
                f2v v01 = gelu_tanh2(f2v{a[0], a[1]} * ms[1] + t01);
                f2v v23 = gelu_tanh2(f2v{a[2], a[3]} * ms[1] + t23);
                pv[n] = u2v{pack_bf2(v01[0], v01[1]), pack_bf2(v23[0], v23[1])};
            }
            if (m == 0) {
            } else {
            }
            char* oc = ob + (c0 >> 5) * 1024;
            if (wn == 0) {
#pragma unroll
                for (int j = 0; j < 4; j++) *(u4v*)(oc + j * 1024) = u4v{pv[2 * j][0], pv[2 * j][1], pv[2 * j + 1][0], pv[2 * j + 1][1]};
                *(u2v*)(oc + 4 * 1024) = pv[8];
            } else {
                *(u2v*)(oc + 4 * 1024 + 8) = pv[0];
#pragma unroll
                for (int j = 0; j < 4; j++) *(u4v*)(oc + (5 + j) * 1024) = u4v{pv[2 * j + 1][0], pv[2 * j + 1][1], pv[2 * j + 2][0], pv[2 * j + 2][1]};
            }
        }
    }
    WAIT_V(0);
    if (u < 1152) {
        const char* sb = smem + 98304 + wave * 5120 + (4 * g + q) * 144 + 8 * p;
        char* dst = w2f + (size_t)u * 5 * 1024 + lane * 16;
#pragma unroll
        for (int nt = 0; nt < 5; nt++)
            *(s8v*)(dst + nt * 1024) = cat8(tr_read(sb + nt * 32), tr_read(sb + 16 * 144 + nt * 32));
    }
}

constexpr int K4_LDS = 8 * 20 * 1024;
constexpr int K4_D = 3;

__global__ __launch_bounds__(512, 2) void k4_down(const char* __restrict__ h1f, const char* __restrict__ w2f,
                                                  const bf16_t* __restrict__ h, const bf16_t* __restrict__ b2,
                                                  bf16_t* __restrict__ out) {
    extern __shared__ __attribute__((aligned(16))) char smem[];
    const int tid = threadIdx.x, lane = tid & 63;
    const int wave = __builtin_amdgcn_readfirstlane(tid >> 6);
    const int xcd = blockIdx.x & 7, idx = blockIdx.x >> 3;
    const int rt = (xcd >> 1) * 8 + (idx & 7), ct = (xcd & 1) * 4 + (idx >> 3);
    const int g = lane >> 4;
    const char* pa = h1f + ((size_t)rt * 4 * 144 + wave) * 1024 + lane * 16;
    const char* pw = w2f + ((size_t)ct * 144 + wave) * 5 * 1024 + lane * 16;
    f4v acc[4][5];
#pragma unroll
    for (int m = 0; m < 4; m++)
#pragma unroll
        for (int n = 0; n < 5; n++) acc[m][n] = f4v{0, 0, 0, 0};
    s8v fa[K4_D][4], fw[K4_D][5];
#pragma unroll
    for (int j = 0; j < K4_D; j++) {
#pragma unroll
        for (int m = 0; m < 4; m++) fa[j][m] = *(const s8v*)(pa + m * (144 * 1024) + j * 8192);
#pragma unroll
        for (int n = 0; n < 5; n++) fw[j][n] = *(const s8v*)(pw + n * 1024 + j * 40960);
        SB0;
    }
#pragma unroll
    for (int j = 0; j < 18; j++) {
        const int sl = j % K4_D;
        SB0;
#pragma unroll
        for (int n = 0; n < 5; n++)
#pragma unroll
            for (int m = 0; m < 4; m++) acc[m][n] = MFMA16(fw[sl][n], fa[sl][m], acc[m][n]);
        SB0;
        if (j + K4_D < 18) {
#pragma unroll
            for (int m = 0; m < 4; m++) fa[sl][m] = *(const s8v*)(pa + m * (144 * 1024) + (j + K4_D) * 8192);
#pragma unroll
            for (int n = 0; n < 5; n++) fw[sl][n] = *(const s8v*)(pw + n * 1024 + (j + K4_D) * 40960);
        }
        SB0;
    }
    u2v hh[3], bb[3];
#pragma unroll
    for (int j = 0; j < 3; j++) {
        int t = wave + 8 * j; if (t > 19) t = 19;
        const int m = t / 5, n = t - m * 5;
        const int row = rt * 64 + m * 16 + (lane & 15);
        int cl = n * 16 + 4 * g; if (cl > 68) cl = 68;
        hh[j] = *(const u2v*)(h + (size_t)row * D_ + ct * 72 + cl);
        bb[j] = *(const u2v*)(b2 + ct * 72 + cl);
    }
    f4v* ex = (f4v*)smem + wave * (20 * 64) + lane;
#pragma unroll
    for (int m = 0; m < 4; m++)
#pragma unroll
        for (int n = 0; n < 5; n++) ex[(m * 5 + n) * 64] = acc[m][n];
    __syncthreads();
#pragma unroll
    for (int j = 0; j < 3; j++) {
        const int t = wave + 8 * j;
        if (t < 20) {
            const int m = t / 5, n = t - m * 5;
            const f4v* e = (const f4v*)smem + t * 64 + lane;
            f4v a = ((e[0] + e[1280]) + (e[2 * 1280] + e[3 * 1280])) + ((e[4 * 1280] + e[5 * 1280]) + (e[6 * 1280] + e[7 * 1280]));
            const int row = rt * 64 + m * 16 + (lane & 15);
            const int cl = n * 16 + 4 * g;
            float v0 = bf_lo(hh[j][0]) + (a[0] + bf_lo(bb[j][0]));
            float v1 = bf_hi(hh[j][0]) + (a[1] + bf_hi(bb[j][0]));
            float v2 = bf_lo(hh[j][1]) + (a[2] + bf_lo(bb[j][1]));
            float v3 = bf_hi(hh[j][1]) + (a[3] + bf_hi(bb[j][1]));
            if (cl < 72) *(u2v*)(out + (size_t)row * D_ + ct * 72 + cl) = u2v{pack_bf2(v0, v1), pack_bf2(v2, v3)};
        }
    }
}

extern "C" void kernel_launch(void* const* d_in, const int* in_sizes, int n_in,
                              void* d_out, int out_size, void* d_ws, size_t ws_size,
                              hipStream_t stream) {
    const bf16_t* x = (const bf16_t*)d_in[0];
    const bf16_t* wqkv = (const bf16_t*)d_in[1];
    const bf16_t* wo = (const bf16_t*)d_in[2];
    const bf16_t* w1 = (const bf16_t*)d_in[4];
    const bf16_t* b1 = (const bf16_t*)d_in[5];
    const bf16_t* w2 = (const bf16_t*)d_in[6];
    const bf16_t* b2 = (const bf16_t*)d_in[7];
    bf16_t* out = (bf16_t*)d_out;
    char* ws = (char*)d_ws;
    char* of = ws;
    char* wof = ws + (3 << 20);
    bf16_t* h = (bf16_t*)(ws + (4 << 20));
    char* hT = ws + (12 << 20);
    f2v* stats = (f2v*)(ws + (8 << 20));
    float* cs1 = (float*)(ws + (9 << 20));
    char* h1 = (char*)(ws + (16 << 20));
    char* w2f = ws + (48 << 20);

    hipFuncSetAttribute((const void*)k1_qkv_attn, hipFuncAttributeMaxDynamicSharedMemorySize, K1_LDS);
    hipFuncSetAttribute((const void*)k2_wo, hipFuncAttributeMaxDynamicSharedMemorySize, K2_LDS);
    hipFuncSetAttribute((const void*)k3_up, hipFuncAttributeMaxDynamicSharedMemorySize, K3_LDS);
    hipFuncSetAttribute((const void*)k4_down, hipFuncAttributeMaxDynamicSharedMemorySize, K4_LDS);

    k1_qkv_attn<<<256, 512, K1_LDS, stream>>>(x, wqkv, w1, wo, of, wof, cs1);
    k2_wo<<<256, 512, K2_LDS, stream>>>(of, wof, x, h, hT, stats);
    k3_up<<<256, 512, K3_LDS, stream>>>(hT, w1, b1, cs1, stats, w2, h1, w2f);
    k4_down<<<256, 512, K4_LDS, stream>>>(h1, w2f, h, b2, out);
}
```

```cpp
#include <hip/hip_runtime.h>
#include <hip/hip_bf16.h>

typedef __attribute__((ext_vector_type(8))) short s8v;
typedef __attribute__((ext_vector_type(4))) short s4v;
typedef __attribute__((ext_vector_type(4))) short v4i16_t;
typedef __attribute__((ext_vector_type(4))) float f4v;
typedef __attribute__((ext_vector_type(2))) float f2v;
typedef __attribute__((ext_vector_type(4))) unsigned u4v;
typedef __attribute__((ext_vector_type(2))) unsigned u2v;
typedef unsigned short bf16_t;

constexpr int S_ = 2048, D_ = 576, NQKV = 1728, F_ = 4608;

__device__ __forceinline__ unsigned lds_addr(const void* l) {
    return (unsigned)(size_t)(__attribute__((address_space(3))) const char*)l;
}
__device__ __forceinline__ void glds16(const void* g, unsigned dst) {
    unsigned keep;
    asm volatile("s_mov_b32 %0, m0\n\ts_mov_b32 m0, %2\n\ts_nop 0\n\tglobal_load_lds_dwordx4 %1, off\n\ts_mov_b32 m0, %0"
                 : "=&s"(keep) : "v"(g), "s"(dst) : "memory");
}
__device__ __forceinline__ void glds16s(const void* sbase, unsigned voff, unsigned dst) {
    unsigned keep;
    asm volatile("s_mov_b32 %0, m0\n\ts_mov_b32 m0, %3\n\ts_nop 0\n\tglobal_load_lds_dwordx4 %1, %2\n\ts_mov_b32 m0, %0"
                 : "=&s"(keep) : "v"(voff), "s"(sbase), "s"(dst) : "memory");
}
__device__ __forceinline__ s4v tr_read(const char* p) {
    return __builtin_bit_cast(s4v, __builtin_amdgcn_ds_read_tr16_b64_v4i16((__attribute__((address_space(3))) v4i16_t*)p));
}
__device__ __forceinline__ s8v cat8(s4v a, s4v b) {
    return __builtin_shufflevector(a, b, 0, 1, 2, 3, 4, 5, 6, 7);
}
__device__ __forceinline__ float bf_lo(unsigned u) { return __builtin_bit_cast(float, u << 16); }
__device__ __forceinline__ float bf_hi(unsigned u) { return __builtin_bit_cast(float, u & 0xffff0000u); }
__device__ __forceinline__ float bf2f(bf16_t v) { return __builtin_bit_cast(float, (unsigned)v << 16); }
__device__ __forceinline__ unsigned pack_bf2(float a, float b) {
    unsigned r;
    asm("v_cvt_pk_bf16_f32 %0, %1, %2" : "=v"(r) : "v"(a), "v"(b));
    return r;
}
#define MFMA16(a, b, c) __builtin_amdgcn_mfma_f32_16x16x32_bf16(a, b, c, 0, 0, 0)
#define WAIT_V(n) asm volatile("s_waitcnt vmcnt(" #n ")" ::: "memory")
#define WAIT_L(n) asm volatile("s_waitcnt lgkmcnt(" #n ")" ::: "memory")

#define SB0 __builtin_amdgcn_sched_barrier(0)
#define BAR __builtin_amdgcn_s_barrier()
constexpr int K1_XN_BYTES = 9 * 80 * 128;
constexpr int K1_STAGE = 14336;
constexpr int K1_NB = 4;
constexpr int K1_DUMMY = K1_XN_BYTES + K1_NB * K1_STAGE;
constexpr int K1_RED = K1_DUMMY + 5120;
constexpr int K1_LDS = K1_RED + 2048;

__global__ __launch_bounds__(512, 2) void k1_qkv_attn(const bf16_t* __restrict__ x, const bf16_t* __restrict__ wqkv,
                                                      const bf16_t* __restrict__ w1, const bf16_t* __restrict__ wo,
                                                      char* __restrict__ of, char* __restrict__ wof,
                                                      float* __restrict__ cs1) {
    extern __shared__ __attribute__((aligned(16))) char smem[];
    const int tid = threadIdx.x, lane = tid & 63;
    const int wave = __builtin_amdgcn_readfirstlane(tid >> 6);
    const int xcd = blockIdx.x & 7, idx = blockIdx.x >> 3;
    const int hg = (xcd & 1) * 4 + (idx >> 3), rt = (xcd >> 1) * 8 + (idx & 7);
    const int s0 = rt * 64;
    const unsigned lring = lds_addr(smem) + K1_XN_BYTES;
    const int g = lane >> 4, q = (lane & 15) >> 2, p = lane & 3;

    const int lw = wave - 4;
    unsigned bsrc[4];
    auto issue = [&](int st) {
        int sc = st > 17 ? 17 : st;
        unsigned lb = lring + (st % K1_NB) * K1_STAGE;
        const char* gb = (const char*)wqkv + (size_t)sc * 32 * NQKV * 2;
#pragma unroll
        for (int i = 0; i < 4; i++) glds16s(gb, bsrc[i], lb + (lw * 4 + i > 13 ? 13 : lw * 4 + i) * 1024);
    };
    u4v cw[6];
    const int cu = blockIdx.x < 216 ? blockIdx.x : blockIdx.x - 216;
    const int cchunk = cu % 72, rthird = cu / 72;
    const unsigned dz = lds_addr(smem) + K1_DUMMY + (wave & 3) * 1024;
    float* mus = (float*)(smem + K1_RED);
    auto stats4 = [&](int hp) {
        const int r = hp * 4 + (lane >> 4);
        const char* sp = smem + r * 128 + (lane & 15) * 8;
        f2v v[18];
        f2v sm = f2v{0.f, 0.f};
#pragma unroll
        for (int i = 0; i < 9; i++) {
            u2v t = *(const u2v*)(sp + i * (80 * 128));
            v[2 * i] = f2v{bf_lo(t[0]), bf_hi(t[0])}; v[2 * i + 1] = f2v{bf_lo(t[1]), bf_hi(t[1])};
            sm += v[2 * i]; sm += v[2 * i + 1];
        }
        float sum = sm[0] + sm[1];
        sum += __shfl_xor(sum, 1); sum += __shfl_xor(sum, 2); sum += __shfl_xor(sum, 4); sum += __shfl_xor(sum, 8);
        const float mean = sum * (1.f / 576.f);
        f2v sq = f2v{0.f, 0.f};
#pragma unroll
        for (int i = 0; i < 18; i++) { f2v d = v[i] - mean; sq += d * d; }
        float sqs = sq[0] + sq[1];
        sqs += __shfl_xor(sqs, 1); sqs += __shfl_xor(sqs, 2); sqs += __shfl_xor(sqs, 4); sqs += __shfl_xor(sqs, 8);
        if ((lane & 15) == 0) { mus[r] = mean; mus[80 + r] = rsqrtf(sqs * (1.f / 576.f) + 1e-5f); }
    };
    if (wave >= 4) {
#pragma unroll
        for (int i = 0; i < 4; i++) {
            int bi = lw * 4 + i; if (bi > 13) bi = 13;
            int off = bi * 1024 + lane * 16;
            int k = off / 448, r = off - k * 448;
            int nt = (r >> 5) ^ ((k >> 3) & 1);
            int pc = nt * 2 + ((r >> 4) & 1); if (pc > 26) pc = 26;
            int sec = pc / 9, wi = pc - sec * 9;
            bsrc[i] = (unsigned)((k * NQKV + sec * 576 + hg * 72 + wi * 8) * 2);
        }
#pragma unroll
        for (int st = 0; st < K1_NB - 1; st++) issue(st);
    } else {
        unsigned xo[3]; int gqs[3];
#pragma unroll
        for (int j = 0; j < 3; j++) {
            int gq = wave + 4 * j; if (gq > 9) gq = wave + 4;
            int row = gq * 8 + (lane >> 3);
            int s = s0 - 16 + row; if (s < 0) s = 0;
            xo[j] = (unsigned)(s * (D_ * 2) + (((lane & 7) ^ (lane >> 3)) << 4));
            gqs[j] = gq;
        }
#pragma unroll
        for (int b = 0; b < 9; b++)
#pragma unroll
            for (int j = 0; j < 3; j++) glds16s((const char*)x + b * 128, xo[j], lds_addr(smem) + b * (80 * 128) + gqs[j] * 1024);
#pragma unroll
        for (int j = 0; j < 8; j++) {
            int c = (wave * 8 + j) * 64 + lane; if (c > 1943) c = 1943;
            int seg = c / 9, pc = c - seg * 9;
            int sec = seg / 72, rr = seg - sec * 72;
            glds16s(wqkv, (unsigned)((((idx & 7) * 72 + rr) * NQKV + sec * 576 + hg * 72 + pc * 8) * 2), dz);
        }
    }

    f4v acc[5][4], acs[4];
    if (wave >= 4) {
        WAIT_V(8);
        __syncthreads();
        WAIT_V(4); BAR;
        for (int i = 0; i < 15; i++) {
            issue(i + K1_NB - 1);
            if (i >= 7 && i < 12) stats4((i - 7) * 4 + lw);
            WAIT_V(4);
            BAR;
        }
        WAIT_V(0); BAR;
        {
            if (wave == 4 && blockIdx.x < 144) {
                const int ctp = blockIdx.x / 18, kst = blockIdx.x - ctp * 18;
#pragma unroll
                for (int j = 0; j < 5; j++) {
                    int c = j * 64 + lane; if (c > 287) c = 287;
                    int row = c / 9, pc = c - row * 9;
                    glds16s(wo, (unsigned)(((kst * 32 + row) * D_ + ctp * 72 + pc * 8) * 2), lds_addr(smem) + K1_DUMMY + j * 1024);
                }
            }
            const u4v* pw = (const u4v*)(w1 + (size_t)(rthird * 192 + lw * 48 + (lane >> 3)) * F_ + cchunk * 64) + (lane & 7);
#pragma unroll
            for (int j = 0; j < 6; j++) cw[j] = pw[(size_t)j * 8 * F_ / 8];
        }
        BAR; BAR;
    } else {
        const int nt0 = wave == 0 ? 0 : 1 + 3 * wave;
        const int nn = (wave == 0 || wave == 3) ? 4 : 3;
#pragma unroll
        for (int m = 0; m < 5; m++)
#pragma unroll
            for (int n = 0; n < 4; n++) acc[m][n] = f4v{0, 0, 0, 0};
#pragma unroll
        for (int n = 0; n < 4; n++) acs[n] = f4v{0, 0, 0, 0};
        const s8v ones = s8v{0x3F80, 0x3F80, 0x3F80, 0x3F80, 0x3F80, 0x3F80, 0x3F80, 0x3F80};
        const int xr = lane & 15;
        int woff[4];
#pragma unroll
        for (int n = 0; n < 4; n++) woff[n] = K1_XN_BYTES + (8 * g + q) * 448 + (((nt0 + n) ^ (g & 1)) << 5) + 8 * p;
        s8v xa[5], xb[5], wf[4];
        auto rdx = [&](s8v* X, int st) {
            const char* xbp = smem + (st >> 1) * (80 * 128) + xr * 128 + ((((st & 1) * 4 + g) ^ (xr & 7)) << 4);
#pragma unroll
            for (int m = 0; m < 5; m++) X[m] = *(const s8v*)(xbp + m * 2048);
        };
        WAIT_V(32);
        __syncthreads();
        rdx(xa, 0);
#pragma unroll
        for (int n = 0; n < 4; n++) wf[n] = cat8(tr_read(smem + woff[n]), tr_read(smem + woff[n] + 4 * 448));
        WAIT_L(0);
        WAIT_V(29);
        BAR;
#define K1_STEP(XC, XN) { \
            const char* base = smem + ((i + 1) % K1_NB) * K1_STAGE; \
            rdx(XN, i + 1 > 17 ? 17 : i + 1); \
            SB0; \
            _Pragma("unroll") for (int n = 0; n < 4; n++) { \
                if (n < 3 || nn == 4) { \
                    if (wave != 0) acc[0][n] = MFMA16(XC[0], wf[n], acc[0][n]); \
                    _Pragma("unroll") for (int m = 1; m < 5; m++) acc[m][n] = MFMA16(XC[m], wf[n], acc[m][n]); \
                    acs[n] = MFMA16(ones, wf[n], acs[n]); \
                    SB0; \
                    wf[n] = cat8(tr_read(base + woff[n]), tr_read(base + woff[n] + 4 * 448)); \
                    SB0; \
                } \
            } \
            switch (i) {     \
                case 0: WAIT_V(26); break; case 1: WAIT_V(23); break; case 2: WAIT_V(20); break; case 3: WAIT_V(17); break; \
                case 4: WAIT_V(14); break; case 5: WAIT_V(11); break; case 6: WAIT_V(8); break; default: break; } \
            BAR; }
#pragma unroll
        for (int i = 0; i < 18; i += 2) {
            K1_STEP(xa, xb);
            i++; K1_STEP(xb, xa); i--;
        }
    }
    __syncthreads();
    constexpr int QS = 84;
    float* qkv = (float*)smem;
    if (wave < 4) {
        const int nt0 = wave == 0 ? 0 : 1 + 3 * wave;
        const int nn = (wave == 0 || wave == 3) ? 4 : 3;
#pragma unroll
        for (int m = 0; m < 5; m++)
#pragma unroll
            for (int n = 0; n < 4; n++) {
                int col = (nt0 + n) * 16 + (lane & 15);
                const f4v mean4 = *(const f4v*)(mus + m * 16 + 4 * g), rs4 = *(const f4v*)(mus + 80 + m * 16 + 4 * g);
                if (n < nn && col < 216) *(f4v*)(qkv + col * QS + m * 16 + 4 * g) = (acc[m][n] - mean4 * acs[n][0]) * rs4;
            }
    }
    __syncthreads();
    if (wave == 4 && blockIdx.x < 144) {
        WAIT_V(6);
        const char* sb = smem + K1_DUMMY + (8 * g + q) * 144 + 8 * p;
        char* dst = wof + (size_t)blockIdx.x * 5 * 1024 + lane * 16;
#pragma unroll
        for (int nt = 0; nt < 5; nt++)
            *(s8v*)(dst + nt * 1024) = cat8(tr_read(sb + nt * 32), tr_read(sb + 4 * 144 + nt * 32));
    }
    {
        const int head = wave >> 1;
        const int rl = (wave & 1) * 32 + (lane & 31);
        const int hw = lane >> 5;
        const int r = 16 + rl;
        const int s = s0 + rl;
        const float* qp = qkv + (head * 18) * QS + r;
        const float* kp = qkv + (72 + head * 18) * QS + r - 8 * hw;
        const float* vp = qkv + (144 + head * 18) * QS + r - 8 * hw;
        float qv[18];
#pragma unroll
        for (int d = 0; d < 18; d++) qv[d] = qp[d * QS];
        float lg[4];
        float mx = -3.0e38f;
#pragma unroll
        for (int w = 0; w < 4; w++) {
            float a = 0.f;
#pragma unroll
            for (int d = 0; d < 18; d++) a += qv[d] * kp[d * QS - 2 * w];
            a *= 0.23570226039551584f;
            lg[w] = (s - 2 * (w + 4 * hw) >= 0) ? a : -1e9f;
            mx = fmaxf(mx, lg[w]);
        }
        mx = fmaxf(mx, __shfl_xor(mx, 32));
        float den = 0.f;
#pragma unroll
        for (int w = 0; w < 4; w++) { lg[w] = __expf(lg[w] - mx); den += lg[w]; }
        den += __shfl_xor(den, 32);
        float inv = 1.f / den;
        float ov[18];
#pragma unroll
        for (int d = 0; d < 18; d++) ov[d] = 0.f;
#pragma unroll
        for (int w = 0; w < 4; w++) {
            float pw = lg[w] * inv;
#pragma unroll
            for (int d = 0; d < 18; d++) ov[d] += pw * vp[d * QS - 2 * w];
        }
#pragma unroll
        for (int d = 0; d < 18; d++) ov[d] += __shfl_xor(ov[d], 32);
        char* ob = of + (size_t)(s >> 4) * (18 * 1024) + (s & 15) * 16;
        const int cb = hg * 72 + head * 18;
#pragma unroll
        for (int d = 0; d < 5; d++) {
            const int dd = hw == 0 ? d : (d + 5 > 8 ? 8 : d + 5);
            const int col = cb + 2 * dd;
            const unsigned pv = hw == 0 ? pack_bf2(ov[2 * d], ov[2 * d + 1]) : pack_bf2(ov[2 * (d + 5 > 8 ? 8 : d + 5)], ov[2 * (d + 5 > 8 ? 8 : d + 5) + 1]);
            if (hw == 0 || d < 4) *(unsigned*)(ob + (col >> 5) * 1024 + ((col & 31) >> 3) * 256 + (col & 7) * 2) = pv;
        }
    }
    if (wave >= 4) {
        float a[8];
#pragma unroll
        for (int j = 0; j < 8; j++) a[j] = 0.f;
#pragma unroll
        for (int i = 0; i < 6; i++)
#pragma unroll
            for (int j = 0; j < 4; j++) { a[2 * j] += bf_lo(cw[i][j]); a[2 * j + 1] += bf_hi(cw[i][j]); }
#pragma unroll
        for (int j = 0; j < 8; j++) {
            float t = a[j];
            t += __shfl_xor(t, 8); t += __shfl_xor(t, 16); t += __shfl_xor(t, 32);
            a[j] = t;
        }
        if (lane < 8 && blockIdx.x < 216) {
            float* rq = cs1 + (rthird * 4 + lw) * F_ + cchunk * 64 + lane * 8;
            *(f4v*)rq = f4v{a[0], a[1], a[2], a[3]};
            *(f4v*)(rq + 4) = f4v{a[4], a[5], a[6], a[7]};
        }
    }
}

constexpr int K2_LDS = 8 * 20 * 1024;

__global__ __launch_bounds__(512, 2) void k2_wo(const char* __restrict__ of, const char* __restrict__ wof,
                                                const bf16_t* __restrict__ x, bf16_t* __restrict__ h,
                                                char* __restrict__ hT, f2v* __restrict__ stats) {
    extern __shared__ __attribute__((aligned(16))) char smem[];
    const int tid = threadIdx.x, lane = tid & 63;
    const int wave = __builtin_amdgcn_readfirstlane(tid >> 6);
    const int rt = (blockIdx.x & 7) * 4 + ((blockIdx.x >> 3) & 3), ct = blockIdx.x >> 5;
    const int r0 = rt * 64, c0 = ct * 72;
    const int g = lane >> 4;
    const char* pa = of + ((size_t)rt * 4 * 18 + wave) * 1024 + lane * 16;
    const char* pw = wof + ((size_t)ct * 18 + wave) * 5 * 1024 + lane * 16;
    s8v fa[3][4], fw[3][5];
#pragma unroll
    for (int j = 0; j < 3; j++) {
        if (j < 2 || wave < 2) {
#pragma unroll
            for (int m = 0; m < 4; m++) fa[j][m] = *(const s8v*)(pa + m * (18 * 1024) + j * 8192);
#pragma unroll
            for (int n = 0; n < 5; n++) fw[j][n] = *(const s8v*)(pw + n * 1024 + j * 40960);
        }
    }
    const int row = r0 + (wave & 3) * 16 + (lane & 15);
    u2v xx[5];
#pragma unroll
    for (int n = 0; n < 5; n++) {
        int col = n * 16 + 4 * g; if (col > 68) col = 68;
        xx[n] = *(const u2v*)(x + (size_t)row * D_ + c0 + col);
    }
    f4v acc[4][5];
#pragma unroll
    for (int m = 0; m < 4; m++)
#pragma unroll
        for (int n = 0; n < 5; n++) acc[m][n] = f4v{0, 0, 0, 0};
#pragma unroll
    for (int j = 0; j < 3; j++) {
        if (j < 2 || wave < 2) {
#pragma unroll
            for (int n = 0; n < 5; n++)
#pragma unroll
                for (int m = 0; m < 4; m++) acc[m][n] = MFMA16(fw[j][n], fa[j][m], acc[m][n]);
        }
    }
    f4v* ex = (f4v*)smem + wave * (20 * 64) + lane;
#pragma unroll
    for (int m = 0; m < 4; m++)
#pragma unroll
        for (int n = 0; n < 5; n++) ex[(m * 5 + n) * 64] = acc[m][n];
    __syncthreads();
    if (wave >= 4) {
        return;
    }
    float hv[5][4];
    float sum = 0.f;
#pragma unroll
    for (int n = 0; n < 5; n++) {
        const f4v* e = (const f4v*)smem + (wave * 5 + n) * 64 + lane;
        f4v a = ((e[0] + e[1280]) + (e[2 * 1280] + e[3 * 1280])) + ((e[4 * 1280] + e[5 * 1280]) + (e[6 * 1280] + e[7 * 1280]));
        int col = n * 16 + 4 * g;
        if (col < 72) {
            unsigned p0 = pack_bf2(bf_lo(xx[n][0]) + a[0], bf_hi(xx[n][0]) + a[1]);
            unsigned p1 = pack_bf2(bf_lo(xx[n][1]) + a[2], bf_hi(xx[n][1]) + a[3]);
            *(u2v*)(h + (size_t)row * D_ + c0 + col) = u2v{p0, p1};
            {
                int cc = c0 + col;
                *(u2v*)(hT + (size_t)((row >> 4) * 18 + (cc >> 5)) * 1024 + (((cc & 31) >> 3) * 16 + (row & 15)) * 16 + (cc & 7) * 2) = u2v{p0, p1};
            }
            hv[n][0] = bf_lo(p0); hv[n][1] = bf_hi(p0); hv[n][2] = bf_lo(p1); hv[n][3] = bf_hi(p1);
            sum += hv[n][0] + hv[n][1] + hv[n][2] + hv[n][3];
        } else {
            hv[n][0] = hv[n][1] = hv[n][2] = hv[n][3] = 0.f;
        }
    }
    sum += __shfl_xor(sum, 16); sum += __shfl_xor(sum, 32);
    float mean = sum * (1.f / 72.f);
    float m2 = 0.f;
#pragma unroll
    for (int n = 0; n < 5; n++) {
        int col = n * 16 + 4 * g;
        if (col < 72) {
#pragma unroll
            for (int j = 0; j < 4; j++) { float d = hv[n][j] - mean; m2 += d * d; }
        }
    }
    m2 += __shfl_xor(m2, 16); m2 += __shfl_xor(m2, 32);
    if (g == 0) stats[(size_t)row * 8 + ct] = f2v{sum, m2};
}

constexpr int K3_STAGE = 8192 + 18432;
constexpr int K3_NB = 5;
constexpr int K3_PAR = K3_NB * K3_STAGE;
constexpr int K3_LDS = K3_PAR + 1024 + 2304;

__device__ __forceinline__ float gelu_tanh(float v) {
    float t = v * v;
    float z = v * (-2.302208198f * 0.044715f * t - 2.302208198f);
    float e = __builtin_amdgcn_exp2f(z);
    return v * __builtin_amdgcn_rcpf(1.f + e);
}

__device__ __forceinline__ f2v gelu_tanh2(f2v v) {
    f2v t = v * v;
    f2v z = v * (t * (-2.302208198f * 0.044715f) + (-2.302208198f));
    f2v d = f2v{__builtin_amdgcn_exp2f(z[0]), __builtin_amdgcn_exp2f(z[1])} + 1.f;
    return v * f2v{__builtin_amdgcn_rcpf(d[0]), __builtin_amdgcn_rcpf(d[1])};
}

__global__ __launch_bounds__(512, 2) void k3_up(const char* __restrict__ hT, const bf16_t* __restrict__ w1,
                                                const bf16_t* __restrict__ b1, const float* __restrict__ cs1,
                                                const f2v* __restrict__ stats, const bf16_t* __restrict__ w2,
                                                char* __restrict__ h1f, char* __restrict__ w2f) {
    extern __shared__ __attribute__((aligned(16))) char smem[];
    const int tid = threadIdx.x, lane = tid & 63;
    const int wave = __builtin_amdgcn_readfirstlane(tid >> 6);
    const int xcd = blockIdx.x & 7, idx = blockIdx.x >> 3;
    const int rt = (xcd & 1) * 8 + (idx & 7), ct = (xcd >> 1) * 4 + (idx >> 3);
    const int r0 = rt * 128, c0 = ct * 288;
    const unsigned lbase = lds_addr(smem);
    const int g = lane >> 4, q = (lane & 15) >> 2, p = lane & 3;
    const int xr = lane & 15;
    f2v* mu = (f2v*)(smem + K3_PAR);
    float* csl = (float*)(smem + K3_PAR + 1024);
    float* bbl = csl + 288;
    f4v acc[4][9];
    const int wm = (wave >> 1) & 1, wn = wave & 1;
    if (wave >= 4) {
        const int lw = wave - 4;
        unsigned bsrc[5];
        const unsigned avo = lane * 16;
        const char* ag = hT + (size_t)(rt * 8 + lw * 2) * 18 * 1024;
#pragma unroll
        for (int i = 0; i < 5; i++) {
            int bi = lw * 5 + i; if (bi > 17) bi = 17;
            int off = bi * 1024 + lane * 16;
            int k = off / 576, r = off - k * 576;
            int nt = (r >> 5) ^ ((k >> 3) & 1);
            bsrc[i] = (unsigned)((k * F_ + c0 + nt * 16 + ((r >> 4) & 1) * 8) * 2);
        }
        auto issue = [&](int st) {
            int sc = st > 17 ? 17 : st;
            unsigned lb = lbase + (st % K3_NB) * K3_STAGE;
            const char* ga = ag + sc * 1024;
            const char* gb = (const char*)w1 + (size_t)sc * 32 * F_ * 2;
            glds16s(ga, avo, lb + (lw * 2) * 1024);
            glds16s(ga + 18 * 1024, avo, lb + (lw * 2 + 1) * 1024);
#pragma unroll
            for (int i = 0; i < 5; i++) glds16s(gb, bsrc[i], lb + 8192 + (lw * 5 + i > 17 ? 17 : lw * 5 + i) * 1024);
        };
#pragma unroll
        for (int st = 0; st < K3_NB - 1; st++) issue(st);
        WAIT_V(21);
        BAR;
        for (int i = -1; i < 18; i++) {
            if (i >= 0) issue(i + K3_NB - 1);
            WAIT_V(14);
            BAR;
        }
        WAIT_V(0);
    } else {
        {
            float c0v = 0.f, b0v = bf2f(b1[c0 + tid]);
            float c1v = 0.f, b1v = 0.f;
            {
                float t0[12], t1[12];
                const int c1i = c0 + 256 + (tid & 31);
#pragma unroll
                for (int i = 0; i < 12; i++) { t0[i] = cs1[i * F_ + c0 + tid]; t1[i] = cs1[i * F_ + c1i]; }
                b1v = bf2f(b1[c1i]);
#pragma unroll
                for (int i = 0; i < 12; i++) { c0v += t0[i]; c1v += t1[i]; }
            }
            if (tid < 128) {
                const f4v* sp = (const f4v*)(stats + (size_t)(r0 + tid) * 8);
                f4v v[4];
#pragma unroll
                for (int i = 0; i < 4; i++) v[i] = sp[i];
                float tot = 0.f;
#pragma unroll
                for (int i = 0; i < 4; i++) tot += v[i][0] + v[i][2];
                float mean = tot * (1.f / 576.f);
                float M2 = 0.f;
#pragma unroll
                for (int i = 0; i < 4; i++) {
                    float d0 = v[i][0] * (1.f / 72.f) - mean, d1 = v[i][2] * (1.f / 72.f) - mean;
                    M2 += v[i][1] + v[i][3] + 72.f * (d0 * d0 + d1 * d1);
                }
                float rstd = rsqrtf(M2 * (1.f / 576.f) + 1e-5f);
                mu[tid] = f2v{mean * rstd, rstd};
            }
            csl[tid] = c0v; bbl[tid] = b0v;
            if (tid < 32) { csl[256 + tid] = c1v; bbl[256 + tid] = b1v; }
        }
#pragma unroll
        for (int m = 0; m < 4; m++)
#pragma unroll
            for (int n = 0; n < 9; n++) acc[m][n] = f4v{0, 0, 0, 0};
        const int dlt = (g & 1) ? 32 : 0;
        const int wb0 = 8192 + (8 * g + q) * 576 + wn * 9 * 32 + 8 * p;
        const int wbe = wb0 + ((wn & 1) ? -dlt : dlt);
        const int wbo = wb0 + ((wn & 1) ? dlt : -dlt);
        const int xoff = wm * 4096 + lane * 16;
        s8v xa[4], xb[4], wf[9];
        BAR;
        {
            const char* base = smem;
#pragma unroll
            for (int m = 0; m < 4; m++) xa[m] = *(const s8v*)(base + xoff + m * 1024);
#pragma unroll
            for (int n = 0; n < 9; n++) {
                const char* wb = base + ((n & 1) ? wbo : wbe) + n * 32;
                wf[n] = cat8(tr_read(wb), tr_read(wb + 4 * 576));
            }
        }
        WAIT_L(0);
        BAR;
#define K3_STEP(XC, XN) { \
            const char* base = smem + ((i + 1) % K3_NB) * K3_STAGE; \
            s4v wlo, whi; \
            _Pragma("unroll") for (int n = 0; n < 9; n++) { \
                const char* wbp = base + (((n - 1) & 1) ? wbo : wbe) + (n - 1) * 32; \
                acc[0][n] = MFMA16(wf[n], XC[0], acc[0][n]); SB0; \
                if (n > 0) { wlo = tr_read(wbp); SB0; } \
                acc[1][n] = MFMA16(wf[n], XC[1], acc[1][n]); SB0; \
                if (n > 0) { whi = tr_read(wbp + 4 * 576); wf[n - 1] = cat8(wlo, whi); SB0; } \
                acc[2][n] = MFMA16(wf[n], XC[2], acc[2][n]); SB0; \
                if (n < 4) { XN[n] = *(const s8v*)(base + xoff + n * 1024); SB0; } \
                acc[3][n] = MFMA16(wf[n], XC[3], acc[3][n]); SB0; \
            } \
            { const char* wbp = base + wbe + 8 * 32; wf[8] = cat8(tr_read(wbp), tr_read(wbp + 4 * 576)); } \
            BAR; }
#pragma unroll 1
        for (int i = 0; i < 18; i += 2) {
            K3_STEP(xa, xb);
            i++; K3_STEP(xb, xa); i--;
        }
    }
    __syncthreads();
    const int u = wave * 256 + blockIdx.x;
    if (u < 1152) {
        const int ctp = u / 144, kst = u - ctp * 144;
#pragma unroll
        for (int j = 0; j < 5; j++) {
            int c = j * 64 + lane; if (c > 287) c = 287;
            int row = c / 9, pc = c - row * 9;
            glds16s(w2, (unsigned)(((kst * 32 + row) * D_ + ctp * 72 + pc * 8) * 2), lbase + 98304 + wave * 5120 + j * 1024);
        }
    }
    f4v* ex = (f4v*)smem + (wave & 3) * (18 * 64) + lane;
    if (wave < 4) {
#pragma unroll
        for (int m = 0; m < 2; m++)
#pragma unroll
            for (int n = 0; n < 9; n++) ex[(m * 9 + n) * 64] = acc[m + 2][n];
    }
    __syncthreads();
    if (wave >= 4) {
#pragma unroll
        for (int m = 0; m < 2; m++)
#pragma unroll
            for (int n = 0; n < 9; n++) acc[m][n] = ex[(m * 9 + n) * 64];
    }
    {
        const int mb = (wave >> 2) * 2;
#pragma unroll
        for (int m = 0; m < 2; m++) {
            int rl = wm * 64 + (mb + m) * 16 + xr;
            f2v ms = mu[rl];
            char* ob = h1f + (size_t)((r0 + wm * 64 + (mb + m) * 16) >> 4) * (144 * 1024) + lane * 16;
            u2v pv[9];
#pragma unroll
            for (int n = 0; n < 9; n++) {
                f4v a = acc[m][n];
                int cl = wn * 144 + n * 16 + 4 * g;
                f4v cs = *(const f4v*)(csl + cl), bb = *(const f4v*)(bbl + cl);
                f2v t01 = f2v{bb[0], bb[1]} - f2v{cs[0], cs[1]} * ms[0];
                f2v t23 = f2v{bb[2], bb[3]} - f2v{cs[2], cs[3]} * ms[0];
                f2v v01 = gelu_tanh2(f2v{a[0], a[1]} * ms[1] + t01);
                f2v v23 = gelu_tanh2(f2v{a[2], a[3]} * ms[1] + t23);
                pv[n] = u2v{pack_bf2(v01[0], v01[1]), pack_bf2(v23[0], v23[1])};
            }
            if (m == 0) {
            } else {
            }
            char* oc = ob + (c0 >> 5) * 1024;
            if (wn == 0) {
#pragma unroll
                for (int j = 0; j < 4; j++) *(u4v*)(oc + j * 1024) = u4v{pv[2 * j][0], pv[2 * j][1], pv[2 * j + 1][0], pv[2 * j + 1][1]};
                *(u2v*)(oc + 4 * 1024) = pv[8];
            } else {
                *(u2v*)(oc + 4 * 1024 + 8) = pv[0];
#pragma unroll
                for (int j = 0; j < 4; j++) *(u4v*)(oc + (5 + j) * 1024) = u4v{pv[2 * j + 1][0], pv[2 * j + 1][1], pv[2 * j + 2][0], pv[2 * j + 2][1]};
            }
        }
    }
    WAIT_V(0);
    if (u < 1152) {
        const char* sb = smem + 98304 + wave * 5120 + (4 * g + q) * 144 + 8 * p;
        char* dst = w2f + (size_t)u * 5 * 1024 + lane * 16;
#pragma unroll
        for (int nt = 0; nt < 5; nt++)
            *(s8v*)(dst + nt * 1024) = cat8(tr_read(sb + nt * 32), tr_read(sb + 16 * 144 + nt * 32));
    }
}

constexpr int K4_LDS = 8 * 20 * 1024;
constexpr int K4_D = 3;

__global__ __launch_bounds__(512, 2) void k4_down(const char* __restrict__ h1f, const char* __restrict__ w2f,
                                                  const bf16_t* __restrict__ h, const bf16_t* __restrict__ b2,
                                                  bf16_t* __restrict__ out) {
    extern __shared__ __attribute__((aligned(16))) char smem[];
    const int tid = threadIdx.x, lane = tid & 63;
    const int wave = __builtin_amdgcn_readfirstlane(tid >> 6);
    const int xcd = blockIdx.x & 7, idx = blockIdx.x >> 3;
    const int rt = (xcd >> 1) * 8 + (idx & 7), ct = (xcd & 1) * 4 + (idx >> 3);
    const int g = lane >> 4;
    const char* pa = h1f + ((size_t)rt * 4 * 144 + wave) * 1024 + lane * 16;
    const char* pw = w2f + ((size_t)ct * 144 + wave) * 5 * 1024 + lane * 16;
    f4v acc[4][5];
#pragma unroll
    for (int m = 0; m < 4; m++)
#pragma unroll
        for (int n = 0; n < 5; n++) acc[m][n] = f4v{0, 0, 0, 0};
    s8v fa[K4_D][4], fw[K4_D][5];
#pragma unroll
    for (int j = 0; j < K4_D; j++) {
#pragma unroll
        for (int m = 0; m < 4; m++) fa[j][m] = *(const s8v*)(pa + m * (144 * 1024) + j * 8192);
#pragma unroll
        for (int n = 0; n < 5; n++) fw[j][n] = *(const s8v*)(pw + n * 1024 + j * 40960);
        SB0;
    }
#pragma unroll
    for (int j = 0; j < 18; j++) {
        const int sl = j % K4_D;
        SB0;
#pragma unroll
        for (int n = 0; n < 5; n++)
#pragma unroll
            for (int m = 0; m < 4; m++) acc[m][n] = MFMA16(fw[sl][n], fa[sl][m], acc[m][n]);
        SB0;
        if (j + K4_D < 18) {
#pragma unroll
            for (int m = 0; m < 4; m++) fa[sl][m] = *(const s8v*)(pa + m * (144 * 1024) + (j + K4_D) * 8192);
#pragma unroll
            for (int n = 0; n < 5; n++) fw[sl][n] = *(const s8v*)(pw + n * 1024 + (j + K4_D) * 40960);
        }
        SB0;
    }
    u2v hh[3], bb[3];
#pragma unroll
    for (int j = 0; j < 3; j++) {
        int t = wave + 8 * j; if (t > 19) t = 19;
        const int m = t / 5, n = t - m * 5;
        const int row = rt * 64 + m * 16 + (lane & 15);
        int cl = n * 16 + 4 * g; if (cl > 68) cl = 68;
        hh[j] = *(const u2v*)(h + (size_t)row * D_ + ct * 72 + cl);
        bb[j] = *(const u2v*)(b2 + ct * 72 + cl);
    }
    f4v* ex = (f4v*)smem + wave * (20 * 64) + lane;
#pragma unroll
    for (int m = 0; m < 4; m++)
#pragma unroll
        for (int n = 0; n < 5; n++) ex[(m * 5 + n) * 64] = acc[m][n];
    __syncthreads();
#pragma unroll
    for (int j = 0; j < 3; j++) {
        const int t = wave + 8 * j;
        if (t < 20) {
            const int m = t / 5, n = t - m * 5;
            const f4v* e = (const f4v*)smem + t * 64 + lane;
            f4v a = ((e[0] + e[1280]) + (e[2 * 1280] + e[3 * 1280])) + ((e[4 * 1280] + e[5 * 1280]) + (e[6 * 1280] + e[7 * 1280]));
            const int row = rt * 64 + m * 16 + (lane & 15);
            const int cl = n * 16 + 4 * g;
            float v0 = bf_lo(hh[j][0]) + (a[0] + bf_lo(bb[j][0]));
            float v1 = bf_hi(hh[j][0]) + (a[1] + bf_hi(bb[j][0]));
            float v2 = bf_lo(hh[j][1]) + (a[2] + bf_lo(bb[j][1]));
            float v3 = bf_hi(hh[j][1]) + (a[3] + bf_hi(bb[j][1]));
            if (cl < 72) *(u2v*)(out + (size_t)row * D_ + ct * 72 + cl) = u2v{pack_bf2(v0, v1), pack_bf2(v2, v3)};
        }
    }
}

extern "C" void kernel_launch(void* const* d_in, const int* in_sizes, int n_in,
                              void* d_out, int out_size, void* d_ws, size_t ws_size,
                              hipStream_t stream) {
    const bf16_t* x = (const bf16_t*)d_in[0];
    const bf16_t* wqkv = (const bf16_t*)d_in[1];
    const bf16_t* wo = (const bf16_t*)d_in[2];
    const bf16_t* w1 = (const bf16_t*)d_in[4];
    const bf16_t* b1 = (const bf16_t*)d_in[5];
    const bf16_t* w2 = (const bf16_t*)d_in[6];
    const bf16_t* b2 = (const bf16_t*)d_in[7];
    bf16_t* out = (bf16_t*)d_out;
    char* ws = (char*)d_ws;
    char* of = ws;
    char* wof = ws + (3 << 20);
    bf16_t* h = (bf16_t*)(ws + (4 << 20));
    char* hT = ws + (12 << 20);
    f2v* stats = (f2v*)(ws + (8 << 20));
    float* cs1 = (float*)(ws + (9 << 20));
    char* h1 = (char*)(ws + (16 << 20));
    char* w2f = ws + (48 << 20);

    hipFuncSetAttribute((const void*)k1_qkv_attn, hipFuncAttributeMaxDynamicSharedMemorySize, K1_LDS);
    hipFuncSetAttribute((const void*)k2_wo, hipFuncAttributeMaxDynamicSharedMemorySize, K2_LDS);
    hipFuncSetAttribute((const void*)k3_up, hipFuncAttributeMaxDynamicSharedMemorySize, K3_LDS);
    hipFuncSetAttribute((const void*)k4_down, hipFuncAttributeMaxDynamicSharedMemorySize, K4_LDS);

    k1_qkv_attn<<<256, 512, K1_LDS, stream>>>(x, wqkv, w1, wo, of, wof, cs1);
    k2_wo<<<256, 512, K2_LDS, stream>>>(of, wof, x, h, hT, stats);
    k3_up<<<256, 512, K3_LDS, stream>>>(hT, w1, b1, cs1, stats, w2, h1, w2f);
    k4_down<<<256, 512, K4_LDS, stream>>>(h1, w2f, h, b2, out);
}
```

```cpp
#include <hip/hip_runtime.h>
#include <hip/hip_bf16.h>

typedef __attribute__((ext_vector_type(8))) short s8v;
typedef __attribute__((ext_vector_type(4))) short s4v;
typedef __attribute__((ext_vector_type(4))) short v4i16_t;
typedef __attribute__((ext_vector_type(4))) float f4v;
typedef __attribute__((ext_vector_type(2))) float f2v;
typedef __attribute__((ext_vector_type(4))) unsigned u4v;
typedef __attribute__((ext_vector_type(2))) unsigned u2v;
typedef unsigned short bf16_t;

constexpr int S_ = 2048, D_ = 576, NQKV = 1728, F_ = 4608;

__device__ __forceinline__ unsigned lds_addr(const void* l) {
    return (unsigned)(size_t)(__attribute__((address_space(3))) const char*)l;
}
__device__ __forceinline__ void glds16(const void* g, unsigned dst) {
    unsigned keep;
    asm volatile("s_mov_b32 %0, m0\n\ts_mov_b32 m0, %2\n\ts_nop 0\n\tglobal_load_lds_dwordx4 %1, off\n\ts_mov_b32 m0, %0"
                 : "=&s"(keep) : "v"(g), "s"(dst) : "memory");
}
__device__ __forceinline__ void glds16s(const void* sbase, unsigned voff, unsigned dst) {
    unsigned keep;
    asm volatile("s_mov_b32 %0, m0\n\ts_mov_b32 m0, %3\n\ts_nop 0\n\tglobal_load_lds_dwordx4 %1, %2\n\ts_mov_b32 m0, %0"
                 : "=&s"(keep) : "v"(voff), "s"(sbase), "s"(dst) : "memory");
}
__device__ __forceinline__ s4v tr_read(const char* p) {
    return __builtin_bit_cast(s4v, __builtin_amdgcn_ds_read_tr16_b64_v4i16((__attribute__((address_space(3))) v4i16_t*)p));
}
__device__ __forceinline__ s8v cat8(s4v a, s4v b) {
    return __builtin_shufflevector(a, b, 0, 1, 2, 3, 4, 5, 6, 7);
}
__device__ __forceinline__ float bf_lo(unsigned u) { return __builtin_bit_cast(float, u << 16); }
__device__ __forceinline__ float bf_hi(unsigned u) { return __builtin_bit_cast(float, u & 0xffff0000u); }
__device__ __forceinline__ float bf2f(bf16_t v) { return __builtin_bit_cast(float, (unsigned)v << 16); }
__device__ __forceinline__ unsigned pack_bf2(float a, float b) {
    unsigned r;
    asm("v_cvt_pk_bf16_f32 %0, %1, %2" : "=v"(r) : "v"(a), "v"(b));
    return r;
}
#define MFMA16(a, b, c) __builtin_amdgcn_mfma_f32_16x16x32_bf16(a, b, c, 0, 0, 0)
#define WAIT_V(n) asm volatile("s_waitcnt vmcnt(" #n ")" ::: "memory")
#define WAIT_L(n) asm volatile("s_waitcnt lgkmcnt(" #n ")" ::: "memory")

#define SB0 __builtin_amdgcn_sched_barrier(0)
#define BAR __builtin_amdgcn_s_barrier()
constexpr int K1_XN_BYTES = 9 * 80 * 128;
constexpr int K1_STAGE = 14336;
constexpr int K1_NB = 4;
constexpr int K1_DUMMY = K1_XN_BYTES + K1_NB * K1_STAGE;
constexpr int K1_RED = K1_DUMMY + 5120;
constexpr int K1_LDS = K1_RED + 2048;

__global__ __launch_bounds__(512, 2) void k1_qkv_attn(const bf16_t* __restrict__ x, const bf16_t* __restrict__ wqkv,
                                                      const bf16_t* __restrict__ w1, const bf16_t* __restrict__ wo,
                                                      char* __restrict__ of, char* __restrict__ wof,
                                                      float* __restrict__ cs1) {
    extern __shared__ __attribute__((aligned(16))) char smem[];
    const int tid = threadIdx.x, lane = tid & 63;
    const int wave = __builtin_amdgcn_readfirstlane(tid >> 6);
    const int xcd = blockIdx.x & 7, idx = blockIdx.x >> 3;
    const int hg = (xcd & 1) * 4 + (idx >> 3), rt = (xcd >> 1) * 8 + (idx & 7);
    const int s0 = rt * 64;
    const unsigned lring = lds_addr(smem) + K1_XN_BYTES;
    const int g = lane >> 4, q = (lane & 15) >> 2, p = lane & 3;

    const int lw = wave - 4;
    unsigned bsrc[4];
    const int pc0 = lw < 2 ? lw * 4 : 2 + lw * 3;
    const int rot = ((idx & 7) + 2 * (idx >> 3)) % 9;
    auto rblk = [&](int b) { int t = b + rot; return t >= 9 ? t - 9 : t; };
    auto issue = [&](int st) {
        int sc = st > 17 ? 17 : st;
        sc = rblk(sc >> 1) * 2 + (sc & 1);
        unsigned lb = lring + (st % K1_NB) * K1_STAGE;
        const char* gb = (const char*)wqkv + (size_t)sc * 32 * NQKV * 2;
#pragma unroll
        for (int i = 0; i < 3; i++) glds16s(gb, bsrc[i], lb + (pc0 + i) * 1024);
        if (lw < 2) glds16s(gb, bsrc[3], lb + (pc0 + 3) * 1024);
    };
    const unsigned dz = lds_addr(smem) + K1_DUMMY + (wave & 3) * 1024;
    float* mus = (float*)(smem + K1_RED);
    unsigned xo[3];
    auto xissue = [&](int b0) {
        const int b = rblk(b0);
#pragma unroll
        for (int j = 0; j < 3; j++)
            glds16s((const char*)x + b * 128, xo[j], lds_addr(smem) + b * (80 * 128) + (j == 2 && wave >= 2 ? wave + 4 : wave + 4 * j) * 1024);
    };
    if (wave >= 4) {
#pragma unroll
        for (int i = 0; i < 4; i++) {
            int bi = pc0 + i; if (bi > 13) bi = 13;
            int off = bi * 1024 + lane * 16;
            int k = off / 448, r = off - k * 448;
            int nt = (r >> 5) ^ ((k >> 3) & 1);
            int pc = nt * 2 + ((r >> 4) & 1); if (pc > 26) pc = 26;
            int sec = pc / 9, wi = pc - sec * 9;
            bsrc[i] = (unsigned)((k * NQKV + sec * 576 + hg * 72 + wi * 8) * 2);
        }
#pragma unroll
        for (int st = 0; st < K1_NB - 1; st++) issue(st);
    } else {
#pragma unroll
        for (int j = 0; j < 3; j++) {
            int gq = wave + 4 * j; if (gq > 9) gq = wave + 4;
            int row = gq * 8 + (lane >> 3);
            int s = s0 - 16 + row; if (s < 0) s = 0;
            xo[j] = (unsigned)(s * (D_ * 2) + (((lane & 7) ^ (lane >> 3)) << 4));
        }
#pragma unroll
        for (int b = 0; b < 3; b++) xissue(b);
    }

    f4v acc[5][4], acs[4], ssum[2], ssq[2];
    if (wave >= 4) {
        if (lw < 2) WAIT_V(8); else WAIT_V(6);
        __syncthreads();
        if (lw < 2) WAIT_V(4); else WAIT_V(3);
        BAR;
        for (int i = 0; i < 15; i++) {
            issue(i + K1_NB - 1);
            if (lw < 2) WAIT_V(4); else WAIT_V(3);
            BAR;
        }
        WAIT_V(0); BAR;
        BAR; BAR;
        {
            if (wave == 4 && blockIdx.x < 144) {
                const int ctp = blockIdx.x / 18, kst = blockIdx.x - ctp * 18;
#pragma unroll
                for (int j = 0; j < 5; j++) {
                    int c = j * 64 + lane; if (c > 287) c = 287;
                    int row = c / 9, pc = c - row * 9;
                    glds16s(wo, (unsigned)(((kst * 32 + row) * D_ + ctp * 72 + pc * 8) * 2), lds_addr(smem) + K1_DUMMY + j * 1024);
                }
            }
        }
    } else {
        const int nt0 = wave == 0 ? 0 : 1 + 3 * wave;
        const int nn = (wave == 0 || wave == 3) ? 4 : 3;
#pragma unroll
        for (int m = 0; m < 5; m++)
#pragma unroll
            for (int n = 0; n < 4; n++) acc[m][n] = f4v{0, 0, 0, 0};
#pragma unroll
        for (int n = 0; n < 4; n++) acs[n] = f4v{0, 0, 0, 0};
        ssum[0] = ssum[1] = ssq[0] = ssq[1] = f4v{0, 0, 0, 0};
        const s8v ones = s8v{0x3F80, 0x3F80, 0x3F80, 0x3F80, 0x3F80, 0x3F80, 0x3F80, 0x3F80};
        const int xr = lane & 15;
        int woff[4];
#pragma unroll
        for (int n = 0; n < 4; n++) woff[n] = K1_XN_BYTES + (8 * g + q) * 448 + (((nt0 + n) ^ (g & 1)) << 5) + 8 * p;
        s8v xa[5], xb[5], wf[4];
        auto rdx = [&](s8v* X, int st) {
            const char* xbp = smem + rblk(st >> 1) * (80 * 128) + xr * 128 + ((((st & 1) * 4 + g) ^ (xr & 7)) << 4);
#pragma unroll
            for (int m = 0; m < 5; m++) X[m] = *(const s8v*)(xbp + m * 2048);
        };
        WAIT_V(6);
        __syncthreads();
        rdx(xa, 0);
#pragma unroll
        for (int n = 0; n < 4; n++) wf[n] = cat8(tr_read(smem + woff[n]), tr_read(smem + woff[n] + 4 * 448));
        WAIT_L(0);
        BAR;
#define K1_STEP(W, XC, XN, PRE, POST) { \
            const char* base = smem + ((i + 1) % K1_NB) * K1_STAGE; \
            PRE; \
            rdx(XN, i + 1 > 17 ? 17 : i + 1); \
            SB0; \
            _Pragma("unroll") for (int n = 0; n < (W == 0 || W == 3 ? 4 : 3); n++) { \
                if (W != 0) acc[0][n] = MFMA16(wf[n], XC[0], acc[0][n]); \
                _Pragma("unroll") for (int m = 1; m < 5; m++) acc[m][n] = MFMA16(wf[n], XC[m], acc[m][n]); \
                acs[n] = MFMA16(wf[n], ones, acs[n]); \
                SB0; \
                wf[n] = cat8(tr_read(base + woff[n]), tr_read(base + woff[n] + 4 * 448)); \
                SB0; \
            } \
            if (W == 0) { ssum[0] = MFMA16(XC[0], ones, ssum[0]); ssq[0] = MFMA16(XC[0], XC[0], ssq[0]); } \
            if (W == 1) { ssum[0] = MFMA16(XC[1], ones, ssum[0]); ssq[0] = MFMA16(XC[1], XC[1], ssq[0]); \
                          ssum[1] = MFMA16(XC[2], ones, ssum[1]); ssq[1] = MFMA16(XC[2], XC[2], ssq[1]); } \
            if (W == 2) { ssum[0] = MFMA16(XC[3], ones, ssum[0]); ssq[0] = MFMA16(XC[3], XC[3], ssq[0]); \
                          ssum[1] = MFMA16(XC[4], ones, ssum[1]); ssq[1] = MFMA16(XC[4], XC[4], ssq[1]); } \
            SB0; \
            POST; \
            BAR; }
#define K1_LOOPS(W) { \
        _Pragma("unroll 1") for (int i = 0; i < 6; i += 2) { \
            K1_STEP(W, xa, xb, xissue(i + 3), WAIT_V(6)); \
            i++; K1_STEP(W, xb, xa, xissue(i + 3), WAIT_V(6)); i--; \
        } \
        _Pragma("unroll 1") for (int i = 6; i < 18; i += 2) { \
            K1_STEP(W, xa, xb, , WAIT_V(0)); \
            i++; K1_STEP(W, xb, xa, , WAIT_V(0)); i--; \
        } }
        if (wave == 0) K1_LOOPS(0) else if (wave == 1) K1_LOOPS(1) else if (wave == 2) K1_LOOPS(2) else K1_LOOPS(3)
        if (wave < 3 && q == g) {
#pragma unroll
            for (int t = 0; t < 2; t++) {
                if (t == 0 || wave > 0) {
                    const float mean = ssum[t][p] * (1.f / 576.f);
                    const float var = fmaxf(ssq[t][p] * (1.f / 576.f) - mean * mean, 0.f);
                    const int r = (wave == 0 ? 0 : 2 * wave - 1 + t) * 16 + (lane & 15);
                    mus[r] = mean; mus[80 + r] = rsqrtf(var + 1e-5f);
                }
            }
        }
    }
    __syncthreads();
    constexpr int RSB = 456;
    if (wave < 4) {
        const int nt0 = wave == 0 ? 0 : 1 + 3 * wave;
        const int nn = (wave == 0 || wave == 3) ? 4 : 3;
        int lco[4][2];
#pragma unroll
        for (int n = 0; n < 4; n++)
#pragma unroll
            for (int h2 = 0; h2 < 2; h2++) {
                const int c = (nt0 + n) * 16 + 4 * g + 2 * h2;
                const int hc = c - 144, hd = hc / 18;
                lco[n][h2] = (n < nn && c < 216) ? (c < 144 ? c : 144 + hd * 20 + (hc - hd * 18)) * 2 : -1;
            }
#pragma unroll
        for (int m = 0; m < 5; m++) {
            if (m > 0 || wave > 0) {
                const float mean = mus[m * 16 + (lane & 15)], rs = mus[80 + m * 16 + (lane & 15)];
                char* rowp = smem + (m * 16 + (lane & 15)) * RSB;
#pragma unroll
                for (int n = 0; n < 4; n++) {
                    const f4v v = (acc[m][n] - acs[n] * mean) * rs;
                    if (lco[n][0] >= 0) *(unsigned*)(rowp + lco[n][0]) = pack_bf2(v[0], v[1]);
                    if (lco[n][1] >= 0) *(unsigned*)(rowp + lco[n][1]) = pack_bf2(v[2], v[3]);
                }
            }
        }
    }
    __syncthreads();
    u4v cw[6];
    const int cu = blockIdx.x < 216 ? blockIdx.x : blockIdx.x - 216;
    const int cchunk = cu % 72, rthird = cu / 72;
    if (wave < 4) {
        const u4v* pq = (const u4v*)(w1 + (size_t)(rthird * 192 + wave * 48 + (lane >> 3)) * F_ + cchunk * 64) + (lane & 7);
#pragma unroll
        for (int j = 0; j < 6; j++) cw[j] = pq[(size_t)j * 8 * F_ / 8];
    }
    if (wave == 4 && blockIdx.x < 144) {
        WAIT_V(0);
        const char* sb = smem + K1_DUMMY + (8 * g + q) * 144 + 8 * p;
        char* dst = wof + (size_t)blockIdx.x * 5 * 1024 + lane * 16;
#pragma unroll
        for (int nt = 0; nt < 5; nt++)
            *(s8v*)(dst + nt * 1024) = cat8(tr_read(sb + nt * 32), tr_read(sb + 4 * 144 + nt * 32));
    }
    {
        const int head = wave >> 1, li = lane & 15;
        const unsigned qmask0 = g < 3 ? 0xffffffffu : 0u, qmask1 = g < 2 ? 0xffffffffu : 0u;
#pragma unroll
        for (int uu = 0; uu < 2; uu++) {
            const int t = (wave & 1) * 2 + uu;
            const char* qp = smem + (16 + 16 * t + li) * RSB + (head * 18 + 8 * g) * 2;
            const char* kp = smem + (16 * t + li) * RSB + (72 + head * 18 + 8 * g) * 2;
            u4v qf, kf0, kf1;
#pragma unroll
            for (int i = 0; i < 4; i++) {
                qf[i] = *(const unsigned*)(qp + 4 * i) & (i == 0 ? qmask0 : qmask1);
                kf0[i] = *(const unsigned*)(kp + 4 * i);
                kf1[i] = *(const unsigned*)(kp + 16 * RSB + 4 * i);
            }
            const char* vp = smem + (16 * t + 4 * g + q) * RSB + (144 + head * 20 + 4 * p) * 2;
            const s8v vf0 = cat8(tr_read(vp), tr_read(vp + 16 * RSB));
            const s8v vf1 = cat8(tr_read(vp + 32), tr_read(vp + 16 * RSB + 32));
            const f4v z4 = f4v{0.f, 0.f, 0.f, 0.f};
            const f4v s0v = MFMA16(__builtin_bit_cast(s8v, kf0), __builtin_bit_cast(s8v, qf), z4);
            const f4v s1v = MFMA16(__builtin_bit_cast(s8v, kf1), __builtin_bit_cast(s8v, qf), z4);
            const bool tile0_ok = !(s0 == 0 && t == 0);
            float a[8]; bool ok[8];
            float mx = -3.0e38f;
#pragma unroll
            for (int j = 0; j < 8; j++) {
                const int dl = 16 * (j >> 2) + 4 * g + (j & 3) - li;
                ok[j] = !(dl & 1) && dl >= 2 && dl <= 16 && (j >= 4 || tile0_ok);
                a[j] = j < 4 ? s0v[j & 3] : s1v[j & 3];
                mx = fmaxf(mx, ok[j] ? a[j] : -3.0e38f);
            }
            mx = fmaxf(mx, __shfl_xor(mx, 16)); mx = fmaxf(mx, __shfl_xor(mx, 32));
            float den = 0.f;
#pragma unroll
            for (int j = 0; j < 8; j++) {
                const float e = __builtin_amdgcn_exp2f((a[j] - mx) * (0.23570226039551584f * 1.4426950408889634f));
                a[j] = ok[j] ? e : 0.f;
            }
            const u4v pb = u4v{pack_bf2(a[0], a[1]), pack_bf2(a[2], a[3]), pack_bf2(a[4], a[5]), pack_bf2(a[6], a[7])};
#pragma unroll
            for (int j = 0; j < 4; j++) den += bf_lo(pb[j]) + bf_hi(pb[j]);
            den += __shfl_xor(den, 16); den += __shfl_xor(den, 32);
            const f4v o0 = MFMA16(vf0, __builtin_bit_cast(s8v, pb), z4) * (1.f / den);
            const f4v o1 = MFMA16(vf1, __builtin_bit_cast(s8v, pb), z4) * (1.f / den);
            char* ob = of + (size_t)((s0 >> 4) + t) * (18 * 1024) + li * 16;
            const int c0 = hg * 72 + head * 18 + 4 * g, c1 = c0 + 2, c2 = hg * 72 + head * 18 + 16;
            *(unsigned*)(ob + (c0 >> 5) * 1024 + ((c0 & 31) >> 3) * 256 + (c0 & 7) * 2) = pack_bf2(o0[0], o0[1]);
            *(unsigned*)(ob + (c1 >> 5) * 1024 + ((c1 & 31) >> 3) * 256 + (c1 & 7) * 2) = pack_bf2(o0[2], o0[3]);
            if (g == 0) *(unsigned*)(ob + (c2 >> 5) * 1024 + ((c2 & 31) >> 3) * 256 + (c2 & 7) * 2) = pack_bf2(o1[0], o1[1]);
        }
    }
    if (wave < 4) {
        float a[8];
#pragma unroll
        for (int j = 0; j < 8; j++) a[j] = 0.f;
#pragma unroll
        for (int i = 0; i < 6; i++)
#pragma unroll
            for (int j = 0; j < 4; j++) { a[2 * j] += bf_lo(cw[i][j]); a[2 * j + 1] += bf_hi(cw[i][j]); }
#pragma unroll
        for (int j = 0; j < 8; j++) {
            float t = a[j];
            t += __shfl_xor(t, 8); t += __shfl_xor(t, 16); t += __shfl_xor(t, 32);
            a[j] = t;
        }
        if (lane < 8 && blockIdx.x < 216) {
            float* rq = cs1 + (rthird * 4 + wave) * F_ + cchunk * 64 + lane * 8;
            *(f4v*)rq = f4v{a[0], a[1], a[2], a[3]};
            *(f4v*)(rq + 4) = f4v{a[4], a[5], a[6], a[7]};
        }
    }
}

constexpr int K2_LDS = 8 * 20 * 1024;

__global__ __launch_bounds__(512, 2) void k2_wo(const char* __restrict__ of, const char* __restrict__ wof,
                                                const bf16_t* __restrict__ x, bf16_t* __restrict__ h,
                                                char* __restrict__ hT, f2v* __restrict__ stats) {
    extern __shared__ __attribute__((aligned(16))) char smem[];
    const int tid = threadIdx.x, lane = tid & 63;
    const int wave = __builtin_amdgcn_readfirstlane(tid >> 6);
    const int rt = (blockIdx.x & 7) * 4 + ((blockIdx.x >> 3) & 3), ct = blockIdx.x >> 5;
    const int r0 = rt * 64, c0 = ct * 72;
    const int g = lane >> 4;
    const char* pa = of + ((size_t)rt * 4 * 18 + wave) * 1024 + lane * 16;
    const char* pw = wof + ((size_t)ct * 18 + wave) * 5 * 1024 + lane * 16;
    s8v fa[3][4], fw[3][5];
#pragma unroll
    for (int j = 0; j < 3; j++) {
        if (j < 2 || wave < 2) {
#pragma unroll
            for (int m = 0; m < 4; m++) fa[j][m] = *(const s8v*)(pa + m * (18 * 1024) + j * 8192);
#pragma unroll
            for (int n = 0; n < 5; n++) fw[j][n] = *(const s8v*)(pw + n * 1024 + j * 40960);
        }
    }
    const int row = r0 + (wave & 3) * 16 + (lane & 15);
    u2v xx[5];
#pragma unroll
    for (int n = 0; n < 5; n++) {
        int col = n * 16 + 4 * g; if (col > 68) col = 68;
        xx[n] = *(const u2v*)(x + (size_t)row * D_ + c0 + col);
    }
    f4v acc[4][5];
#pragma unroll
    for (int m = 0; m < 4; m++)
#pragma unroll
        for (int n = 0; n < 5; n++) acc[m][n] = f4v{0, 0, 0, 0};
#pragma unroll
    for (int j = 0; j < 3; j++) {
        if (j < 2 || wave < 2) {
#pragma unroll
            for (int n = 0; n < 5; n++)
#pragma unroll
                for (int m = 0; m < 4; m++) acc[m][n] = MFMA16(fw[j][n], fa[j][m], acc[m][n]);
        }
    }
    f4v* ex = (f4v*)smem + wave * (20 * 64) + lane;
#pragma unroll
    for (int m = 0; m < 4; m++)
#pragma unroll
        for (int n = 0; n < 5; n++) ex[(m * 5 + n) * 64] = acc[m][n];
    __syncthreads();
    if (wave >= 4) {
        return;
    }
    float hv[5][4];
    float sum = 0.f;
#pragma unroll
    for (int n = 0; n < 5; n++) {
        const f4v* e = (const f4v*)smem + (wave * 5 + n) * 64 + lane;
        f4v a = ((e[0] + e[1280]) + (e[2 * 1280] + e[3 * 1280])) + ((e[4 * 1280] + e[5 * 1280]) + (e[6 * 1280] + e[7 * 1280]));
        int col = n * 16 + 4 * g;
        if (col < 72) {
            unsigned p0 = pack_bf2(bf_lo(xx[n][0]) + a[0], bf_hi(xx[n][0]) + a[1]);
            unsigned p1 = pack_bf2(bf_lo(xx[n][1]) + a[2], bf_hi(xx[n][1]) + a[3]);
            *(u2v*)(h + (size_t)row * D_ + c0 + col) = u2v{p0, p1};
            {
                int cc = c0 + col;
                *(u2v*)(hT + (size_t)((row >> 4) * 18 + (cc >> 5)) * 1024 + (((cc & 31) >> 3) * 16 + (row & 15)) * 16 + (cc & 7) * 2) = u2v{p0, p1};
            }
            hv[n][0] = bf_lo(p0); hv[n][1] = bf_hi(p0); hv[n][2] = bf_lo(p1); hv[n][3] = bf_hi(p1);
            sum += hv[n][0] + hv[n][1] + hv[n][2] + hv[n][3];
        } else {
            hv[n][0] = hv[n][1] = hv[n][2] = hv[n][3] = 0.f;
        }
    }
    sum += __shfl_xor(sum, 16); sum += __shfl_xor(sum, 32);
    float mean = sum * (1.f / 72.f);
    float m2 = 0.f;
#pragma unroll
    for (int n = 0; n < 5; n++) {
        int col = n * 16 + 4 * g;
        if (col < 72) {
#pragma unroll
            for (int j = 0; j < 4; j++) { float d = hv[n][j] - mean; m2 += d * d; }
        }
    }
    m2 += __shfl_xor(m2, 16); m2 += __shfl_xor(m2, 32);
    if (g == 0) stats[(size_t)row * 8 + ct] = f2v{sum, m2};
}

constexpr int K3_STAGE = 8192 + 18432;
constexpr int K3_NB = 5;
constexpr int K3_PAR = K3_NB * K3_STAGE;
constexpr int K3_LDS = K3_PAR + 1024 + 2304;

__device__ __forceinline__ float gelu_tanh(float v) {
    float t = v * v;
    float z = v * (-2.302208198f * 0.044715f * t - 2.302208198f);
    float e = __builtin_amdgcn_exp2f(z);
    return v * __builtin_amdgcn_rcpf(1.f + e);
}

__device__ __forceinline__ f2v gelu_tanh2(f2v v) {
    f2v t = v * v;
    f2v z = v * (t * (-2.302208198f * 0.044715f) + (-2.302208198f));
    f2v d = f2v{__builtin_amdgcn_exp2f(z[0]), __builtin_amdgcn_exp2f(z[1])} + 1.f;
    return v * f2v{__builtin_amdgcn_rcpf(d[0]), __builtin_amdgcn_rcpf(d[1])};
}

__global__ __launch_bounds__(512, 2) void k3_up(const char* __restrict__ hT, const bf16_t* __restrict__ w1,
                                                const bf16_t* __restrict__ b1, const float* __restrict__ cs1,
                                                const f2v* __restrict__ stats, const bf16_t* __restrict__ w2,
                                                char* __restrict__ h1f, char* __restrict__ w2f) {
    extern __shared__ __attribute__((aligned(16))) char smem[];
    const int tid = threadIdx.x, lane = tid & 63;
    const int wave = __builtin_amdgcn_readfirstlane(tid >> 6);
    const int xcd = blockIdx.x & 7, idx = blockIdx.x >> 3;
    const int rt = (xcd & 1) * 8 + (idx & 7), ct = (xcd >> 1) * 4 + (idx >> 3);
    const int r0 = rt * 128, c0 = ct * 288;
    const unsigned lbase = lds_addr(smem);
    const int g = lane >> 4, q = (lane & 15) >> 2, p = lane & 3;
    const int xr = lane & 15;
    f2v* mu = (f2v*)(smem + K3_PAR);
    float* csl = (float*)(smem + K3_PAR + 1024);
    float* bbl = csl + 288;
    f4v acc[4][9];
    const int wm = (wave >> 1) & 1, wn = wave & 1;
    if (wave >= 4) {
        const int lw = wave - 4;
        unsigned bsrc[5];
        const unsigned avo = lane * 16;
        const char* ag = hT + (size_t)(rt * 8 + lw * 2) * 18 * 1024;
#pragma unroll
        for (int i = 0; i < 5; i++) {
            int bi = lw * 5 + i; if (bi > 17) bi = 17;
            int off = bi * 1024 + lane * 16;
            int k = off / 576, r = off - k * 576;
            int nt = (r >> 5) ^ ((k >> 3) & 1);
            bsrc[i] = (unsigned)((k * F_ + c0 + nt * 16 + ((r >> 4) & 1) * 8) * 2);
        }
        auto issue = [&](int st) {
            int sc = st > 17 ? 17 : st;
            unsigned lb = lbase + (st % K3_NB) * K3_STAGE;
            const char* ga = ag + sc * 1024;
            const char* gb = (const char*)w1 + (size_t)sc * 32 * F_ * 2;
            glds16s(ga, avo, lb + (lw * 2) * 1024);
            glds16s(ga + 18 * 1024, avo, lb + (lw * 2 + 1) * 1024);
#pragma unroll
            for (int i = 0; i < 5; i++) glds16s(gb, bsrc[i], lb + 8192 + (lw * 5 + i > 17 ? 17 : lw * 5 + i) * 1024);
        };
#pragma unroll
        for (int st = 0; st < K3_NB - 1; st++) issue(st);
        WAIT_V(21);
        BAR;
        for (int i = -1; i < 18; i++) {
            if (i >= 0) issue(i + K3_NB - 1);
            WAIT_V(14);
            BAR;
        }
        WAIT_V(0);
    } else {
        {
            float c0v = 0.f, b0v = bf2f(b1[c0 + tid]);
            float c1v = 0.f, b1v = 0.f;
            {
                float t0[12], t1[12];
                const int c1i = c0 + 256 + (tid & 31);
#pragma unroll
                for (int i = 0; i < 12; i++) { t0[i] = cs1[i * F_ + c0 + tid]; t1[i] = cs1[i * F_ + c1i]; }
                b1v = bf2f(b1[c1i]);
#pragma unroll
                for (int i = 0; i < 12; i++) { c0v += t0[i]; c1v += t1[i]; }
            }
            if (tid < 128) {
                const f4v* sp = (const f4v*)(stats + (size_t)(r0 + tid) * 8);
                f4v v[4];
#pragma unroll
                for (int i = 0; i < 4; i++) v[i] = sp[i];
                float tot = 0.f;
#pragma unroll
                for (int i = 0; i < 4; i++) tot += v[i][0] + v[i][2];
                float mean = tot * (1.f / 576.f);
                float M2 = 0.f;
#pragma unroll
                for (int i = 0; i < 4; i++) {
                    float d0 = v[i][0] * (1.f / 72.f) - mean, d1 = v[i][2] * (1.f / 72.f) - mean;
                    M2 += v[i][1] + v[i][3] + 72.f * (d0 * d0 + d1 * d1);
                }
                float rstd = rsqrtf(M2 * (1.f / 576.f) + 1e-5f);
                mu[tid] = f2v{mean * rstd, rstd};
            }
            csl[tid] = c0v; bbl[tid] = b0v;
            if (tid < 32) { csl[256 + tid] = c1v; bbl[256 + tid] = b1v; }
        }
#pragma unroll
        for (int m = 0; m < 4; m++)
#pragma unroll
            for (int n = 0; n < 9; n++) acc[m][n] = f4v{0, 0, 0, 0};
        const int dlt = (g & 1) ? 32 : 0;
        const int wb0 = 8192 + (8 * g + q) * 576 + wn * 9 * 32 + 8 * p;
        const int wbe = wb0 + ((wn & 1) ? -dlt : dlt);
        const int wbo = wb0 + ((wn & 1) ? dlt : -dlt);
        const int xoff = wm * 4096 + lane * 16;
        s8v xa[4], xb[4], wf[9];
        BAR;
        {
            const char* base = smem;
#pragma unroll
            for (int m = 0; m < 4; m++) xa[m] = *(const s8v*)(base + xoff + m * 1024);
#pragma unroll
            for (int n = 0; n < 9; n++) {
                const char* wb = base + ((n & 1) ? wbo : wbe) + n * 32;
                wf[n] = cat8(tr_read(wb), tr_read(wb + 4 * 576));
            }
        }
        WAIT_L(0);
        BAR;
#define K3_STEP(XC, XN) { \
            const char* base = smem + ((i + 1) % K3_NB) * K3_STAGE; \
            s4v wlo, whi; \
            _Pragma("unroll") for (int n = 0; n < 9; n++) { \
                const char* wbp = base + (((n - 1) & 1) ? wbo : wbe) + (n - 1) * 32; \
                acc[0][n] = MFMA16(wf[n], XC[0], acc[0][n]); SB0; \
                if (n > 0) { wlo = tr_read(wbp); SB0; } \
                acc[1][n] = MFMA16(wf[n], XC[1], acc[1][n]); SB0; \
                if (n > 0) { whi = tr_read(wbp + 4 * 576); wf[n - 1] = cat8(wlo, whi); SB0; } \
                acc[2][n] = MFMA16(wf[n], XC[2], acc[2][n]); SB0; \
                if (n < 4) { XN[n] = *(const s8v*)(base + xoff + n * 1024); SB0; } \
                acc[3][n] = MFMA16(wf[n], XC[3], acc[3][n]); SB0; \
            } \
            { const char* wbp = base + wbe + 8 * 32; wf[8] = cat8(tr_read(wbp), tr_read(wbp + 4 * 576)); } \
            BAR; }
#pragma unroll 1
        for (int i = 0; i < 18; i += 2) {
            K3_STEP(xa, xb);
            i++; K3_STEP(xb, xa); i--;
        }
    }
    __syncthreads();
    const int u = wave * 256 + blockIdx.x;
    if (u < 1152) {
        const int ctp = u / 144, kst = u - ctp * 144;
#pragma unroll
        for (int j = 0; j < 5; j++) {
            int c = j * 64 + lane; if (c > 287) c = 287;
            int row = c / 9, pc = c - row * 9;
            glds16s(w2, (unsigned)(((kst * 32 + row) * D_ + ctp * 72 + pc * 8) * 2), lbase + 98304 + wave * 5120 + j * 1024);
        }
    }
    f4v* ex = (f4v*)smem + (wave & 3) * (18 * 64) + lane;
    if (wave < 4) {
#pragma unroll
        for (int m = 0; m < 2; m++)
#pragma unroll
            for (int n = 0; n < 9; n++) ex[(m * 9 + n) * 64] = acc[m + 2][n];
    }
    __syncthreads();
    if (wave >= 4) {
#pragma unroll
        for (int m = 0; m < 2; m++)
#pragma unroll
            for (int n = 0; n < 9; n++) acc[m][n] = ex[(m * 9 + n) * 64];
    }
    {
        const int mb = (wave >> 2) * 2;
#pragma unroll
        for (int m = 0; m < 2; m++) {
            int rl = wm * 64 + (mb + m) * 16 + xr;
            f2v ms = mu[rl];
            char* ob = h1f + (size_t)((r0 + wm * 64 + (mb + m) * 16) >> 4) * (144 * 1024) + lane * 16;
            u2v pv[9];
#pragma unroll
            for (int n = 0; n < 9; n++) {
                f4v a = acc[m][n];
                int cl = wn * 144 + n * 16 + 4 * g;
                f4v cs = *(const f4v*)(csl + cl), bb = *(const f4v*)(bbl + cl);
                f2v t01 = f2v{bb[0], bb[1]} - f2v{cs[0], cs[1]} * ms[0];
                f2v t23 = f2v{bb[2], bb[3]} - f2v{cs[2], cs[3]} * ms[0];
                f2v v01 = gelu_tanh2(f2v{a[0], a[1]} * ms[1] + t01);
                f2v v23 = gelu_tanh2(f2v{a[2], a[3]} * ms[1] + t23);
                pv[n] = u2v{pack_bf2(v01[0], v01[1]), pack_bf2(v23[0], v23[1])};
            }
            if (m == 0) {
            } else {
            }
            char* oc = ob + (c0 >> 5) * 1024;
            if (wn == 0) {
#pragma unroll
                for (int j = 0; j < 4; j++) *(u4v*)(oc + j * 1024) = u4v{pv[2 * j][0], pv[2 * j][1], pv[2 * j + 1][0], pv[2 * j + 1][1]};
                *(u2v*)(oc + 4 * 1024) = pv[8];
            } else {
                *(u2v*)(oc + 4 * 1024 + 8) = pv[0];
#pragma unroll
                for (int j = 0; j < 4; j++) *(u4v*)(oc + (5 + j) * 1024) = u4v{pv[2 * j + 1][0], pv[2 * j + 1][1], pv[2 * j + 2][0], pv[2 * j + 2][1]};
            }
        }
    }
    WAIT_V(0);
    if (u < 1152) {
        const char* sb = smem + 98304 + wave * 5120 + (4 * g + q) * 144 + 8 * p;
        char* dst = w2f + (size_t)u * 5 * 1024 + lane * 16;
#pragma unroll
        for (int nt = 0; nt < 5; nt++)
            *(s8v*)(dst + nt * 1024) = cat8(tr_read(sb + nt * 32), tr_read(sb + 16 * 144 + nt * 32));
    }
}

constexpr int K4_LDS = 8 * 20 * 1024;
constexpr int K4_D = 3;

__global__ __launch_bounds__(512, 2) void k4_down(const char* __restrict__ h1f, const char* __restrict__ w2f,
                                                  const bf16_t* __restrict__ h, const bf16_t* __restrict__ b2,
                                                  bf16_t* __restrict__ out) {
    extern __shared__ __attribute__((aligned(16))) char smem[];
    const int tid = threadIdx.x, lane = tid & 63;
    const int wave = __builtin_amdgcn_readfirstlane(tid >> 6);
    const int xcd = blockIdx.x & 7, idx = blockIdx.x >> 3;
    const int rt = (xcd >> 1) * 8 + (idx & 7), ct = (xcd & 1) * 4 + (idx >> 3);
    const int g = lane >> 4;
    const char* pa = h1f + ((size_t)rt * 4 * 144 + wave) * 1024 + lane * 16;
    const char* pw = w2f + ((size_t)ct * 144 + wave) * 5 * 1024 + lane * 16;
    f4v acc[4][5];
#pragma unroll
    for (int m = 0; m < 4; m++)
#pragma unroll
        for (int n = 0; n < 5; n++) acc[m][n] = f4v{0, 0, 0, 0};
    s8v fa[K4_D][4], fw[K4_D][5];
#pragma unroll
    for (int j = 0; j < K4_D; j++) {
#pragma unroll
        for (int m = 0; m < 4; m++) fa[j][m] = *(const s8v*)(pa + m * (144 * 1024) + j * 8192);
#pragma unroll
        for (int n = 0; n < 5; n++) fw[j][n] = *(const s8v*)(pw + n * 1024 + j * 40960);
        SB0;
    }
#pragma unroll
    for (int j = 0; j < 18; j++) {
        const int sl = j % K4_D;
        SB0;
#pragma unroll
        for (int n = 0; n < 5; n++)
#pragma unroll
            for (int m = 0; m < 4; m++) acc[m][n] = MFMA16(fw[sl][n], fa[sl][m], acc[m][n]);
        SB0;
        if (j + K4_D < 18) {
#pragma unroll
            for (int m = 0; m < 4; m++) fa[sl][m] = *(const s8v*)(pa + m * (144 * 1024) + (j + K4_D) * 8192);
#pragma unroll
            for (int n = 0; n < 5; n++) fw[sl][n] = *(const s8v*)(pw + n * 1024 + (j + K4_D) * 40960);
        }
        SB0;
    }
    u2v hh[3], bb[3];
#pragma unroll
    for (int j = 0; j < 3; j++) {
        int t = wave + 8 * j; if (t > 19) t = 19;
        const int m = t / 5, n = t - m * 5;
        const int row = rt * 64 + m * 16 + (lane & 15);
        int cl = n * 16 + 4 * g; if (cl > 68) cl = 68;
        hh[j] = *(const u2v*)(h + (size_t)row * D_ + ct * 72 + cl);
        bb[j] = *(const u2v*)(b2 + ct * 72 + cl);
    }
    f4v* ex = (f4v*)smem + wave * (20 * 64) + lane;
#pragma unroll
    for (int m = 0; m < 4; m++)
#pragma unroll
        for (int n = 0; n < 5; n++) ex[(m * 5 + n) * 64] = acc[m][n];
    __syncthreads();
#pragma unroll
    for (int j = 0; j < 3; j++) {
        const int t = wave + 8 * j;
        if (t < 20) {
            const int m = t / 5, n = t - m * 5;
            const f4v* e = (const f4v*)smem + t * 64 + lane;
            f4v a = ((e[0] + e[1280]) + (e[2 * 1280] + e[3 * 1280])) + ((e[4 * 1280] + e[5 * 1280]) + (e[6 * 1280] + e[7 * 1280]));
            const int row = rt * 64 + m * 16 + (lane & 15);
            const int cl = n * 16 + 4 * g;
            float v0 = bf_lo(hh[j][0]) + (a[0] + bf_lo(bb[j][0]));
            float v1 = bf_hi(hh[j][0]) + (a[1] + bf_hi(bb[j][0]));
            float v2 = bf_lo(hh[j][1]) + (a[2] + bf_lo(bb[j][1]));
            float v3 = bf_hi(hh[j][1]) + (a[3] + bf_hi(bb[j][1]));
            if (cl < 72) *(u2v*)(out + (size_t)row * D_ + ct * 72 + cl) = u2v{pack_bf2(v0, v1), pack_bf2(v2, v3)};
        }
    }
}

extern "C" void kernel_launch(void* const* d_in, const int* in_sizes, int n_in,
                              void* d_out, int out_size, void* d_ws, size_t ws_size,
                              hipStream_t stream) {
    const bf16_t* x = (const bf16_t*)d_in[0];
    const bf16_t* wqkv = (const bf16_t*)d_in[1];
    const bf16_t* wo = (const bf16_t*)d_in[2];
    const bf16_t* w1 = (const bf16_t*)d_in[4];
    const bf16_t* b1 = (const bf16_t*)d_in[5];
    const bf16_t* w2 = (const bf16_t*)d_in[6];
    const bf16_t* b2 = (const bf16_t*)d_in[7];
    bf16_t* out = (bf16_t*)d_out;
    char* ws = (char*)d_ws;
    char* of = ws;
    char* wof = ws + (3 << 20);
    bf16_t* h = (bf16_t*)(ws + (4 << 20));
    char* hT = ws + (12 << 20);
    f2v* stats = (f2v*)(ws + (8 << 20));
    float* cs1 = (float*)(ws + (9 << 20));
    char* h1 = (char*)(ws + (16 << 20));
    char* w2f = ws + (48 << 20);

    hipFuncSetAttribute((const void*)k1_qkv_attn, hipFuncAttributeMaxDynamicSharedMemorySize, K1_LDS);
    hipFuncSetAttribute((const void*)k2_wo, hipFuncAttributeMaxDynamicSharedMemorySize, K2_LDS);
    hipFuncSetAttribute((const void*)k3_up, hipFuncAttributeMaxDynamicSharedMemorySize, K3_LDS);
    hipFuncSetAttribute((const void*)k4_down, hipFuncAttributeMaxDynamicSharedMemorySize, K4_LDS);

    k1_qkv_attn<<<256, 512, K1_LDS, stream>>>(x, wqkv, w1, wo, of, wof, cs1);
    k2_wo<<<256, 512, K2_LDS, stream>>>(of, wof, x, h, hT, stats);
    k3_up<<<256, 512, K3_LDS, stream>>>(hT, w1, b1, cs1, stats, w2, h1, w2f);
    k4_down<<<256, 512, K4_LDS, stream>>>(h1, w2f, h, b2, out);
}
```

```cpp
#include <hip/hip_runtime.h>
#include <hip/hip_bf16.h>

typedef __attribute__((ext_vector_type(8))) short s8v;
typedef __attribute__((ext_vector_type(4))) short s4v;
typedef __attribute__((ext_vector_type(4))) short v4i16_t;
typedef __attribute__((ext_vector_type(4))) float f4v;
typedef __attribute__((ext_vector_type(2))) float f2v;
typedef __attribute__((ext_vector_type(4))) unsigned u4v;
typedef __attribute__((ext_vector_type(2))) unsigned u2v;
typedef unsigned short bf16_t;

constexpr int S_ = 2048, D_ = 576, NQKV = 1728, F_ = 4608;

__device__ __forceinline__ unsigned lds_addr(const void* l) {
    return (unsigned)(size_t)(__attribute__((address_space(3))) const char*)l;
}
__device__ __forceinline__ void glds16(const void* g, unsigned dst) {
    unsigned keep;
    asm volatile("s_mov_b32 %0, m0\n\ts_mov_b32 m0, %2\n\ts_nop 0\n\tglobal_load_lds_dwordx4 %1, off\n\ts_mov_b32 m0, %0"
                 : "=&s"(keep) : "v"(g), "s"(dst) : "memory");
}
__device__ __forceinline__ void glds16s(const void* sbase, unsigned voff, unsigned dst) {
    unsigned keep;
    asm volatile("s_mov_b32 %0, m0\n\ts_mov_b32 m0, %3\n\ts_nop 0\n\tglobal_load_lds_dwordx4 %1, %2\n\ts_mov_b32 m0, %0"
                 : "=&s"(keep) : "v"(voff), "s"(sbase), "s"(dst) : "memory");
}
__device__ __forceinline__ s4v tr_read(const char* p) {
    return __builtin_bit_cast(s4v, __builtin_amdgcn_ds_read_tr16_b64_v4i16((__attribute__((address_space(3))) v4i16_t*)p));
}
__device__ __forceinline__ s8v cat8(s4v a, s4v b) {
    return __builtin_shufflevector(a, b, 0, 1, 2, 3, 4, 5, 6, 7);
}
__device__ __forceinline__ float bf_lo(unsigned u) { return __builtin_bit_cast(float, u << 16); }
__device__ __forceinline__ float bf_hi(unsigned u) { return __builtin_bit_cast(float, u & 0xffff0000u); }
__device__ __forceinline__ float bf2f(bf16_t v) { return __builtin_bit_cast(float, (unsigned)v << 16); }
__device__ __forceinline__ unsigned pack_bf2(float a, float b) {
    unsigned r;
    asm("v_cvt_pk_bf16_f32 %0, %1, %2" : "=v"(r) : "v"(a), "v"(b));
    return r;
}
#define MFMA16(a, b, c) __builtin_amdgcn_mfma_f32_16x16x32_bf16(a, b, c, 0, 0, 0)
#define WAIT_V(n) asm volatile("s_waitcnt vmcnt(" #n ")" ::: "memory")
#define WAIT_L(n) asm volatile("s_waitcnt lgkmcnt(" #n ")" ::: "memory")

#define SB0 __builtin_amdgcn_sched_barrier(0)
#define BAR __builtin_amdgcn_s_barrier()
constexpr int K1_XN_BYTES = 9 * 80 * 128;
constexpr int K1_STAGE = 14336;
constexpr int K1_NB = 4;
constexpr int K1_DUMMY = K1_XN_BYTES + K1_NB * K1_STAGE;
constexpr int K1_RED = K1_DUMMY + 5120;
constexpr int K1_LDS = K1_RED + 2048;

__global__ __launch_bounds__(512, 2) void k1_qkv_attn(const bf16_t* __restrict__ x, const bf16_t* __restrict__ wqkv,
                                                      const bf16_t* __restrict__ w1, const bf16_t* __restrict__ wo,
                                                      char* __restrict__ of, char* __restrict__ wof,
                                                      float* __restrict__ cs1) {
    extern __shared__ __attribute__((aligned(16))) char smem[];
    const int tid = threadIdx.x, lane = tid & 63;
    const int wave = __builtin_amdgcn_readfirstlane(tid >> 6);
    const int xcd = blockIdx.x & 7, idx = blockIdx.x >> 3;
    const int hg = (xcd & 1) * 4 + (idx >> 3), rt = (xcd >> 1) * 8 + (idx & 7);
    const int s0 = rt * 64;
    const unsigned lring = lds_addr(smem) + K1_XN_BYTES;
    const int g = lane >> 4, q = (lane & 15) >> 2, p = lane & 3;

    const int lw = wave - 4;
    unsigned bsrc[4];
    const int pc0 = lw < 2 ? lw * 4 : 2 + lw * 3;
    const int rot = ((idx & 7) + 2 * (idx >> 3)) % 9;
    auto rblk = [&](int b) { int t = b + rot; return t >= 9 ? t - 9 : t; };
    auto issue = [&](int st) {
        int sc = st > 17 ? 17 : st;
        sc = rblk(sc >> 1) * 2 + (sc & 1);
        unsigned lb = lring + (st % K1_NB) * K1_STAGE;
        const char* gb = (const char*)wqkv + (size_t)sc * 32 * NQKV * 2;
#pragma unroll
        for (int i = 0; i < 3; i++) glds16s(gb, bsrc[i], lb + (pc0 + i) * 1024);
        if (lw < 2) glds16s(gb, bsrc[3], lb + (pc0 + 3) * 1024);
    };
    const unsigned dz = lds_addr(smem) + K1_DUMMY + (wave & 3) * 1024;
    float* mus = (float*)(smem + K1_RED);
    unsigned xo[3];
    auto xissue = [&](int b0) {
        const int b = rblk(b0);
#pragma unroll
        for (int j = 0; j < 3; j++)
            glds16s((const char*)x + b * 128, xo[j], lds_addr(smem) + b * (80 * 128) + (j == 2 && wave >= 2 ? wave + 4 : wave + 4 * j) * 1024);
    };
    if (wave >= 4) {
#pragma unroll
        for (int i = 0; i < 4; i++) {
            int bi = pc0 + i; if (bi > 13) bi = 13;
            int off = bi * 1024 + lane * 16;
            int k = off / 448, r = off - k * 448;
            int nt = (r >> 5) ^ ((k >> 3) & 1);
            int pc = nt * 2 + ((r >> 4) & 1); if (pc > 26) pc = 26;
            int sec = pc / 9, wi = pc - sec * 9;
            bsrc[i] = (unsigned)((k * NQKV + sec * 576 + hg * 72 + wi * 8) * 2);
        }
#pragma unroll
        for (int st = 0; st < K1_NB - 1; st++) issue(st);
    } else {
#pragma unroll
        for (int j = 0; j < 3; j++) {
            int gq = wave + 4 * j; if (gq > 9) gq = wave + 4;
            int row = gq * 8 + (lane >> 3);
            int s = s0 - 16 + row; if (s < 0) s = 0;
            xo[j] = (unsigned)(s * (D_ * 2) + (((lane & 7) ^ (lane >> 3)) << 4));
        }
#pragma unroll
        for (int b = 0; b < 3; b++) xissue(b);
    }

    f4v acc[5][4], acs[4], ssum[2], ssq[2];
    u4v cw[6];
    const int cu = blockIdx.x < 216 ? blockIdx.x : blockIdx.x - 216;
    const int cchunk = cu % 72, rthird = cu / 72;
    if (wave >= 4) {
        if (lw < 2) WAIT_V(8); else WAIT_V(6);
        __syncthreads();
        if (lw < 2) WAIT_V(4); else WAIT_V(3);
        BAR;
        for (int i = 0; i < 15; i++) {
            issue(i + K1_NB - 1);
            if (lw < 2) WAIT_V(4); else WAIT_V(3);
            BAR;
        }
        WAIT_V(0); BAR;
        BAR; BAR;
        {
            if (wave == 4 && blockIdx.x < 144) {
                const int ctp = blockIdx.x / 18, kst = blockIdx.x - ctp * 18;
#pragma unroll
                for (int j = 0; j < 5; j++) {
                    int c = j * 64 + lane; if (c > 287) c = 287;
                    int row = c / 9, pc = c - row * 9;
                    glds16s(wo, (unsigned)(((kst * 32 + row) * D_ + ctp * 72 + pc * 8) * 2), lds_addr(smem) + K1_DUMMY + j * 1024);
                }
            }
        }
    } else {
        const int nt0 = wave == 0 ? 0 : 1 + 3 * wave;
        const int nn = (wave == 0 || wave == 3) ? 4 : 3;
#pragma unroll
        for (int m = 0; m < 5; m++)
#pragma unroll
            for (int n = 0; n < 4; n++) acc[m][n] = f4v{0, 0, 0, 0};
#pragma unroll
        for (int n = 0; n < 4; n++) acs[n] = f4v{0, 0, 0, 0};
        ssum[0] = ssum[1] = ssq[0] = ssq[1] = f4v{0, 0, 0, 0};
        const s8v ones = s8v{0x3F80, 0x3F80, 0x3F80, 0x3F80, 0x3F80, 0x3F80, 0x3F80, 0x3F80};
        const int xr = lane & 15;
        int woff[4];
#pragma unroll
        for (int n = 0; n < 4; n++) woff[n] = K1_XN_BYTES + (8 * g + q) * 448 + (((nt0 + n) ^ (g & 1)) << 5) + 8 * p;
        s8v xa[5], xb[5], wf[4];
        auto rdx = [&](s8v* X, int st) {
            const char* xbp = smem + rblk(st >> 1) * (80 * 128) + xr * 128 + ((((st & 1) * 4 + g) ^ (xr & 7)) << 4);
#pragma unroll
            for (int m = 0; m < 5; m++) X[m] = *(const s8v*)(xbp + m * 2048);
        };
        WAIT_V(6);
        __syncthreads();
        rdx(xa, 0);
#pragma unroll
        for (int n = 0; n < 4; n++) wf[n] = cat8(tr_read(smem + woff[n]), tr_read(smem + woff[n] + 4 * 448));
        WAIT_L(0);
        BAR;
#define K1_STEP(W, XC, XN, PRE, POST) { \
            const char* base = smem + ((i + 1) % K1_NB) * K1_STAGE; \
            PRE; \
            rdx(XN, i + 1 > 17 ? 17 : i + 1); \
            SB0; \
            _Pragma("unroll") for (int n = 0; n < (W == 0 || W == 3 ? 4 : 3); n++) { \
                if (W != 0) acc[0][n] = MFMA16(wf[n], XC[0], acc[0][n]); \
                _Pragma("unroll") for (int m = 1; m < 5; m++) acc[m][n] = MFMA16(wf[n], XC[m], acc[m][n]); \
                acs[n] = MFMA16(wf[n], ones, acs[n]); \
                SB0; \
                wf[n] = cat8(tr_read(base + woff[n]), tr_read(base + woff[n] + 4 * 448)); \
                SB0; \
            } \
            if (W == 0) { ssum[0] = MFMA16(XC[0], ones, ssum[0]); ssq[0] = MFMA16(XC[0], XC[0], ssq[0]); } \
            if (W == 1) { ssum[0] = MFMA16(XC[1], ones, ssum[0]); ssq[0] = MFMA16(XC[1], XC[1], ssq[0]); \
                          ssum[1] = MFMA16(XC[2], ones, ssum[1]); ssq[1] = MFMA16(XC[2], XC[2], ssq[1]); } \
            if (W == 2) { ssum[0] = MFMA16(XC[3], ones, ssum[0]); ssq[0] = MFMA16(XC[3], XC[3], ssq[0]); \
                          ssum[1] = MFMA16(XC[4], ones, ssum[1]); ssq[1] = MFMA16(XC[4], XC[4], ssq[1]); } \
            SB0; \
            POST; \
            BAR; }
#define K1_LOOPS(W) { \
        _Pragma("unroll 1") for (int i = 0; i < 6; i += 2) { \
            K1_STEP(W, xa, xb, xissue(i + 3), WAIT_V(6)); \
            i++; K1_STEP(W, xb, xa, xissue(i + 3), WAIT_V(6)); i--; \
        } \
        _Pragma("unroll 1") for (int i = 6; i < 18; i += 2) { \
            K1_STEP(W, xa, xb, , WAIT_V(0)); \
            i++; K1_STEP(W, xb, xa, , WAIT_V(0)); i--; \
        } }
        if (wave == 0) K1_LOOPS(0) else if (wave == 1) K1_LOOPS(1) else if (wave == 2) K1_LOOPS(2) else K1_LOOPS(3)
        if (wave < 3 && q == g) {
#pragma unroll
            for (int t = 0; t < 2; t++) {
                if (t == 0 || wave > 0) {
                    const float mean = ssum[t][p] * (1.f / 576.f);
                    const float var = fmaxf(ssq[t][p] * (1.f / 576.f) - mean * mean, 0.f);
                    const int r = (wave == 0 ? 0 : 2 * wave - 1 + t) * 16 + (lane & 15);
                    mus[r] = mean; mus[80 + r] = rsqrtf(var + 1e-5f);
                }
            }
        }
        const u4v* pcw = (const u4v*)(w1 + (size_t)(rthird * 192 + wave * 48 + (lane >> 3)) * F_ + cchunk * 64) + (lane & 7);
#pragma unroll
        for (int j = 0; j < 6; j++) cw[j] = pcw[(size_t)j * 8 * F_ / 8];
    }
    __syncthreads();
    constexpr int RSB = 456;
    if (wave < 4) {
        const int nt0 = wave == 0 ? 0 : 1 + 3 * wave;
        const int nn = (wave == 0 || wave == 3) ? 4 : 3;
        int lco[4][2];
#pragma unroll
        for (int n = 0; n < 4; n++)
#pragma unroll
            for (int h2 = 0; h2 < 2; h2++) {
                const int c = (nt0 + n) * 16 + 4 * g + 2 * h2;
                const int hc = c - 144, hd = hc / 18;
                lco[n][h2] = (n < nn && c < 216) ? (c < 144 ? c : 144 + hd * 20 + (hc - hd * 18)) * 2 : -1;
            }
        float mean[5], rs[5];
#pragma unroll
        for (int m = 0; m < 5; m++) { mean[m] = mus[m * 16 + (lane & 15)]; rs[m] = mus[80 + m * 16 + (lane & 15)]; }
        const int dump = K1_RED + 1024 + lane * 4;
#pragma unroll
        for (int m = 0; m < 5; m++) {
            const int rowo = (m * 16 + (lane & 15)) * RSB;
            const bool mok = m > 0 || wave > 0;
#pragma unroll
            for (int n = 0; n < 4; n++) {
                const f4v v = (acc[m][n] - acs[n] * mean[m]) * rs[m];
                *(unsigned*)(smem + (mok && lco[n][0] >= 0 ? rowo + lco[n][0] : dump)) = pack_bf2(v[0], v[1]);
                *(unsigned*)(smem + (mok && lco[n][1] >= 0 ? rowo + lco[n][1] : dump)) = pack_bf2(v[2], v[3]);
            }
        }
    }
    __syncthreads();
    if (wave == 4 && blockIdx.x < 144) {
        WAIT_V(0);
        const char* sb = smem + K1_DUMMY + (8 * g + q) * 144 + 8 * p;
        char* dst = wof + (size_t)blockIdx.x * 5 * 1024 + lane * 16;
#pragma unroll
        for (int nt = 0; nt < 5; nt++)
            *(s8v*)(dst + nt * 1024) = cat8(tr_read(sb + nt * 32), tr_read(sb + 4 * 144 + nt * 32));
    }
    {
        const int head = wave >> 1, li = lane & 15;
        const unsigned qmask0 = g < 3 ? 0xffffffffu : 0u, qmask1 = g < 2 ? 0xffffffffu : 0u;
        auto xmax = [](float v) {
            float a = v, b = v;
            asm volatile("s_nop 1\n\tv_permlane16_swap_b32 %0, %1" : "+v"(a), "+v"(b));
            a = fmaxf(a, b); b = a;
            asm volatile("s_nop 1\n\tv_permlane32_swap_b32 %0, %1" : "+v"(a), "+v"(b));
            return fmaxf(a, b);
        };
        auto xsum = [](float v) {
            float a = v, b = v;
            asm volatile("s_nop 1\n\tv_permlane16_swap_b32 %0, %1" : "+v"(a), "+v"(b));
            a = a + b; b = a;
            asm volatile("s_nop 1\n\tv_permlane32_swap_b32 %0, %1" : "+v"(a), "+v"(b));
            return a + b;
        };
        const int t0 = (wave & 1) * 2;
        u4v qf[2], kf[3];
        s8v vf0[2], vf1[2];
#pragma unroll
        for (int uu = 0; uu < 2; uu++) {
            const char* qp = smem + (16 + 16 * (t0 + uu) + li) * RSB + (head * 18 + 8 * g) * 2;
#pragma unroll
            for (int i = 0; i < 4; i++) qf[uu][i] = *(const unsigned*)(qp + 4 * i) & (i == 0 ? qmask0 : qmask1);
        }
#pragma unroll
        for (int kt = 0; kt < 3; kt++) {
            const char* kp = smem + (16 * (t0 + kt) + li) * RSB + (72 + head * 18 + 8 * g) * 2;
#pragma unroll
            for (int i = 0; i < 4; i++) kf[kt][i] = *(const unsigned*)(kp + 4 * i);
        }
#pragma unroll
        for (int uu = 0; uu < 2; uu++) {
            const char* vp = smem + (16 * (t0 + uu) + 4 * g + q) * RSB + (144 + head * 20 + 4 * p) * 2;
            vf0[uu] = cat8(tr_read(vp), tr_read(vp + 16 * RSB));
            vf1[uu] = cat8(tr_read(vp + 32), tr_read(vp + 16 * RSB + 32));
        }
        const f4v z4 = f4v{0.f, 0.f, 0.f, 0.f};
        f4v sv[2][2];
#pragma unroll
        for (int uu = 0; uu < 2; uu++)
#pragma unroll
            for (int tau = 0; tau < 2; tau++)
                sv[uu][tau] = MFMA16(__builtin_bit_cast(s8v, kf[uu + tau]), __builtin_bit_cast(s8v, qf[uu]), z4);
        bool ok[2][8]; float mx[2], den[2]; u4v pb[2];
#pragma unroll
        for (int uu = 0; uu < 2; uu++) {
            const bool tile0_ok = !(s0 == 0 && t0 + uu == 0);
            mx[uu] = -3.0e38f;
#pragma unroll
            for (int j = 0; j < 8; j++) {
                const int dl = 16 * (j >> 2) + 4 * g + (j & 3) - li;
                ok[uu][j] = !(dl & 1) && dl >= 2 && dl <= 16 && (j >= 4 || tile0_ok);
                mx[uu] = fmaxf(mx[uu], ok[uu][j] ? sv[uu][j >> 2][j & 3] : -3.0e38f);
            }
        }
#pragma unroll
        for (int uu = 0; uu < 2; uu++) mx[uu] = xmax(mx[uu]);
#pragma unroll
        for (int uu = 0; uu < 2; uu++) {
            float a[8];
#pragma unroll
            for (int j = 0; j < 8; j++) {
                const float e = __builtin_amdgcn_exp2f((sv[uu][j >> 2][j & 3] - mx[uu]) * (0.23570226039551584f * 1.4426950408889634f));
                a[j] = ok[uu][j] ? e : 0.f;
            }
            pb[uu] = u4v{pack_bf2(a[0], a[1]), pack_bf2(a[2], a[3]), pack_bf2(a[4], a[5]), pack_bf2(a[6], a[7])};
            den[uu] = 0.f;
#pragma unroll
            for (int j = 0; j < 4; j++) den[uu] += bf_lo(pb[uu][j]) + bf_hi(pb[uu][j]);
        }
#pragma unroll
        for (int uu = 0; uu < 2; uu++) den[uu] = 1.f / xsum(den[uu]);
#pragma unroll
        for (int uu = 0; uu < 2; uu++) {
            const f4v o0 = MFMA16(vf0[uu], __builtin_bit_cast(s8v, pb[uu]), z4) * den[uu];
            const f4v o1 = MFMA16(vf1[uu], __builtin_bit_cast(s8v, pb[uu]), z4) * den[uu];
            char* ob = of + (size_t)((s0 >> 4) + t0 + uu) * (18 * 1024) + li * 16;
            const int c0 = hg * 72 + head * 18 + 4 * g, c1 = c0 + 2, c2 = hg * 72 + head * 18 + 16;
            *(unsigned*)(ob + (c0 >> 5) * 1024 + ((c0 & 31) >> 3) * 256 + (c0 & 7) * 2) = pack_bf2(o0[0], o0[1]);
            *(unsigned*)(ob + (c1 >> 5) * 1024 + ((c1 & 31) >> 3) * 256 + (c1 & 7) * 2) = pack_bf2(o0[2], o0[3]);
            if (g == 0) *(unsigned*)(ob + (c2 >> 5) * 1024 + ((c2 & 31) >> 3) * 256 + (c2 & 7) * 2) = pack_bf2(o1[0], o1[1]);
        }
    }
    if (wave < 4) {
        float a[8];
#pragma unroll
        for (int j = 0; j < 8; j++) a[j] = 0.f;
#pragma unroll
        for (int i = 0; i < 6; i++)
#pragma unroll
            for (int j = 0; j < 4; j++) { a[2 * j] += bf_lo(cw[i][j]); a[2 * j + 1] += bf_hi(cw[i][j]); }
#pragma unroll
        for (int j = 0; j < 8; j++) {
            float t = a[j];
            t += __shfl_xor(t, 8); t += __shfl_xor(t, 16); t += __shfl_xor(t, 32);
            a[j] = t;
        }
        if (lane < 8 && blockIdx.x < 216) {
            float* rq = cs1 + (rthird * 4 + wave) * F_ + cchunk * 64 + lane * 8;
            *(f4v*)rq = f4v{a[0], a[1], a[2], a[3]};
            *(f4v*)(rq + 4) = f4v{a[4], a[5], a[6], a[7]};
        }
    }
}

constexpr int K2_LDS = 8 * 20 * 1024;

__global__ __launch_bounds__(512, 2) void k2_wo(const char* __restrict__ of, const char* __restrict__ wof,
                                                const bf16_t* __restrict__ x, bf16_t* __restrict__ h,
                                                char* __restrict__ hT, f2v* __restrict__ stats) {
    extern __shared__ __attribute__((aligned(16))) char smem[];
    const int tid = threadIdx.x, lane = tid & 63;
    const int wave = __builtin_amdgcn_readfirstlane(tid >> 6);
    const int rt = (blockIdx.x & 7) * 4 + ((blockIdx.x >> 3) & 3), ct = blockIdx.x >> 5;
    const int r0 = rt * 64, c0 = ct * 72;
    const int g = lane >> 4;
    const char* pa = of + ((size_t)rt * 4 * 18 + wave) * 1024 + lane * 16;
    const char* pw = wof + ((size_t)ct * 18 + wave) * 5 * 1024 + lane * 16;
    s8v fa[3][4], fw[3][5];
#pragma unroll
    for (int j = 0; j < 3; j++) {
        if (j < 2 || wave < 2) {
#pragma unroll
            for (int m = 0; m < 4; m++) fa[j][m] = *(const s8v*)(pa + m * (18 * 1024) + j * 8192);
#pragma unroll
            for (int n = 0; n < 5; n++) fw[j][n] = *(const s8v*)(pw + n * 1024 + j * 40960);
        }
    }
    const int row = r0 + (wave & 3) * 16 + (lane & 15);
    u2v xx[5];
#pragma unroll
    for (int n = 0; n < 5; n++) {
        int col = n * 16 + 4 * g; if (col > 68) col = 68;
        xx[n] = *(const u2v*)(x + (size_t)row * D_ + c0 + col);
    }
    f4v acc[4][5];
#pragma unroll
    for (int m = 0; m < 4; m++)
#pragma unroll
        for (int n = 0; n < 5; n++) acc[m][n] = f4v{0, 0, 0, 0};
#pragma unroll
    for (int j = 0; j < 3; j++) {
        if (j < 2 || wave < 2) {
#pragma unroll
            for (int n = 0; n < 5; n++)
#pragma unroll
                for (int m = 0; m < 4; m++) acc[m][n] = MFMA16(fw[j][n], fa[j][m], acc[m][n]);
        }
    }
    f4v* ex = (f4v*)smem + wave * (20 * 64) + lane;
#pragma unroll
    for (int m = 0; m < 4; m++)
#pragma unroll
        for (int n = 0; n < 5; n++) ex[(m * 5 + n) * 64] = acc[m][n];
    __syncthreads();
    if (wave >= 4) {
        return;
    }
    float hv[5][4];
    float sum = 0.f;
#pragma unroll
    for (int n = 0; n < 5; n++) {
        const f4v* e = (const f4v*)smem + (wave * 5 + n) * 64 + lane;
        f4v a = ((e[0] + e[1280]) + (e[2 * 1280] + e[3 * 1280])) + ((e[4 * 1280] + e[5 * 1280]) + (e[6 * 1280] + e[7 * 1280]));
        int col = n * 16 + 4 * g;
        if (col < 72) {
            unsigned p0 = pack_bf2(bf_lo(xx[n][0]) + a[0], bf_hi(xx[n][0]) + a[1]);
            unsigned p1 = pack_bf2(bf_lo(xx[n][1]) + a[2], bf_hi(xx[n][1]) + a[3]);
            *(u2v*)(h + (size_t)row * D_ + c0 + col) = u2v{p0, p1};
            {
                int cc = c0 + col;
                *(u2v*)(hT + (size_t)((row >> 4) * 18 + (cc >> 5)) * 1024 + (((cc & 31) >> 3) * 16 + (row & 15)) * 16 + (cc & 7) * 2) = u2v{p0, p1};
            }
            hv[n][0] = bf_lo(p0); hv[n][1] = bf_hi(p0); hv[n][2] = bf_lo(p1); hv[n][3] = bf_hi(p1);
            sum += hv[n][0] + hv[n][1] + hv[n][2] + hv[n][3];
        } else {
            hv[n][0] = hv[n][1] = hv[n][2] = hv[n][3] = 0.f;
        }
    }
    sum += __shfl_xor(sum, 16); sum += __shfl_xor(sum, 32);
    float mean = sum * (1.f / 72.f);
    float m2 = 0.f;
#pragma unroll
    for (int n = 0; n < 5; n++) {
        int col = n * 16 + 4 * g;
        if (col < 72) {
#pragma unroll
            for (int j = 0; j < 4; j++) { float d = hv[n][j] - mean; m2 += d * d; }
        }
    }
    m2 += __shfl_xor(m2, 16); m2 += __shfl_xor(m2, 32);
    if (g == 0) stats[(size_t)row * 8 + ct] = f2v{sum, m2};
}

constexpr int K3_STAGE = 8192 + 18432;
constexpr int K3_NB = 5;
constexpr int K3_PAR = K3_NB * K3_STAGE;
constexpr int K3_LDS = K3_PAR + 1024 + 2304;

__device__ __forceinline__ float gelu_tanh(float v) {
    float t = v * v;
    float z = v * (-2.302208198f * 0.044715f * t - 2.302208198f);
    float e = __builtin_amdgcn_exp2f(z);
    return v * __builtin_amdgcn_rcpf(1.f + e);
}

__device__ __forceinline__ f2v gelu_tanh2(f2v v) {
    f2v t = v * v;
    f2v z = v * (t * (-2.302208198f * 0.044715f) + (-2.302208198f));
    f2v d = f2v{__builtin_amdgcn_exp2f(z[0]), __builtin_amdgcn_exp2f(z[1])} + 1.f;
    return v * f2v{__builtin_amdgcn_rcpf(d[0]), __builtin_amdgcn_rcpf(d[1])};
}

__global__ __launch_bounds__(512, 2) void k3_up(const char* __restrict__ hT, const bf16_t* __restrict__ w1,
                                                const bf16_t* __restrict__ b1, const float* __restrict__ cs1,
                                                const f2v* __restrict__ stats, const bf16_t* __restrict__ w2,
                                                char* __restrict__ h1f, char* __restrict__ w2f) {
    extern __shared__ __attribute__((aligned(16))) char smem[];
    const int tid = threadIdx.x, lane = tid & 63;
    const int wave = __builtin_amdgcn_readfirstlane(tid >> 6);
    const int xcd = blockIdx.x & 7, idx = blockIdx.x >> 3;
    const int rt = (xcd & 1) * 8 + (idx & 7), ct = (xcd >> 1) * 4 + (idx >> 3);
    const int r0 = rt * 128, c0 = ct * 288;
    const unsigned lbase = lds_addr(smem);
    const int g = lane >> 4, q = (lane & 15) >> 2, p = lane & 3;
    const int xr = lane & 15;
    f2v* mu = (f2v*)(smem + K3_PAR);
    float* csl = (float*)(smem + K3_PAR + 1024);
    float* bbl = csl + 288;
    f4v acc[4][9];
    const int wm = (wave >> 1) & 1, wn = wave & 1;
    if (wave >= 4) {
        const int lw = wave - 4;
        unsigned bsrc[5];
        const unsigned avo = lane * 16;
        const char* ag = hT + (size_t)(rt * 8 + lw * 2) * 18 * 1024;
#pragma unroll
        for (int i = 0; i < 5; i++) {
            int bi = lw * 5 + i; if (bi > 17) bi = 17;
            int off = bi * 1024 + lane * 16;
            int k = off / 576, r = off - k * 576;
            int nt = (r >> 5) ^ ((k >> 3) & 1);
            bsrc[i] = (unsigned)((k * F_ + c0 + nt * 16 + ((r >> 4) & 1) * 8) * 2);
        }
        auto issue = [&](int st) {
            int sc = st > 17 ? 17 : st;
            unsigned lb = lbase + (st % K3_NB) * K3_STAGE;
            const char* ga = ag + sc * 1024;
            const char* gb = (const char*)w1 + (size_t)sc * 32 * F_ * 2;
            glds16s(ga, avo, lb + (lw * 2) * 1024);
            glds16s(ga + 18 * 1024, avo, lb + (lw * 2 + 1) * 1024);
#pragma unroll
            for (int i = 0; i < 5; i++) glds16s(gb, bsrc[i], lb + 8192 + (lw * 5 + i > 17 ? 17 : lw * 5 + i) * 1024);
        };
#pragma unroll
        for (int st = 0; st < K3_NB - 1; st++) issue(st);
        WAIT_V(21);
        BAR;
        for (int i = -1; i < 18; i++) {
            if (i >= 0) issue(i + K3_NB - 1);
            WAIT_V(14);
            BAR;
        }
        WAIT_V(0);
    } else {
        {
            float c0v = 0.f, b0v = bf2f(b1[c0 + tid]);
            float c1v = 0.f, b1v = 0.f;
            {
                float t0[12], t1[12];
                const int c1i = c0 + 256 + (tid & 31);
#pragma unroll
                for (int i = 0; i < 12; i++) { t0[i] = cs1[i * F_ + c0 + tid]; t1[i] = cs1[i * F_ + c1i]; }
                b1v = bf2f(b1[c1i]);
#pragma unroll
                for (int i = 0; i < 12; i++) { c0v += t0[i]; c1v += t1[i]; }
            }
            if (tid < 128) {
                const f4v* sp = (const f4v*)(stats + (size_t)(r0 + tid) * 8);
                f4v v[4];
#pragma unroll
                for (int i = 0; i < 4; i++) v[i] = sp[i];
                float tot = 0.f;
#pragma unroll
                for (int i = 0; i < 4; i++) tot += v[i][0] + v[i][2];
                float mean = tot * (1.f / 576.f);
                float M2 = 0.f;
#pragma unroll
                for (int i = 0; i < 4; i++) {
                    float d0 = v[i][0] * (1.f / 72.f) - mean, d1 = v[i][2] * (1.f / 72.f) - mean;
                    M2 += v[i][1] + v[i][3] + 72.f * (d0 * d0 + d1 * d1);
                }
                float rstd = rsqrtf(M2 * (1.f / 576.f) + 1e-5f);
                mu[tid] = f2v{mean * rstd, rstd};
            }
            csl[tid] = c0v; bbl[tid] = b0v;
            if (tid < 32) { csl[256 + tid] = c1v; bbl[256 + tid] = b1v; }
        }
#pragma unroll
        for (int m = 0; m < 4; m++)
#pragma unroll
            for (int n = 0; n < 9; n++) acc[m][n] = f4v{0, 0, 0, 0};
        const int dlt = (g & 1) ? 32 : 0;
        const int wb0 = 8192 + (8 * g + q) * 576 + wn * 9 * 32 + 8 * p;
        const int wbe = wb0 + ((wn & 1) ? -dlt : dlt);
        const int wbo = wb0 + ((wn & 1) ? dlt : -dlt);
        const int xoff = wm * 4096 + lane * 16;
        s8v xa[4], xb[4], wf[9];
        BAR;
        {
            const char* base = smem;
#pragma unroll
            for (int m = 0; m < 4; m++) xa[m] = *(const s8v*)(base + xoff + m * 1024);
#pragma unroll
            for (int n = 0; n < 9; n++) {
                const char* wb = base + ((n & 1) ? wbo : wbe) + n * 32;
                wf[n] = cat8(tr_read(wb), tr_read(wb + 4 * 576));
            }
        }
        WAIT_L(0);
        BAR;
#define K3_STEP(XC, XN) { \
            const char* base = smem + ((i + 1) % K3_NB) * K3_STAGE; \
            s4v wlo, whi; \
            _Pragma("unroll") for (int n = 0; n < 9; n++) { \
                const char* wbp = base + (((n - 1) & 1) ? wbo : wbe) + (n - 1) * 32; \
                acc[0][n] = MFMA16(wf[n], XC[0], acc[0][n]); SB0; \
                if (n > 0) { wlo = tr_read(wbp); SB0; } \
                acc[1][n] = MFMA16(wf[n], XC[1], acc[1][n]); SB0; \
                if (n > 0) { whi = tr_read(wbp + 4 * 576); wf[n - 1] = cat8(wlo, whi); SB0; } \
                acc[2][n] = MFMA16(wf[n], XC[2], acc[2][n]); SB0; \
                if (n < 4) { XN[n] = *(const s8v*)(base + xoff + n * 1024); SB0; } \
                acc[3][n] = MFMA16(wf[n], XC[3], acc[3][n]); SB0; \
            } \
            { const char* wbp = base + wbe + 8 * 32; wf[8] = cat8(tr_read(wbp), tr_read(wbp + 4 * 576)); } \
            BAR; }
#pragma unroll 1
        for (int i = 0; i < 18; i += 2) {
            K3_STEP(xa, xb);
            i++; K3_STEP(xb, xa); i--;
        }
    }
    __syncthreads();
    const int u = wave * 256 + blockIdx.x;
    if (u < 1152) {
        const int ctp = u / 144, kst = u - ctp * 144;
#pragma unroll
        for (int j = 0; j < 5; j++) {
            int c = j * 64 + lane; if (c > 287) c = 287;
            int row = c / 9, pc = c - row * 9;
            glds16s(w2, (unsigned)(((kst * 32 + row) * D_ + ctp * 72 + pc * 8) * 2), lbase + 98304 + wave * 5120 + j * 1024);
        }
    }
    f4v* ex = (f4v*)smem + (wave & 3) * (18 * 64) + lane;
    if (wave < 4) {
#pragma unroll
        for (int m = 0; m < 2; m++)
#pragma unroll
            for (int n = 0; n < 9; n++) ex[(m * 9 + n) * 64] = acc[m + 2][n];
    }
    __syncthreads();
    if (wave >= 4) {
#pragma unroll
        for (int m = 0; m < 2; m++)
#pragma unroll
            for (int n = 0; n < 9; n++) acc[m][n] = ex[(m * 9 + n) * 64];
    }
    {
        const int mb = (wave >> 2) * 2;
#pragma unroll
        for (int m = 0; m < 2; m++) {
            int rl = wm * 64 + (mb + m) * 16 + xr;
            f2v ms = mu[rl];
            char* ob = h1f + (size_t)((r0 + wm * 64 + (mb + m) * 16) >> 4) * (144 * 1024) + lane * 16;
            u2v pv[9];
#pragma unroll
            for (int n = 0; n < 9; n++) {
                f4v a = acc[m][n];
                int cl = wn * 144 + n * 16 + 4 * g;
                f4v cs = *(const f4v*)(csl + cl), bb = *(const f4v*)(bbl + cl);
                f2v t01 = f2v{bb[0], bb[1]} - f2v{cs[0], cs[1]} * ms[0];
                f2v t23 = f2v{bb[2], bb[3]} - f2v{cs[2], cs[3]} * ms[0];
                f2v v01 = gelu_tanh2(f2v{a[0], a[1]} * ms[1] + t01);
                f2v v23 = gelu_tanh2(f2v{a[2], a[3]} * ms[1] + t23);
                pv[n] = u2v{pack_bf2(v01[0], v01[1]), pack_bf2(v23[0], v23[1])};
            }
            if (m == 0) {
            } else {
            }
            char* oc = ob + (c0 >> 5) * 1024;
            if (wn == 0) {
#pragma unroll
                for (int j = 0; j < 4; j++) *(u4v*)(oc + j * 1024) = u4v{pv[2 * j][0], pv[2 * j][1], pv[2 * j + 1][0], pv[2 * j + 1][1]};
                *(u2v*)(oc + 4 * 1024) = pv[8];
            } else {
                *(u2v*)(oc + 4 * 1024 + 8) = pv[0];
#pragma unroll
                for (int j = 0; j < 4; j++) *(u4v*)(oc + (5 + j) * 1024) = u4v{pv[2 * j + 1][0], pv[2 * j + 1][1], pv[2 * j + 2][0], pv[2 * j + 2][1]};
            }
        }
    }
    WAIT_V(0);
    if (u < 1152) {
        const char* sb = smem + 98304 + wave * 5120 + (4 * g + q) * 144 + 8 * p;
        char* dst = w2f + (size_t)u * 5 * 1024 + lane * 16;
#pragma unroll
        for (int nt = 0; nt < 5; nt++)
            *(s8v*)(dst + nt * 1024) = cat8(tr_read(sb + nt * 32), tr_read(sb + 16 * 144 + nt * 32));
    }
}

constexpr int K4_LDS = 8 * 20 * 1024;
constexpr int K4_D = 3;

__global__ __launch_bounds__(512, 2) void k4_down(const char* __restrict__ h1f, const char* __restrict__ w2f,
                                                  const bf16_t* __restrict__ h, const bf16_t* __restrict__ b2,
                                                  bf16_t* __restrict__ out) {
    extern __shared__ __attribute__((aligned(16))) char smem[];
    const int tid = threadIdx.x, lane = tid & 63;
    const int wave = __builtin_amdgcn_readfirstlane(tid >> 6);
    const int xcd = blockIdx.x & 7, idx = blockIdx.x >> 3;
    const int rt = (xcd >> 1) * 8 + (idx & 7), ct = (xcd & 1) * 4 + (idx >> 3);
    const int g = lane >> 4;
    const char* pa = h1f + ((size_t)rt * 4 * 144 + wave) * 1024 + lane * 16;
    const char* pw = w2f + ((size_t)ct * 144 + wave) * 5 * 1024 + lane * 16;
    f4v acc[4][5];
#pragma unroll
    for (int m = 0; m < 4; m++)
#pragma unroll
        for (int n = 0; n < 5; n++) acc[m][n] = f4v{0, 0, 0, 0};
    s8v fa[K4_D][4], fw[K4_D][5];
#pragma unroll
    for (int j = 0; j < K4_D; j++) {
#pragma unroll
        for (int m = 0; m < 4; m++) fa[j][m] = *(const s8v*)(pa + m * (144 * 1024) + j * 8192);
#pragma unroll
        for (int n = 0; n < 5; n++) fw[j][n] = *(const s8v*)(pw + n * 1024 + j * 40960);
        SB0;
    }
#pragma unroll
    for (int j = 0; j < 18; j++) {
        const int sl = j % K4_D;
        SB0;
#pragma unroll
        for (int n = 0; n < 5; n++)
#pragma unroll
            for (int m = 0; m < 4; m++) acc[m][n] = MFMA16(fw[sl][n], fa[sl][m], acc[m][n]);
        SB0;
        if (j + K4_D < 18) {
#pragma unroll
            for (int m = 0; m < 4; m++) fa[sl][m] = *(const s8v*)(pa + m * (144 * 1024) + (j + K4_D) * 8192);
#pragma unroll
            for (int n = 0; n < 5; n++) fw[sl][n] = *(const s8v*)(pw + n * 1024 + (j + K4_D) * 40960);
        }
        SB0;
    }
    u2v hh[3], bb[3];
#pragma unroll
    for (int j = 0; j < 3; j++) {
        int t = wave + 8 * j; if (t > 19) t = 19;
        const int m = t / 5, n = t - m * 5;
        const int row = rt * 64 + m * 16 + (lane & 15);
        int cl = n * 16 + 4 * g; if (cl > 68) cl = 68;
        hh[j] = *(const u2v*)(h + (size_t)row * D_ + ct * 72 + cl);
        bb[j] = *(const u2v*)(b2 + ct * 72 + cl);
    }
    f4v* ex = (f4v*)smem + wave * (20 * 64) + lane;
#pragma unroll
    for (int m = 0; m < 4; m++)
#pragma unroll
        for (int n = 0; n < 5; n++) ex[(m * 5 + n) * 64] = acc[m][n];
    __syncthreads();
#pragma unroll
    for (int j = 0; j < 3; j++) {
        const int t = wave + 8 * j;
        if (t < 20) {
            const int m = t / 5, n = t - m * 5;
            const f4v* e = (const f4v*)smem + t * 64 + lane;
            f4v a = ((e[0] + e[1280]) + (e[2 * 1280] + e[3 * 1280])) + ((e[4 * 1280] + e[5 * 1280]) + (e[6 * 1280] + e[7 * 1280]));
            const int row = rt * 64 + m * 16 + (lane & 15);
            const int cl = n * 16 + 4 * g;
            float v0 = bf_lo(hh[j][0]) + (a[0] + bf_lo(bb[j][0]));
            float v1 = bf_hi(hh[j][0]) + (a[1] + bf_hi(bb[j][0]));
            float v2 = bf_lo(hh[j][1]) + (a[2] + bf_lo(bb[j][1]));
            float v3 = bf_hi(hh[j][1]) + (a[3] + bf_hi(bb[j][1]));
            if (cl < 72) *(u2v*)(out + (size_t)row * D_ + ct * 72 + cl) = u2v{pack_bf2(v0, v1), pack_bf2(v2, v3)};
        }
    }
}

extern "C" void kernel_launch(void* const* d_in, const int* in_sizes, int n_in,
                              void* d_out, int out_size, void* d_ws, size_t ws_size,
                              hipStream_t stream) {
    const bf16_t* x = (const bf16_t*)d_in[0];
    const bf16_t* wqkv = (const bf16_t*)d_in[1];
    const bf16_t* wo = (const bf16_t*)d_in[2];
    const bf16_t* w1 = (const bf16_t*)d_in[4];
    const bf16_t* b1 = (const bf16_t*)d_in[5];
    const bf16_t* w2 = (const bf16_t*)d_in[6];
    const bf16_t* b2 = (const bf16_t*)d_in[7];
    bf16_t* out = (bf16_t*)d_out;
    char* ws = (char*)d_ws;
    char* of = ws;
    char* wof = ws + (3 << 20);
    bf16_t* h = (bf16_t*)(ws + (4 << 20));
    char* hT = ws + (12 << 20);
    f2v* stats = (f2v*)(ws + (8 << 20));
    float* cs1 = (float*)(ws + (9 << 20));
    char* h1 = (char*)(ws + (16 << 20));
    char* w2f = ws + (48 << 20);

    hipFuncSetAttribute((const void*)k1_qkv_attn, hipFuncAttributeMaxDynamicSharedMemorySize, K1_LDS);
    hipFuncSetAttribute((const void*)k2_wo, hipFuncAttributeMaxDynamicSharedMemorySize, K2_LDS);
    hipFuncSetAttribute((const void*)k3_up, hipFuncAttributeMaxDynamicSharedMemorySize, K3_LDS);
    hipFuncSetAttribute((const void*)k4_down, hipFuncAttributeMaxDynamicSharedMemorySize, K4_LDS);

    k1_qkv_attn<<<256, 512, K1_LDS, stream>>>(x, wqkv, w1, wo, of, wof, cs1);
    k2_wo<<<256, 512, K2_LDS, stream>>>(of, wof, x, h, hT, stats);
    k3_up<<<256, 512, K3_LDS, stream>>>(hT, w1, b1, cs1, stats, w2, h1, w2f);
    k4_down<<<256, 512, K4_LDS, stream>>>(h1, w2f, h, b2, out);
}
```

```cpp
#include <hip/hip_runtime.h>
#include <hip/hip_bf16.h>

typedef __attribute__((ext_vector_type(8))) short s8v;
typedef __attribute__((ext_vector_type(4))) short s4v;
typedef __attribute__((ext_vector_type(4))) short v4i16_t;
typedef __attribute__((ext_vector_type(4))) float f4v;
typedef __attribute__((ext_vector_type(2))) float f2v;
typedef __attribute__((ext_vector_type(4))) unsigned u4v;
typedef __attribute__((ext_vector_type(2))) unsigned u2v;
typedef unsigned short bf16_t;

constexpr int S_ = 2048, D_ = 576, NQKV = 1728, F_ = 4608;

__device__ __forceinline__ unsigned lds_addr(const void* l) {
    return (unsigned)(size_t)(__attribute__((address_space(3))) const char*)l;
}
__device__ __forceinline__ void glds16(const void* g, unsigned dst) {
    unsigned keep;
    asm volatile("s_mov_b32 %0, m0\n\ts_mov_b32 m0, %2\n\ts_nop 0\n\tglobal_load_lds_dwordx4 %1, off\n\ts_mov_b32 m0, %0"
                 : "=&s"(keep) : "v"(g), "s"(dst) : "memory");
}
__device__ __forceinline__ void glds16s(const void* sbase, unsigned voff, unsigned dst) {
    unsigned keep;
    asm volatile("s_mov_b32 %0, m0\n\ts_mov_b32 m0, %3\n\ts_nop 0\n\tglobal_load_lds_dwordx4 %1, %2\n\ts_mov_b32 m0, %0"
                 : "=&s"(keep) : "v"(voff), "s"(sbase), "s"(dst) : "memory");
}
__device__ __forceinline__ s4v tr_read(const char* p) {
    return __builtin_bit_cast(s4v, __builtin_amdgcn_ds_read_tr16_b64_v4i16((__attribute__((address_space(3))) v4i16_t*)p));
}
__device__ __forceinline__ s8v cat8(s4v a, s4v b) {
    return __builtin_shufflevector(a, b, 0, 1, 2, 3, 4, 5, 6, 7);
}
__device__ __forceinline__ float bf_lo(unsigned u) { return __builtin_bit_cast(float, u << 16); }
__device__ __forceinline__ float bf_hi(unsigned u) { return __builtin_bit_cast(float, u & 0xffff0000u); }
__device__ __forceinline__ float bf2f(bf16_t v) { return __builtin_bit_cast(float, (unsigned)v << 16); }
__device__ __forceinline__ unsigned pack_bf2(float a, float b) {
    unsigned r;
    asm("v_cvt_pk_bf16_f32 %0, %1, %2" : "=v"(r) : "v"(a), "v"(b));
    return r;
}
#define MFMA16(a, b, c) __builtin_amdgcn_mfma_f32_16x16x32_bf16(a, b, c, 0, 0, 0)
#define WAIT_V(n) asm volatile("s_waitcnt vmcnt(" #n ")" ::: "memory")
#define WAIT_L(n) asm volatile("s_waitcnt lgkmcnt(" #n ")" ::: "memory")

__device__ __forceinline__ float xmax4(float v) {
    float a = v, b = v;
    asm volatile("s_nop 1\n\tv_permlane16_swap_b32 %0, %1" : "+v"(a), "+v"(b));
    a = fmaxf(a, b); b = a;
    asm volatile("s_nop 1\n\tv_permlane32_swap_b32 %0, %1" : "+v"(a), "+v"(b));
    return fmaxf(a, b);
}
__device__ __forceinline__ float xsum4(float v) {
    float a = v, b = v;
    asm volatile("s_nop 1\n\tv_permlane16_swap_b32 %0, %1" : "+v"(a), "+v"(b));
    a = a + b; b = a;
    asm volatile("s_nop 1\n\tv_permlane32_swap_b32 %0, %1" : "+v"(a), "+v"(b));
    return a + b;
}
#define SB0 __builtin_amdgcn_sched_barrier(0)
#define BAR __builtin_amdgcn_s_barrier()
constexpr int K1_XN_BYTES = 9 * 80 * 128;
constexpr int K1_STAGE = 14336;
constexpr int K1_NB = 4;
constexpr int K1_DUMMY = K1_XN_BYTES + K1_NB * K1_STAGE;
constexpr int K1_RED = K1_DUMMY + 5120;
constexpr int K1_LDS = K1_RED + 2048;

__global__ __launch_bounds__(512, 2) void k1_qkv_attn(const bf16_t* __restrict__ x, const bf16_t* __restrict__ wqkv,
                                                      const bf16_t* __restrict__ w1, const bf16_t* __restrict__ wo,
                                                      char* __restrict__ of, char* __restrict__ wof,
                                                      float* __restrict__ cs1) {
    extern __shared__ __attribute__((aligned(16))) char smem[];
    const int tid = threadIdx.x, lane = tid & 63;
    const int wave = __builtin_amdgcn_readfirstlane(tid >> 6);
    const int xcd = blockIdx.x & 7, idx = blockIdx.x >> 3;
    const int hg = (xcd & 1) * 4 + (idx >> 3), rt = (xcd >> 1) * 8 + (idx & 7);
    const int s0 = rt * 64;
    const unsigned lring = lds_addr(smem) + K1_XN_BYTES;
    const int g = lane >> 4, q = (lane & 15) >> 2, p = lane & 3;

    const int lw = wave - 4;
    unsigned bsrc[4];
    const int pc0 = lw < 2 ? lw * 4 : 2 + lw * 3;
    const int rot = ((idx & 7) + 2 * (idx >> 3)) % 9;
    auto rblk = [&](int b) { int t = b + rot; return t >= 9 ? t - 9 : t; };
    auto issue = [&](int st) {
        int sc = st > 17 ? 17 : st;
        sc = rblk(sc >> 1) * 2 + (sc & 1);
        unsigned lb = lring + (st % K1_NB) * K1_STAGE;
        const char* gb = (const char*)wqkv + (size_t)sc * 32 * NQKV * 2;
#pragma unroll
        for (int i = 0; i < 3; i++) glds16s(gb, bsrc[i], lb + (pc0 + i) * 1024);
        if (lw < 2) glds16s(gb, bsrc[3], lb + (pc0 + 3) * 1024);
    };
    const unsigned dz = lds_addr(smem) + K1_DUMMY + (wave & 3) * 1024;
    float* mus = (float*)(smem + K1_RED);
    unsigned xo[3];
    auto xissue = [&](int b0) {
        const int b = rblk(b0);
#pragma unroll
        for (int j = 0; j < 3; j++)
            glds16s((const char*)x + b * 128, xo[j], lds_addr(smem) + b * (80 * 128) + (j == 2 && wave >= 2 ? wave + 4 : wave + 4 * j) * 1024);
    };
    if (wave >= 4) {
#pragma unroll
        for (int i = 0; i < 4; i++) {
            int bi = pc0 + i; if (bi > 13) bi = 13;
            int off = bi * 1024 + lane * 16;
            int k = off / 448, r = off - k * 448;
            int nt = (r >> 5) ^ ((k >> 3) & 1);
            int pc = nt * 2 + ((r >> 4) & 1); if (pc > 26) pc = 26;
            int sec = pc / 9, wi = pc - sec * 9;
            bsrc[i] = (unsigned)((k * NQKV + sec * 576 + hg * 72 + wi * 8) * 2);
        }
#pragma unroll
        for (int st = 0; st < K1_NB - 1; st++) issue(st);
    } else {
#pragma unroll
        for (int j = 0; j < 3; j++) {
            int gq = wave + 4 * j; if (gq > 9) gq = wave + 4;
            int row = gq * 8 + (lane >> 3);
            int s = s0 - 16 + row; if (s < 0) s = 0;
            xo[j] = (unsigned)(s * (D_ * 2) + (((lane & 7) ^ (lane >> 3)) << 4));
        }
#pragma unroll
        for (int b = 0; b < 3; b++) xissue(b);
    }

    f4v acc[5][4], acs[4], ssum[2], ssq[2];
    u4v cw[6];
    const int cu = blockIdx.x < 216 ? blockIdx.x : blockIdx.x - 216;
    const int cchunk = cu % 72, rthird = cu / 72;
    if (wave >= 4) {
        if (lw < 2) WAIT_V(8); else WAIT_V(6);
        __syncthreads();
        if (lw < 2) WAIT_V(4); else WAIT_V(3);
        BAR;
        for (int i = 0; i < 15; i++) {
            issue(i + K1_NB - 1);
            if (lw < 2) WAIT_V(4); else WAIT_V(3);
            BAR;
        }
        WAIT_V(0); BAR;
        BAR; BAR;
        {
            if (wave == 4 && blockIdx.x < 144) {
                const int ctp = blockIdx.x / 18, kst = blockIdx.x - ctp * 18;
#pragma unroll
                for (int j = 0; j < 5; j++) {
                    int c = j * 64 + lane; if (c > 287) c = 287;
                    int row = c / 9, pc = c - row * 9;
                    glds16s(wo, (unsigned)(((kst * 32 + row) * D_ + ctp * 72 + pc * 8) * 2), lds_addr(smem) + K1_DUMMY + j * 1024);
                }
            }
        }
    } else {
        const int nt0 = wave == 0 ? 0 : 1 + 3 * wave;
        const int nn = (wave == 0 || wave == 3) ? 4 : 3;
#pragma unroll
        for (int m = 0; m < 5; m++)
#pragma unroll
            for (int n = 0; n < 4; n++) acc[m][n] = f4v{0, 0, 0, 0};
#pragma unroll
        for (int n = 0; n < 4; n++) acs[n] = f4v{0, 0, 0, 0};
        ssum[0] = ssum[1] = ssq[0] = ssq[1] = f4v{0, 0, 0, 0};
        const s8v ones = s8v{0x3F80, 0x3F80, 0x3F80, 0x3F80, 0x3F80, 0x3F80, 0x3F80, 0x3F80};
        const int xr = lane & 15;
        int woff[4];
#pragma unroll
        for (int n = 0; n < 4; n++) woff[n] = K1_XN_BYTES + (8 * g + q) * 448 + (((nt0 + n) ^ (g & 1)) << 5) + 8 * p;
        s8v xa[5], xb[5], wf[4];
        auto rdx = [&](s8v* X, int st) {
            const char* xbp = smem + rblk(st >> 1) * (80 * 128) + xr * 128 + ((((st & 1) * 4 + g) ^ (xr & 7)) << 4);
#pragma unroll
            for (int m = 0; m < 5; m++) X[m] = *(const s8v*)(xbp + m * 2048);
        };
        WAIT_V(6);
        __syncthreads();
        rdx(xa, 0);
#pragma unroll
        for (int n = 0; n < 4; n++) wf[n] = cat8(tr_read(smem + woff[n]), tr_read(smem + woff[n] + 4 * 448));
        WAIT_L(0);
        BAR;
#define K1_STEP(W, XC, XN, PRE, POST) { \
            const char* base = smem + ((i + 1) % K1_NB) * K1_STAGE; \
            PRE; \
            rdx(XN, i + 1 > 17 ? 17 : i + 1); \
            SB0; \
            _Pragma("unroll") for (int n = 0; n < (W == 0 || W == 3 ? 4 : 3); n++) { \
                if (W != 0) acc[0][n] = MFMA16(wf[n], XC[0], acc[0][n]); \
                _Pragma("unroll") for (int m = 1; m < 5; m++) acc[m][n] = MFMA16(wf[n], XC[m], acc[m][n]); \
                acs[n] = MFMA16(wf[n], ones, acs[n]); \
                SB0; \
                wf[n] = cat8(tr_read(base + woff[n]), tr_read(base + woff[n] + 4 * 448)); \
                SB0; \
            } \
            if (W == 0) { ssum[0] = MFMA16(XC[0], ones, ssum[0]); ssq[0] = MFMA16(XC[0], XC[0], ssq[0]); } \
            if (W == 1) { ssum[0] = MFMA16(XC[1], ones, ssum[0]); ssq[0] = MFMA16(XC[1], XC[1], ssq[0]); \
                          ssum[1] = MFMA16(XC[2], ones, ssum[1]); ssq[1] = MFMA16(XC[2], XC[2], ssq[1]); } \
            if (W == 2) { ssum[0] = MFMA16(XC[3], ones, ssum[0]); ssq[0] = MFMA16(XC[3], XC[3], ssq[0]); \
                          ssum[1] = MFMA16(XC[4], ones, ssum[1]); ssq[1] = MFMA16(XC[4], XC[4], ssq[1]); } \
            SB0; \
            POST; \
            BAR; }
#define K1_LOOPS(W) { \
        _Pragma("unroll 1") for (int i = 0; i < 6; i += 2) { \
            K1_STEP(W, xa, xb, xissue(i + 3), WAIT_V(6)); \
            i++; K1_STEP(W, xb, xa, xissue(i + 3), WAIT_V(6)); i--; \
        } \
        _Pragma("unroll 1") for (int i = 6; i < 18; i += 2) { \
            K1_STEP(W, xa, xb, , WAIT_V(0)); \
            i++; K1_STEP(W, xb, xa, , WAIT_V(0)); i--; \
        } }
        if (wave == 0) K1_LOOPS(0) else if (wave == 1) K1_LOOPS(1) else if (wave == 2) K1_LOOPS(2) else K1_LOOPS(3)
        if (wave < 3 && q == g) {
#pragma unroll
            for (int t = 0; t < 2; t++) {
                if (t == 0 || wave > 0) {
                    const float mean = ssum[t][p] * (1.f / 576.f);
                    const float var = fmaxf(ssq[t][p] * (1.f / 576.f) - mean * mean, 0.f);
                    const int r = (wave == 0 ? 0 : 2 * wave - 1 + t) * 16 + (lane & 15);
                    mus[r] = mean; mus[80 + r] = rsqrtf(var + 1e-5f);
                }
            }
        }
        const u4v* pcw = (const u4v*)(w1 + (size_t)(rthird * 192 + wave * 48 + (lane >> 3)) * F_ + cchunk * 64) + (lane & 7);
#pragma unroll
        for (int j = 0; j < 6; j++) cw[j] = pcw[(size_t)j * 8 * F_ / 8];
    }
    __syncthreads();
    constexpr int RSB = 456;
    if (wave < 4) {
        const int nt0 = wave == 0 ? 0 : 1 + 3 * wave;
        const int nn = (wave == 0 || wave == 3) ? 4 : 3;
        int lco[4][2];
#pragma unroll
        for (int n = 0; n < 4; n++)
#pragma unroll
            for (int h2 = 0; h2 < 2; h2++) {
                const int c = (nt0 + n) * 16 + 4 * g + 2 * h2;
                const int hc = c - 144, hd = hc / 18;
                lco[n][h2] = (n < nn && c < 216) ? (c < 144 ? c : 144 + hd * 20 + (hc - hd * 18)) * 2 : -1;
            }
        float mean[5], rs[5];
#pragma unroll
        for (int m = 0; m < 5; m++) { mean[m] = mus[m * 16 + (lane & 15)]; rs[m] = mus[80 + m * 16 + (lane & 15)]; }
        const int dump = K1_RED + 1024 + lane * 4;
#pragma unroll
        for (int m = 0; m < 5; m++) {
            const int rowo = (m * 16 + (lane & 15)) * RSB;
            const bool mok = m > 0 || wave > 0;
#pragma unroll
            for (int n = 0; n < 4; n++) {
                const f4v v = (acc[m][n] - acs[n] * mean[m]) * rs[m];
                *(unsigned*)(smem + (mok && lco[n][0] >= 0 ? rowo + lco[n][0] : dump)) = pack_bf2(v[0], v[1]);
                *(unsigned*)(smem + (mok && lco[n][1] >= 0 ? rowo + lco[n][1] : dump)) = pack_bf2(v[2], v[3]);
            }
        }
    }
    __syncthreads();
    if (wave == 4 && blockIdx.x < 144) {
        WAIT_V(0);
        const char* sb = smem + K1_DUMMY + (8 * g + q) * 144 + 8 * p;
        char* dst = wof + (size_t)blockIdx.x * 5 * 1024 + lane * 16;
#pragma unroll
        for (int nt = 0; nt < 5; nt++)
            *(s8v*)(dst + nt * 1024) = cat8(tr_read(sb + nt * 32), tr_read(sb + 4 * 144 + nt * 32));
    }
    {
        const int head = wave >> 1, li = lane & 15;
        const unsigned qmask0 = g < 3 ? 0xffffffffu : 0u, qmask1 = g < 2 ? 0xffffffffu : 0u;
        const int t0 = (wave & 1) * 2;
        u4v qf[2], kf[3];
        s8v vf0[2], vf1[2];
#pragma unroll
        for (int uu = 0; uu < 2; uu++) {
            const char* qp = smem + (16 + 16 * (t0 + uu) + li) * RSB + (head * 18 + 8 * g) * 2;
#pragma unroll
            for (int i = 0; i < 4; i++) qf[uu][i] = *(const unsigned*)(qp + 4 * i) & (i == 0 ? qmask0 : qmask1);
        }
#pragma unroll
        for (int kt = 0; kt < 3; kt++) {
            const char* kp = smem + (16 * (t0 + kt) + li) * RSB + (72 + head * 18 + 8 * g) * 2;
#pragma unroll
            for (int i = 0; i < 4; i++) kf[kt][i] = *(const unsigned*)(kp + 4 * i);
        }
#pragma unroll
        for (int uu = 0; uu < 2; uu++) {
            const char* vp = smem + (16 * (t0 + uu) + 4 * g + q) * RSB + (144 + head * 20 + 4 * p) * 2;
            vf0[uu] = cat8(tr_read(vp), tr_read(vp + 16 * RSB));
            vf1[uu] = cat8(tr_read(vp + 32), tr_read(vp + 16 * RSB + 32));
        }
        const f4v z4 = f4v{0.f, 0.f, 0.f, 0.f};
        f4v sv[2][2];
#pragma unroll
        for (int uu = 0; uu < 2; uu++)
#pragma unroll
            for (int tau = 0; tau < 2; tau++)
                sv[uu][tau] = MFMA16(__builtin_bit_cast(s8v, kf[uu + tau]), __builtin_bit_cast(s8v, qf[uu]), z4);
        bool ok[2][8]; float mx[2], den[2]; u4v pb[2];
#pragma unroll
        for (int uu = 0; uu < 2; uu++) {
            const bool tile0_ok = !(s0 == 0 && t0 + uu == 0);
            mx[uu] = -3.0e38f;
#pragma unroll
            for (int j = 0; j < 8; j++) {
                const int dl = 16 * (j >> 2) + 4 * g + (j & 3) - li;
                ok[uu][j] = !(dl & 1) && dl >= 2 && dl <= 16 && (j >= 4 || tile0_ok);
                mx[uu] = fmaxf(mx[uu], ok[uu][j] ? sv[uu][j >> 2][j & 3] : -3.0e38f);
            }
        }
#pragma unroll
        for (int uu = 0; uu < 2; uu++) mx[uu] = xmax4(mx[uu]);
#pragma unroll
        for (int uu = 0; uu < 2; uu++) {
            float a[8];
#pragma unroll
            for (int j = 0; j < 8; j++) {
                const float e = __builtin_amdgcn_exp2f((sv[uu][j >> 2][j & 3] - mx[uu]) * (0.23570226039551584f * 1.4426950408889634f));
                a[j] = ok[uu][j] ? e : 0.f;
            }
            pb[uu] = u4v{pack_bf2(a[0], a[1]), pack_bf2(a[2], a[3]), pack_bf2(a[4], a[5]), pack_bf2(a[6], a[7])};
            den[uu] = 0.f;
#pragma unroll
            for (int j = 0; j < 4; j++) den[uu] += bf_lo(pb[uu][j]) + bf_hi(pb[uu][j]);
        }
#pragma unroll
        for (int uu = 0; uu < 2; uu++) den[uu] = 1.f / xsum4(den[uu]);
#pragma unroll
        for (int uu = 0; uu < 2; uu++) {
            const f4v o0 = MFMA16(vf0[uu], __builtin_bit_cast(s8v, pb[uu]), z4) * den[uu];
            const f4v o1 = MFMA16(vf1[uu], __builtin_bit_cast(s8v, pb[uu]), z4) * den[uu];
            char* ob = of + (size_t)((s0 >> 4) + t0 + uu) * (18 * 1024) + li * 16;
            const int c0 = hg * 72 + head * 18 + 4 * g, c1 = c0 + 2, c2 = hg * 72 + head * 18 + 16;
            *(unsigned*)(ob + (c0 >> 5) * 1024 + ((c0 & 31) >> 3) * 256 + (c0 & 7) * 2) = pack_bf2(o0[0], o0[1]);
            *(unsigned*)(ob + (c1 >> 5) * 1024 + ((c1 & 31) >> 3) * 256 + (c1 & 7) * 2) = pack_bf2(o0[2], o0[3]);
            if (g == 0) *(unsigned*)(ob + (c2 >> 5) * 1024 + ((c2 & 31) >> 3) * 256 + (c2 & 7) * 2) = pack_bf2(o1[0], o1[1]);
        }
    }
    if (wave < 4) {
        float a[8];
#pragma unroll
        for (int j = 0; j < 8; j++) a[j] = 0.f;
#pragma unroll
        for (int i = 0; i < 6; i++)
#pragma unroll
            for (int j = 0; j < 4; j++) { a[2 * j] += bf_lo(cw[i][j]); a[2 * j + 1] += bf_hi(cw[i][j]); }
#pragma unroll
        for (int j = 0; j < 8; j++) {
            float t = a[j];
            t += __shfl_xor(t, 8); t += __shfl_xor(t, 16); t += __shfl_xor(t, 32);
            a[j] = t;
        }
        if (lane < 8 && blockIdx.x < 216) {
            float* rq = cs1 + (rthird * 4 + wave) * F_ + cchunk * 64 + lane * 8;
            *(f4v*)rq = f4v{a[0], a[1], a[2], a[3]};
            *(f4v*)(rq + 4) = f4v{a[4], a[5], a[6], a[7]};
        }
    }
}

constexpr int K2_LDS = 8 * 12 * 1024;

__global__ __launch_bounds__(512, 2) void k2_wo(const char* __restrict__ of, const char* __restrict__ wof,
                                                const bf16_t* __restrict__ x, bf16_t* __restrict__ h,
                                                char* __restrict__ hT, f2v* __restrict__ stats) {
    extern __shared__ __attribute__((aligned(16))) char smem[];
    const int tid = threadIdx.x, lane = tid & 63;
    const int wave = __builtin_amdgcn_readfirstlane(tid >> 6);
    const int rt = (blockIdx.x & 7) * 4 + ((blockIdx.x >> 3) & 3), ct = blockIdx.x >> 5;
    const int r0 = rt * 64, c0 = ct * 72;
    const int g = lane >> 4;
    const int kq = wave & 3, nh = wave >> 2, nb = nh * 3;
    const char* pa = of + ((size_t)rt * 4 * 18 + kq) * 1024 + lane * 16;
    const char* pw = wof + (((size_t)ct * 18 + kq) * 5 + nb) * 1024 + lane * 16;
    s8v fa[5][4], fw[5][3];
#pragma unroll
    for (int j = 0; j < 5; j++) {
        if (j < 4 || kq < 2) {
#pragma unroll
            for (int m = 0; m < 4; m++) fa[j][m] = *(const s8v*)(pa + m * (18 * 1024) + j * 4096);
#pragma unroll
            for (int n = 0; n < 3; n++) if (n < 2 || nh == 0) fw[j][n] = *(const s8v*)(pw + n * 1024 + j * 20480);
        }
    }
    const int row = r0 + (wave & 3) * 16 + (lane & 15);
    u2v xx[3];
#pragma unroll
    for (int n = 0; n < 3; n++) {
        int col = (nb + n) * 16 + 4 * g; if (col > 68) col = 68;
        xx[n] = *(const u2v*)(x + (size_t)row * D_ + c0 + col);
    }
    f4v acc[4][3];
#pragma unroll
    for (int m = 0; m < 4; m++)
#pragma unroll
        for (int n = 0; n < 3; n++) acc[m][n] = f4v{0, 0, 0, 0};
#pragma unroll
    for (int j = 0; j < 5; j++) {
        if (j < 4 || kq < 2) {
#pragma unroll
            for (int n = 0; n < 3; n++)
                if (n < 2 || nh == 0) {
#pragma unroll
                    for (int m = 0; m < 4; m++) acc[m][n] = MFMA16(fw[j][n], fa[j][m], acc[m][n]);
                }
        }
    }
    f4v* ex = (f4v*)smem + wave * (12 * 64) + lane;
#pragma unroll
    for (int m = 0; m < 4; m++)
#pragma unroll
        for (int n = 0; n < 3; n++) if (n < 2 || nh == 0) ex[(m * 3 + n) * 64] = acc[m][n];
    __syncthreads();
    float hv[3][4];
    float sum = 0.f;
#pragma unroll
    for (int n = 0; n < 3; n++) {
        const f4v* e = (const f4v*)smem + (nh * 4 * 12 + (wave & 3) * 3 + n) * 64 + lane;
        const int col = (nb + n) * 16 + 4 * g;
        if (col < 72) {
            f4v a = (e[0] + e[768]) + (e[2 * 768] + e[3 * 768]);
            unsigned p0 = pack_bf2(bf_lo(xx[n][0]) + a[0], bf_hi(xx[n][0]) + a[1]);
            unsigned p1 = pack_bf2(bf_lo(xx[n][1]) + a[2], bf_hi(xx[n][1]) + a[3]);
            *(u2v*)(h + (size_t)row * D_ + c0 + col) = u2v{p0, p1};
            {
                int cc = c0 + col;
                *(u2v*)(hT + (size_t)((row >> 4) * 18 + (cc >> 5)) * 1024 + (((cc & 31) >> 3) * 16 + (row & 15)) * 16 + (cc & 7) * 2) = u2v{p0, p1};
            }
            hv[n][0] = bf_lo(p0); hv[n][1] = bf_hi(p0); hv[n][2] = bf_lo(p1); hv[n][3] = bf_hi(p1);
            sum += hv[n][0] + hv[n][1] + hv[n][2] + hv[n][3];
        } else {
            hv[n][0] = hv[n][1] = hv[n][2] = hv[n][3] = 0.f;
        }
    }
    sum = xsum4(sum);
    const float mean = sum * (nh == 0 ? 1.f / 48.f : 1.f / 24.f);
    float m2 = 0.f;
#pragma unroll
    for (int n = 0; n < 3; n++) {
        const int col = (nb + n) * 16 + 4 * g;
        if (col < 72) {
#pragma unroll
            for (int j = 0; j < 4; j++) { float d = hv[n][j] - mean; m2 += d * d; }
        }
    }
    m2 = xsum4(m2);
    if (g == 0) stats[((size_t)row * 8 + ct) * 2 + nh] = f2v{sum, m2};
}

constexpr int K3_STAGE = 8192 + 18432;
constexpr int K3_NB = 5;
constexpr int K3_PAR = K3_NB * K3_STAGE;
constexpr int K3_LDS = K3_PAR + 1024 + 2304;

__device__ __forceinline__ float gelu_tanh(float v) {
    float t = v * v;
    float z = v * (-2.302208198f * 0.044715f * t - 2.302208198f);
    float e = __builtin_amdgcn_exp2f(z);
    return v * __builtin_amdgcn_rcpf(1.f + e);
}

__device__ __forceinline__ f2v gelu_tanh2(f2v v) {
    f2v t = v * v;
    f2v z = v * (t * (-2.302208198f * 0.044715f) + (-2.302208198f));
    f2v d = f2v{__builtin_amdgcn_exp2f(z[0]), __builtin_amdgcn_exp2f(z[1])} + 1.f;
    return v * f2v{__builtin_amdgcn_rcpf(d[0]), __builtin_amdgcn_rcpf(d[1])};
}

__global__ __launch_bounds__(512, 2) void k3_up(const char* __restrict__ hT, const bf16_t* __restrict__ w1,
                                                const bf16_t* __restrict__ b1, const float* __restrict__ cs1,
                                                const f2v* __restrict__ stats, const bf16_t* __restrict__ w2,
                                                char* __restrict__ h1f, char* __restrict__ w2f) {
    extern __shared__ __attribute__((aligned(16))) char smem[];
    const int tid = threadIdx.x, lane = tid & 63;
    const int wave = __builtin_amdgcn_readfirstlane(tid >> 6);
    const int xcd = blockIdx.x & 7, idx = blockIdx.x >> 3;
    const int rt = (xcd & 1) * 8 + (idx & 7), ct = (xcd >> 1) * 4 + (idx >> 3);
    const int r0 = rt * 128, c0 = ct * 288;
    const unsigned lbase = lds_addr(smem);
    const int g = lane >> 4, q = (lane & 15) >> 2, p = lane & 3;
    const int xr = lane & 15;
    f2v* mu = (f2v*)(smem + K3_PAR);
    float* csl = (float*)(smem + K3_PAR + 1024);
    float* bbl = csl + 288;
    f4v acc[4][9];
    const int wm = (wave >> 1) & 1, wn = wave & 1;
    if (wave >= 4) {
        const int lw = wave - 4;
        unsigned bsrc[5];
        const unsigned avo = lane * 16;
        const char* ag = hT + (size_t)(rt * 8 + lw * 2) * 18 * 1024;
#pragma unroll
        for (int i = 0; i < 5; i++) {
            int bi = lw * 5 + i; if (bi > 17) bi = 17;
            int off = bi * 1024 + lane * 16;
            int k = off / 576, r = off - k * 576;
            int nt = (r >> 5) ^ ((k >> 3) & 1);
            bsrc[i] = (unsigned)((k * F_ + c0 + nt * 16 + ((r >> 4) & 1) * 8) * 2);
        }
        auto issue = [&](int st) {
            int sc = st > 17 ? 17 : st;
            unsigned lb = lbase + (st % K3_NB) * K3_STAGE;
            const char* ga = ag + sc * 1024;
            const char* gb = (const char*)w1 + (size_t)sc * 32 * F_ * 2;
            glds16s(ga, avo, lb + (lw * 2) * 1024);
            glds16s(ga + 18 * 1024, avo, lb + (lw * 2 + 1) * 1024);
#pragma unroll
            for (int i = 0; i < 5; i++) glds16s(gb, bsrc[i], lb + 8192 + (lw * 5 + i > 17 ? 17 : lw * 5 + i) * 1024);
        };
#pragma unroll
        for (int st = 0; st < K3_NB - 1; st++) issue(st);
        WAIT_V(21);
        BAR;
        for (int i = -1; i < 14; i++) {
            if (i >= 0) issue(i + K3_NB - 1);
            WAIT_V(14);
            BAR;
        }
        WAIT_V(7); BAR;
        WAIT_V(0); BAR;
#pragma unroll
        for (int uu = 0; uu < 2; uu++) {
            if (uu == 0 || (lw == 0 && blockIdx.x < 128)) {
                const int u = uu * 1024 + lw * 256 + blockIdx.x;
                const int ctp = u / 144, kst = u - ctp * 144;
#pragma unroll
                for (int j = 0; j < 5; j++) {
                    int c = j * 64 + lane; if (c > 287) c = 287;
                    int row = c / 9, pc = c - row * 9;
                    glds16s(w2, (unsigned)(((kst * 32 + row) * D_ + ctp * 72 + pc * 8) * 2), lbase + 3 * K3_STAGE + (uu * 4 + lw) * 5120 + j * 1024);
                }
            }
        }
        BAR; BAR;
    } else {
        {
            float c0v = 0.f, b0v = bf2f(b1[c0 + tid]);
            float c1v = 0.f, b1v = 0.f;
            {
                float t0[12], t1[12];
                const int c1i = c0 + 256 + (tid & 31);
#pragma unroll
                for (int i = 0; i < 12; i++) { t0[i] = cs1[i * F_ + c0 + tid]; t1[i] = cs1[i * F_ + c1i]; }
                b1v = bf2f(b1[c1i]);
#pragma unroll
                for (int i = 0; i < 12; i++) { c0v += t0[i]; c1v += t1[i]; }
            }
            if (tid < 128) {
                const f4v* sp = (const f4v*)(stats + (size_t)(r0 + tid) * 16);
                f4v v[8];
#pragma unroll
                for (int i = 0; i < 8; i++) v[i] = sp[i];
                float tot = 0.f;
#pragma unroll
                for (int i = 0; i < 8; i++) tot += v[i][0] + v[i][2];
                float mean = tot * (1.f / 576.f);
                float M2 = 0.f;
#pragma unroll
                for (int i = 0; i < 8; i++) {
                    float d0 = v[i][0] * (1.f / 48.f) - mean, d1 = v[i][2] * (1.f / 24.f) - mean;
                    M2 += v[i][1] + v[i][3] + (48.f * d0 * d0 + 24.f * d1 * d1);
                }
                float rstd = rsqrtf(M2 * (1.f / 576.f) + 1e-5f);
                mu[tid] = f2v{mean * rstd, rstd};
            }
            csl[tid] = c0v; bbl[tid] = b0v;
            if (tid < 32) { csl[256 + tid] = c1v; bbl[256 + tid] = b1v; }
        }
#pragma unroll
        for (int m = 0; m < 4; m++)
#pragma unroll
            for (int n = 0; n < 9; n++) acc[m][n] = f4v{0, 0, 0, 0};
        const int dlt = (g & 1) ? 32 : 0;
        const int wb0 = 8192 + (8 * g + q) * 576 + wn * 9 * 32 + 8 * p;
        const int wbe = wb0 + ((wn & 1) ? -dlt : dlt);
        const int wbo = wb0 + ((wn & 1) ? dlt : -dlt);
        const int xoff = wm * 4096 + lane * 16;
        s8v xa[4], xb[4], wf[9];
        BAR;
        {
            const char* base = smem;
#pragma unroll
            for (int m = 0; m < 4; m++) xa[m] = *(const s8v*)(base + xoff + m * 1024);
#pragma unroll
            for (int n = 0; n < 9; n++) {
                const char* wb = base + ((n & 1) ? wbo : wbe) + n * 32;
                wf[n] = cat8(tr_read(wb), tr_read(wb + 4 * 576));
            }
        }
        WAIT_L(0);
        BAR;
#define K3_STEP(XC, XN) { \
            const char* base = smem + ((i + 1) % K3_NB) * K3_STAGE; \
            s4v wlo, whi; \
            _Pragma("unroll") for (int n = 0; n < 9; n++) { \
                const char* wbp = base + (((n - 1) & 1) ? wbo : wbe) + (n - 1) * 32; \
                acc[0][n] = MFMA16(wf[n], XC[0], acc[0][n]); SB0; \
                if (n > 0) { wlo = tr_read(wbp); SB0; } \
                acc[1][n] = MFMA16(wf[n], XC[1], acc[1][n]); SB0; \
                if (n > 0) { whi = tr_read(wbp + 4 * 576); wf[n - 1] = cat8(wlo, whi); SB0; } \
                acc[2][n] = MFMA16(wf[n], XC[2], acc[2][n]); SB0; \
                if (n < 4) { XN[n] = *(const s8v*)(base + xoff + n * 1024); SB0; } \
                acc[3][n] = MFMA16(wf[n], XC[3], acc[3][n]); SB0; \
            } \
            { const char* wbp = base + wbe + 8 * 32; wf[8] = cat8(tr_read(wbp), tr_read(wbp + 4 * 576)); } \
            BAR; }
#pragma unroll 1
        for (int i = 0; i < 18; i += 2) {
            K3_STEP(xa, xb);
            i++; K3_STEP(xb, xa); i--;
        }
    }
    __syncthreads();
    f4v* ex = (f4v*)smem + (wave & 3) * (18 * 64) + lane;
    if (wave < 4) {
#pragma unroll
        for (int m = 0; m < 2; m++)
#pragma unroll
            for (int n = 0; n < 9; n++) ex[(m * 9 + n) * 64] = acc[m + 2][n];
    }
    __syncthreads();
    if (wave >= 4) {
        __builtin_amdgcn_s_setprio(2);
#pragma unroll
        for (int m = 0; m < 2; m++)
#pragma unroll
            for (int n = 0; n < 9; n++) acc[m][n] = ex[(m * 9 + n) * 64];
    }
    {
        const int mb = (wave >> 2) * 2;
#pragma unroll
        for (int m = 0; m < 2; m++) {
            int rl = wm * 64 + (mb + m) * 16 + xr;
            f2v ms = mu[rl];
            char* ob = h1f + (size_t)((r0 + wm * 64 + (mb + m) * 16) >> 4) * (144 * 1024) + lane * 16;
            u2v pv[9];
#pragma unroll
            for (int n = 0; n < 9; n++) {
                f4v a = acc[m][n];
                int cl = wn * 144 + n * 16 + 4 * g;
                f4v cs = *(const f4v*)(csl + cl), bb = *(const f4v*)(bbl + cl);
                f2v t01 = f2v{bb[0], bb[1]} - f2v{cs[0], cs[1]} * ms[0];
                f2v t23 = f2v{bb[2], bb[3]} - f2v{cs[2], cs[3]} * ms[0];
                f2v v01 = gelu_tanh2(f2v{a[0], a[1]} * ms[1] + t01);
                f2v v23 = gelu_tanh2(f2v{a[2], a[3]} * ms[1] + t23);
                pv[n] = u2v{pack_bf2(v01[0], v01[1]), pack_bf2(v23[0], v23[1])};
            }
            if (m == 0) {
            } else {
            }
            char* oc = ob + (c0 >> 5) * 1024;
            if (wn == 0) {
#pragma unroll
                for (int j = 0; j < 4; j++) *(u4v*)(oc + j * 1024) = u4v{pv[2 * j][0], pv[2 * j][1], pv[2 * j + 1][0], pv[2 * j + 1][1]};
                *(u2v*)(oc + 4 * 1024) = pv[8];
            } else {
                *(u2v*)(oc + 4 * 1024 + 8) = pv[0];
#pragma unroll
                for (int j = 0; j < 4; j++) *(u4v*)(oc + (5 + j) * 1024) = u4v{pv[2 * j + 1][0], pv[2 * j + 1][1], pv[2 * j + 2][0], pv[2 * j + 2][1]};
            }
        }
    }
    if (wave >= 4) {
        WAIT_V(0);
#pragma unroll
        for (int uu = 0; uu < 2; uu++) {
            if (uu == 0 || (wave == 4 && blockIdx.x < 128)) {
                const int u = uu * 1024 + (wave - 4) * 256 + blockIdx.x;
                const char* sb = smem + 3 * K3_STAGE + (uu * 4 + wave - 4) * 5120 + (4 * g + q) * 144 + 8 * p;
                char* dst = w2f + (size_t)u * 5 * 1024 + lane * 16;
#pragma unroll
                for (int nt = 0; nt < 5; nt++)
                    *(s8v*)(dst + nt * 1024) = cat8(tr_read(sb + nt * 32), tr_read(sb + 16 * 144 + nt * 32));
            }
        }
    }
}

constexpr int K4_LDS = 8 * 20 * 1024;
constexpr int K4_D = 3;

__global__ __launch_bounds__(512, 2) void k4_down(const char* __restrict__ h1f, const char* __restrict__ w2f,
                                                  const bf16_t* __restrict__ h, const bf16_t* __restrict__ b2,
                                                  bf16_t* __restrict__ out) {
    extern __shared__ __attribute__((aligned(16))) char smem[];
    const int tid = threadIdx.x, lane = tid & 63;
    const int wave = __builtin_amdgcn_readfirstlane(tid >> 6);
    const int xcd = blockIdx.x & 7, idx = blockIdx.x >> 3;
    const int rt = (xcd >> 1) * 8 + (idx & 7), ct = (xcd & 1) * 4 + (idx >> 3);
    const int g = lane >> 4;
    const char* pa = h1f + ((size_t)rt * 4 * 144 + wave) * 1024 + lane * 16;
    const char* pw = w2f + ((size_t)ct * 144 + wave) * 5 * 1024 + lane * 16;
    f4v acc[4][5];
#pragma unroll
    for (int m = 0; m < 4; m++)
#pragma unroll
        for (int n = 0; n < 5; n++) acc[m][n] = f4v{0, 0, 0, 0};
    s8v fa[K4_D][4], fw[K4_D][5];
#pragma unroll
    for (int j = 0; j < K4_D; j++) {
#pragma unroll
        for (int m = 0; m < 4; m++) fa[j][m] = *(const s8v*)(pa + m * (144 * 1024) + j * 8192);
#pragma unroll
        for (int n = 0; n < 5; n++) fw[j][n] = *(const s8v*)(pw + n * 1024 + j * 40960);
        SB0;
    }
#pragma unroll
    for (int j = 0; j < 18; j++) {
        const int sl = j % K4_D;
        SB0;
#pragma unroll
        for (int n = 0; n < 5; n++)
#pragma unroll
            for (int m = 0; m < 4; m++) acc[m][n] = MFMA16(fw[sl][n], fa[sl][m], acc[m][n]);
        SB0;
        if (j + K4_D < 18) {
#pragma unroll
            for (int m = 0; m < 4; m++) fa[sl][m] = *(const s8v*)(pa + m * (144 * 1024) + (j + K4_D) * 8192);
#pragma unroll
            for (int n = 0; n < 5; n++) fw[sl][n] = *(const s8v*)(pw + n * 1024 + (j + K4_D) * 40960);
        }
        SB0;
    }
    u2v hh[3], bb[3];
#pragma unroll
    for (int j = 0; j < 3; j++) {
        int t = wave + 8 * j; if (t > 19) t = 19;
        const int m = t / 5, n = t - m * 5;
        const int row = rt * 64 + m * 16 + (lane & 15);
        int cl = n * 16 + 4 * g; if (cl > 68) cl = 68;
        hh[j] = *(const u2v*)(h + (size_t)row * D_ + ct * 72 + cl);
        bb[j] = *(const u2v*)(b2 + ct * 72 + cl);
    }
    f4v* ex = (f4v*)smem + wave * (20 * 64) + lane;
#pragma unroll
    for (int m = 0; m < 4; m++)
#pragma unroll
        for (int n = 0; n < 5; n++) ex[(m * 5 + n) * 64] = acc[m][n];
    __syncthreads();
#pragma unroll
    for (int j = 0; j < 3; j++) {
        const int t = wave + 8 * j;
        if (t < 20) {
            const int m = t / 5, n = t - m * 5;
            const f4v* e = (const f4v*)smem + t * 64 + lane;
            f4v a = ((e[0] + e[1280]) + (e[2 * 1280] + e[3 * 1280])) + ((e[4 * 1280] + e[5 * 1280]) + (e[6 * 1280] + e[7 * 1280]));
            const int row = rt * 64 + m * 16 + (lane & 15);
            const int cl = n * 16 + 4 * g;
            float v0 = bf_lo(hh[j][0]) + (a[0] + bf_lo(bb[j][0]));
            float v1 = bf_hi(hh[j][0]) + (a[1] + bf_hi(bb[j][0]));
            float v2 = bf_lo(hh[j][1]) + (a[2] + bf_lo(bb[j][1]));
            float v3 = bf_hi(hh[j][1]) + (a[3] + bf_hi(bb[j][1]));
            if (cl < 72) *(u2v*)(out + (size_t)row * D_ + ct * 72 + cl) = u2v{pack_bf2(v0, v1), pack_bf2(v2, v3)};
        }
    }
}

extern "C" void kernel_launch(void* const* d_in, const int* in_sizes, int n_in,
                              void* d_out, int out_size, void* d_ws, size_t ws_size,
                              hipStream_t stream) {
    const bf16_t* x = (const bf16_t*)d_in[0];
    const bf16_t* wqkv = (const bf16_t*)d_in[1];
    const bf16_t* wo = (const bf16_t*)d_in[2];
    const bf16_t* w1 = (const bf16_t*)d_in[4];
    const bf16_t* b1 = (const bf16_t*)d_in[5];
    const bf16_t* w2 = (const bf16_t*)d_in[6];
    const bf16_t* b2 = (const bf16_t*)d_in[7];
    bf16_t* out = (bf16_t*)d_out;
    char* ws = (char*)d_ws;
    char* of = ws;
    char* wof = ws + (3 << 20);
    bf16_t* h = (bf16_t*)(ws + (4 << 20));
    char* hT = ws + (12 << 20);
    f2v* stats = (f2v*)(ws + (8 << 20));
    float* cs1 = (float*)(ws + (9 << 20));
    char* h1 = (char*)(ws + (16 << 20));
    char* w2f = ws + (48 << 20);

    hipFuncSetAttribute((const void*)k1_qkv_attn, hipFuncAttributeMaxDynamicSharedMemorySize, K1_LDS);
    hipFuncSetAttribute((const void*)k2_wo, hipFuncAttributeMaxDynamicSharedMemorySize, K2_LDS);
    hipFuncSetAttribute((const void*)k3_up, hipFuncAttributeMaxDynamicSharedMemorySize, K3_LDS);
    hipFuncSetAttribute((const void*)k4_down, hipFuncAttributeMaxDynamicSharedMemorySize, K4_LDS);

    k1_qkv_attn<<<256, 512, K1_LDS, stream>>>(x, wqkv, w1, wo, of, wof, cs1);
    k2_wo<<<256, 512, K2_LDS, stream>>>(of, wof, x, h, hT, stats);
    k3_up<<<256, 512, K3_LDS, stream>>>(hT, w1, b1, cs1, stats, w2, h1, w2f);
    k4_down<<<256, 512, K4_LDS, stream>>>(h1, w2f, h, b2, out);
}
```

```cpp
#include <hip/hip_runtime.h>
#include <hip/hip_bf16.h>

typedef __attribute__((ext_vector_type(8))) short s8v;
typedef __attribute__((ext_vector_type(4))) short s4v;
typedef __attribute__((ext_vector_type(4))) short v4i16_t;
typedef __attribute__((ext_vector_type(4))) float f4v;
typedef __attribute__((ext_vector_type(2))) float f2v;
typedef __attribute__((ext_vector_type(4))) unsigned u4v;
typedef __attribute__((ext_vector_type(2))) unsigned u2v;
typedef unsigned short bf16_t;

constexpr int S_ = 2048, D_ = 576, NQKV = 1728, F_ = 4608;

__device__ __forceinline__ unsigned lds_addr(const void* l) {
    return (unsigned)(size_t)(__attribute__((address_space(3))) const char*)l;
}
__device__ __forceinline__ void glds16(const void* g, unsigned dst) {
    unsigned keep;
    asm volatile("s_mov_b32 %0, m0\n\ts_mov_b32 m0, %2\n\ts_nop 0\n\tglobal_load_lds_dwordx4 %1, off\n\ts_mov_b32 m0, %0"
                 : "=&s"(keep) : "v"(g), "s"(dst) : "memory");
}
__device__ __forceinline__ void glds16s(const void* sbase, unsigned voff, unsigned dst) {
    unsigned keep;
    asm volatile("s_mov_b32 %0, m0\n\ts_mov_b32 m0, %3\n\ts_nop 0\n\tglobal_load_lds_dwordx4 %1, %2\n\ts_mov_b32 m0, %0"
                 : "=&s"(keep) : "v"(voff), "s"(sbase), "s"(dst) : "memory");
}
__device__ __forceinline__ s4v tr_read(const char* p) {
    return __builtin_bit_cast(s4v, __builtin_amdgcn_ds_read_tr16_b64_v4i16((__attribute__((address_space(3))) v4i16_t*)p));
}
__device__ __forceinline__ s8v cat8(s4v a, s4v b) {
    return __builtin_shufflevector(a, b, 0, 1, 2, 3, 4, 5, 6, 7);
}
__device__ __forceinline__ float bf_lo(unsigned u) { return __builtin_bit_cast(float, u << 16); }
__device__ __forceinline__ float bf_hi(unsigned u) { return __builtin_bit_cast(float, u & 0xffff0000u); }
__device__ __forceinline__ float bf2f(bf16_t v) { return __builtin_bit_cast(float, (unsigned)v << 16); }
__device__ __forceinline__ unsigned pack_bf2(float a, float b) {
    unsigned r;
    asm("v_cvt_pk_bf16_f32 %0, %1, %2" : "=v"(r) : "v"(a), "v"(b));
    return r;
}
#define MFMA16(a, b, c) __builtin_amdgcn_mfma_f32_16x16x32_bf16(a, b, c, 0, 0, 0)
#define WAIT_V(n) asm volatile("s_waitcnt vmcnt(" #n ")" ::: "memory")
#define WAIT_L(n) asm volatile("s_waitcnt lgkmcnt(" #n ")" ::: "memory")

__device__ __forceinline__ float xmax4(float v) {
    float a = v, b = v;
    asm volatile("s_nop 1\n\tv_permlane16_swap_b32 %0, %1" : "+v"(a), "+v"(b));
    a = fmaxf(a, b); b = a;
    asm volatile("s_nop 1\n\tv_permlane32_swap_b32 %0, %1" : "+v"(a), "+v"(b));
    return fmaxf(a, b);
}
__device__ __forceinline__ float xsum4(float v) {
    float a = v, b = v;
    asm volatile("s_nop 1\n\tv_permlane16_swap_b32 %0, %1" : "+v"(a), "+v"(b));
    a = a + b; b = a;
    asm volatile("s_nop 1\n\tv_permlane32_swap_b32 %0, %1" : "+v"(a), "+v"(b));
    return a + b;
}
#define SB0 __builtin_amdgcn_sched_barrier(0)
#define BAR __builtin_amdgcn_s_barrier()
constexpr int K1_XN_BYTES = 9 * 80 * 128;
constexpr int K1_STAGE = 14336;
constexpr int K1_NB = 4;
constexpr int K1_DUMMY = K1_XN_BYTES + K1_NB * K1_STAGE;
constexpr int K1_RED = K1_DUMMY + 5120;
constexpr int K1_LDS = K1_RED + 2048;

__global__ __launch_bounds__(512, 2) void k1_qkv_attn(const bf16_t* __restrict__ x, const bf16_t* __restrict__ wqkv,
                                                      const bf16_t* __restrict__ w1, const bf16_t* __restrict__ wo,
                                                      char* __restrict__ of, char* __restrict__ wof,
                                                      float* __restrict__ cs1) {
    extern __shared__ __attribute__((aligned(16))) char smem[];
    const int tid = threadIdx.x, lane = tid & 63;
    const int wave = __builtin_amdgcn_readfirstlane(tid >> 6);
    const int xcd = blockIdx.x & 7, idx = blockIdx.x >> 3;
    const int hg = (xcd & 1) * 4 + (idx >> 3), rt = (xcd >> 1) * 8 + (idx & 7);
    const int s0 = rt * 64;
    const unsigned lring = lds_addr(smem) + K1_XN_BYTES;
    const int g = lane >> 4, q = (lane & 15) >> 2, p = lane & 3;

    const int lw = wave - 4;
    unsigned bsrc[4];
    const int pc0 = lw < 2 ? lw * 4 : 2 + lw * 3;
    const int rot = ((idx & 7) + 2 * (idx >> 3)) % 9;
    auto rblk = [&](int b) { int t = b + rot; return t >= 9 ? t - 9 : t; };
    auto issue = [&](int st) {
        int sc = st > 17 ? 17 : st;
        sc = rblk(sc >> 1) * 2 + (sc & 1);
        unsigned lb = lring + (st % K1_NB) * K1_STAGE;
        const char* gb = (const char*)wqkv + (size_t)sc * 32 * NQKV * 2;
#pragma unroll
        for (int i = 0; i < 3; i++) glds16s(gb, bsrc[i], lb + (pc0 + i) * 1024);
        if (lw < 2) glds16s(gb, bsrc[3], lb + (pc0 + 3) * 1024);
    };
    const unsigned dz = lds_addr(smem) + K1_DUMMY + (wave & 3) * 1024;
    float* mus = (float*)(smem + K1_RED);
    unsigned xo[3];
    auto xissue = [&](int b0) {
        const int b = rblk(b0);
#pragma unroll
        for (int j = 0; j < 3; j++)
            glds16s((const char*)x + b * 128, xo[j], lds_addr(smem) + b * (80 * 128) + (j == 2 && wave >= 2 ? wave + 4 : wave + 4 * j) * 1024);
    };
    if (wave >= 4) {
#pragma unroll
        for (int i = 0; i < 4; i++) {
            int bi = pc0 + i; if (bi > 13) bi = 13;
            int off = bi * 1024 + lane * 16;
            int k = off / 448, r = off - k * 448;
            int nt = (r >> 5) ^ ((k >> 3) & 1);
            int pc = nt * 2 + ((r >> 4) & 1); if (pc > 26) pc = 26;
            int sec = pc / 9, wi = pc - sec * 9;
            bsrc[i] = (unsigned)((k * NQKV + sec * 576 + hg * 72 + wi * 8) * 2);
        }
#pragma unroll
        for (int st = 0; st < K1_NB - 1; st++) issue(st);
    } else {
#pragma unroll
        for (int j = 0; j < 3; j++) {
            int gq = wave + 4 * j; if (gq > 9) gq = wave + 4;
            int row = gq * 8 + (lane >> 3);
            int s = s0 - 16 + row; if (s < 0) s = 0;
            xo[j] = (unsigned)(s * (D_ * 2) + (((lane & 7) ^ (lane >> 3)) << 4));
        }
#pragma unroll
        for (int b = 0; b < 3; b++) xissue(b);
    }

    f4v acc[5][4], acs[4], ssum[2], ssq[2];
    u4v cw[6];
    const int cu = blockIdx.x < 216 ? blockIdx.x : blockIdx.x - 216;
    const int cchunk = cu % 72, rthird = cu / 72;
    if (wave >= 4) {
        if (lw < 2) WAIT_V(8); else WAIT_V(6);
        __syncthreads();
        if (lw < 2) WAIT_V(4); else WAIT_V(3);
        BAR;
        for (int i = 0; i < 15; i++) {
            issue(i + K1_NB - 1);
            if (lw < 2) WAIT_V(4); else WAIT_V(3);
            BAR;
        }
        WAIT_V(0); BAR;
        BAR; BAR;
        {
            if (wave == 4 && blockIdx.x < 144) {
                const int ctp = blockIdx.x / 18, kst = blockIdx.x - ctp * 18;
#pragma unroll
                for (int j = 0; j < 5; j++) {
                    int c = j * 64 + lane; if (c > 287) c = 287;
                    int row = c / 9, pc = c - row * 9;
                    glds16s(wo, (unsigned)(((kst * 32 + row) * D_ + ctp * 72 + pc * 8) * 2), lds_addr(smem) + K1_DUMMY + j * 1024);
                }
            }
        }
    } else {
        const int nt0 = wave == 0 ? 0 : 1 + 3 * wave;
        const int nn = (wave == 0 || wave == 3) ? 4 : 3;
#pragma unroll
        for (int m = 0; m < 5; m++)
#pragma unroll
            for (int n = 0; n < 4; n++) acc[m][n] = f4v{0, 0, 0, 0};
#pragma unroll
        for (int n = 0; n < 4; n++) acs[n] = f4v{0, 0, 0, 0};
        ssum[0] = ssum[1] = ssq[0] = ssq[1] = f4v{0, 0, 0, 0};
        const s8v ones = s8v{0x3F80, 0x3F80, 0x3F80, 0x3F80, 0x3F80, 0x3F80, 0x3F80, 0x3F80};
        const int xr = lane & 15;
        int woff[4];
#pragma unroll
        for (int n = 0; n < 4; n++) woff[n] = K1_XN_BYTES + (8 * g + q) * 448 + (((nt0 + n) ^ (g & 1)) << 5) + 8 * p;
        s8v xa[5], xb[5], wf[4];
        auto rdx = [&](s8v* X, int st) {
            const char* xbp = smem + rblk(st >> 1) * (80 * 128) + xr * 128 + ((((st & 1) * 4 + g) ^ (xr & 7)) << 4);
#pragma unroll
            for (int m = 0; m < 5; m++) X[m] = *(const s8v*)(xbp + m * 2048);
        };
        WAIT_V(6);
        __syncthreads();
        rdx(xa, 0);
#pragma unroll
        for (int n = 0; n < 4; n++) wf[n] = cat8(tr_read(smem + woff[n]), tr_read(smem + woff[n] + 4 * 448));
        WAIT_L(0);
        BAR;
#define K1_STEP(W, XC, XN, PRE, POST) { \
            const char* base = smem + ((i + 1) % K1_NB) * K1_STAGE; \
            PRE; \
            rdx(XN, i + 1 > 17 ? 17 : i + 1); \
            SB0; \
            _Pragma("unroll") for (int n = 0; n < (W == 0 || W == 3 ? 4 : 3); n++) { \
                if (W != 0) acc[0][n] = MFMA16(wf[n], XC[0], acc[0][n]); \
                _Pragma("unroll") for (int m = 1; m < 5; m++) acc[m][n] = MFMA16(wf[n], XC[m], acc[m][n]); \
                acs[n] = MFMA16(wf[n], ones, acs[n]); \
                SB0; \
                wf[n] = cat8(tr_read(base + woff[n]), tr_read(base + woff[n] + 4 * 448)); \
                SB0; \
            } \
            if (W == 0) { ssum[0] = MFMA16(XC[0], ones, ssum[0]); ssq[0] = MFMA16(XC[0], XC[0], ssq[0]); } \
            if (W == 1) { ssum[0] = MFMA16(XC[1], ones, ssum[0]); ssq[0] = MFMA16(XC[1], XC[1], ssq[0]); \
                          ssum[1] = MFMA16(XC[2], ones, ssum[1]); ssq[1] = MFMA16(XC[2], XC[2], ssq[1]); } \
            if (W == 2) { ssum[0] = MFMA16(XC[3], ones, ssum[0]); ssq[0] = MFMA16(XC[3], XC[3], ssq[0]); \
                          ssum[1] = MFMA16(XC[4], ones, ssum[1]); ssq[1] = MFMA16(XC[4], XC[4], ssq[1]); } \
            SB0; \
            POST; \
            BAR; }
#define K1_LOOPS(W) { \
        _Pragma("unroll 1") for (int i = 0; i < 6; i += 2) { \
            K1_STEP(W, xa, xb, xissue(i + 3), WAIT_V(6)); \
            i++; K1_STEP(W, xb, xa, xissue(i + 3), WAIT_V(6)); i--; \
        } \
        _Pragma("unroll 1") for (int i = 6; i < 18; i += 2) { \
            K1_STEP(W, xa, xb, , WAIT_V(0)); \
            i++; K1_STEP(W, xb, xa, , WAIT_V(0)); i--; \
        } }
        if (wave == 0) K1_LOOPS(0) else if (wave == 1) K1_LOOPS(1) else if (wave == 2) K1_LOOPS(2) else K1_LOOPS(3)
        if (wave < 3 && q == g) {
#pragma unroll
            for (int t = 0; t < 2; t++) {
                if (t == 0 || wave > 0) {
                    const float mean = ssum[t][p] * (1.f / 576.f);
                    const float var = fmaxf(ssq[t][p] * (1.f / 576.f) - mean * mean, 0.f);
                    const int r = (wave == 0 ? 0 : 2 * wave - 1 + t) * 16 + (lane & 15);
                    mus[r] = mean; mus[80 + r] = rsqrtf(var + 1e-5f);
                }
            }
        }
        const u4v* pcw = (const u4v*)(w1 + (size_t)(rthird * 192 + wave * 48 + (lane >> 3)) * F_ + cchunk * 64) + (lane & 7);
#pragma unroll
        for (int j = 0; j < 6; j++) cw[j] = pcw[(size_t)j * 8 * F_ / 8];
    }
    __syncthreads();
    constexpr int RSB = 456;
    if (wave < 4) {
        const int nt0 = wave == 0 ? 0 : 1 + 3 * wave;
        const int nn = (wave == 0 || wave == 3) ? 4 : 3;
        int lco[4][2];
#pragma unroll
        for (int n = 0; n < 4; n++)
#pragma unroll
            for (int h2 = 0; h2 < 2; h2++) {
                const int c = (nt0 + n) * 16 + 4 * g + 2 * h2;
                const int hc = c - 144, hd = hc / 18;
                lco[n][h2] = (n < nn && c < 216) ? (c < 144 ? c : 144 + hd * 20 + (hc - hd * 18)) * 2 : -1;
            }
        float mean[5], rs[5];
#pragma unroll
        for (int m = 0; m < 5; m++) { mean[m] = mus[m * 16 + (lane & 15)]; rs[m] = mus[80 + m * 16 + (lane & 15)]; }
        const int dump = K1_RED + 1024 + lane * 4;
#pragma unroll
        for (int m = 0; m < 5; m++) {
            const int rowo = (m * 16 + (lane & 15)) * RSB;
            const bool mok = m > 0 || wave > 0;
#pragma unroll
            for (int n = 0; n < 4; n++) {
                const f4v v = (acc[m][n] - acs[n] * mean[m]) * rs[m];
                *(unsigned*)(smem + (mok && lco[n][0] >= 0 ? rowo + lco[n][0] : dump)) = pack_bf2(v[0], v[1]);
                *(unsigned*)(smem + (mok && lco[n][1] >= 0 ? rowo + lco[n][1] : dump)) = pack_bf2(v[2], v[3]);
            }
        }
    }
    __syncthreads();
    if (wave == 4 && blockIdx.x < 144) {
        WAIT_V(0);
        const char* sb = smem + K1_DUMMY + (8 * g + q) * 144 + 8 * p;
        char* dst = wof + (size_t)blockIdx.x * 5 * 1024 + lane * 16;
#pragma unroll
        for (int nt = 0; nt < 5; nt++)
            *(s8v*)(dst + nt * 1024) = cat8(tr_read(sb + nt * 32), tr_read(sb + 4 * 144 + nt * 32));
    }
    {
        const int head = wave >> 1, li = lane & 15;
        const unsigned qmask0 = g < 3 ? 0xffffffffu : 0u, qmask1 = g < 2 ? 0xffffffffu : 0u;
        const int t0 = (wave & 1) * 2;
        u4v qf[2], kf[3];
        s8v vf0[2], vf1[2];
#pragma unroll
        for (int uu = 0; uu < 2; uu++) {
            const char* qp = smem + (16 + 16 * (t0 + uu) + li) * RSB + (head * 18 + 8 * g) * 2;
#pragma unroll
            for (int i = 0; i < 4; i++) qf[uu][i] = *(const unsigned*)(qp + 4 * i) & (i == 0 ? qmask0 : qmask1);
        }
#pragma unroll
        for (int kt = 0; kt < 3; kt++) {
            const char* kp = smem + (16 * (t0 + kt) + li) * RSB + (72 + head * 18 + 8 * g) * 2;
#pragma unroll
            for (int i = 0; i < 4; i++) kf[kt][i] = *(const unsigned*)(kp + 4 * i);
        }
#pragma unroll
        for (int uu = 0; uu < 2; uu++) {
            const char* vp = smem + (16 * (t0 + uu) + 4 * g + q) * RSB + (144 + head * 20 + 4 * p) * 2;
            vf0[uu] = cat8(tr_read(vp), tr_read(vp + 16 * RSB));
            vf1[uu] = cat8(tr_read(vp + 32), tr_read(vp + 16 * RSB + 32));
        }
        const f4v z4 = f4v{0.f, 0.f, 0.f, 0.f};
        f4v sv[2][2];
#pragma unroll
        for (int uu = 0; uu < 2; uu++)
#pragma unroll
            for (int tau = 0; tau < 2; tau++)
                sv[uu][tau] = MFMA16(__builtin_bit_cast(s8v, kf[uu + tau]), __builtin_bit_cast(s8v, qf[uu]), z4);
        bool ok[2][8]; float mx[2], den[2]; u4v pb[2];
#pragma unroll
        for (int uu = 0; uu < 2; uu++) {
            const bool tile0_ok = !(s0 == 0 && t0 + uu == 0);
            mx[uu] = -3.0e38f;
#pragma unroll
            for (int j = 0; j < 8; j++) {
                const int dl = 16 * (j >> 2) + 4 * g + (j & 3) - li;
                ok[uu][j] = !(dl & 1) && dl >= 2 && dl <= 16 && (j >= 4 || tile0_ok);
                mx[uu] = fmaxf(mx[uu], ok[uu][j] ? sv[uu][j >> 2][j & 3] : -3.0e38f);
            }
        }
#pragma unroll
        for (int uu = 0; uu < 2; uu++) mx[uu] = xmax4(mx[uu]);
#pragma unroll
        for (int uu = 0; uu < 2; uu++) {
            float a[8];
#pragma unroll
            for (int j = 0; j < 8; j++) {
                const float e = __builtin_amdgcn_exp2f((sv[uu][j >> 2][j & 3] - mx[uu]) * (0.23570226039551584f * 1.4426950408889634f));
                a[j] = ok[uu][j] ? e : 0.f;
            }
            pb[uu] = u4v{pack_bf2(a[0], a[1]), pack_bf2(a[2], a[3]), pack_bf2(a[4], a[5]), pack_bf2(a[6], a[7])};
            den[uu] = 0.f;
#pragma unroll
            for (int j = 0; j < 4; j++) den[uu] += bf_lo(pb[uu][j]) + bf_hi(pb[uu][j]);
        }
#pragma unroll
        for (int uu = 0; uu < 2; uu++) den[uu] = 1.f / xsum4(den[uu]);
#pragma unroll
        for (int uu = 0; uu < 2; uu++) {
            const f4v o0 = MFMA16(vf0[uu], __builtin_bit_cast(s8v, pb[uu]), z4) * den[uu];
            const f4v o1 = MFMA16(vf1[uu], __builtin_bit_cast(s8v, pb[uu]), z4) * den[uu];
            char* ob = of + (size_t)((s0 >> 4) + t0 + uu) * (18 * 1024) + li * 16;
            const int c0 = hg * 72 + head * 18 + 4 * g, c1 = c0 + 2, c2 = hg * 72 + head * 18 + 16;
            *(unsigned*)(ob + (c0 >> 5) * 1024 + ((c0 & 31) >> 3) * 256 + (c0 & 7) * 2) = pack_bf2(o0[0], o0[1]);
            *(unsigned*)(ob + (c1 >> 5) * 1024 + ((c1 & 31) >> 3) * 256 + (c1 & 7) * 2) = pack_bf2(o0[2], o0[3]);
            if (g == 0) *(unsigned*)(ob + (c2 >> 5) * 1024 + ((c2 & 31) >> 3) * 256 + (c2 & 7) * 2) = pack_bf2(o1[0], o1[1]);
        }
    }
    if (wave < 4) {
        float a[8];
#pragma unroll
        for (int j = 0; j < 8; j++) a[j] = 0.f;
#pragma unroll
        for (int i = 0; i < 6; i++)
#pragma unroll
            for (int j = 0; j < 4; j++) { a[2 * j] += bf_lo(cw[i][j]); a[2 * j + 1] += bf_hi(cw[i][j]); }
#pragma unroll
        for (int j = 0; j < 8; j++) {
            float t = a[j];
            t += __shfl_xor(t, 8); t += __shfl_xor(t, 16); t += __shfl_xor(t, 32);
            a[j] = t;
        }
        if (lane < 8 && blockIdx.x < 216) {
            float* rq = cs1 + (rthird * 4 + wave) * F_ + cchunk * 64 + lane * 8;
            *(f4v*)rq = f4v{a[0], a[1], a[2], a[3]};
            *(f4v*)(rq + 4) = f4v{a[4], a[5], a[6], a[7]};
        }
    }
}

constexpr int K2_LDS = 8 * 12 * 1024;

__global__ __launch_bounds__(512, 2) void k2_wo(const char* __restrict__ of, const char* __restrict__ wof,
                                                const bf16_t* __restrict__ x, bf16_t* __restrict__ h,
                                                char* __restrict__ hT, f2v* __restrict__ stats) {
    extern __shared__ __attribute__((aligned(16))) char smem[];
    const int tid = threadIdx.x, lane = tid & 63;
    const int wave = __builtin_amdgcn_readfirstlane(tid >> 6);
    const int rt = (blockIdx.x & 7) * 4 + ((blockIdx.x >> 3) & 3), ct = blockIdx.x >> 5;
    const int r0 = rt * 64, c0 = ct * 72;
    const int g = lane >> 4;
    const int kq = wave & 3, nh = wave >> 2, nb = nh * 3;
    const char* pa = of + ((size_t)rt * 4 * 18 + kq) * 1024 + lane * 16;
    const char* pw = wof + (((size_t)ct * 18 + kq) * 5 + nb) * 1024 + lane * 16;
    s8v fa[5][4], fw[5][3];
#pragma unroll
    for (int j = 0; j < 5; j++) {
        if (j < 4 || kq < 2) {
#pragma unroll
            for (int m = 0; m < 4; m++) fa[j][m] = *(const s8v*)(pa + m * (18 * 1024) + j * 4096);
#pragma unroll
            for (int n = 0; n < 3; n++) if (n < 2 || nh == 0) fw[j][n] = *(const s8v*)(pw + n * 1024 + j * 20480);
        }
    }
    const int row = r0 + (wave & 3) * 16 + (lane & 15);
    u2v xx[3];
#pragma unroll
    for (int n = 0; n < 3; n++) {
        int col = (nb + n) * 16 + 4 * g; if (col > 68) col = 68;
        xx[n] = *(const u2v*)(x + (size_t)row * D_ + c0 + col);
    }
    f4v acc[4][3];
#pragma unroll
    for (int m = 0; m < 4; m++)
#pragma unroll
        for (int n = 0; n < 3; n++) acc[m][n] = f4v{0, 0, 0, 0};
#pragma unroll
    for (int j = 0; j < 5; j++) {
        if (j < 4 || kq < 2) {
#pragma unroll
            for (int n = 0; n < 3; n++)
                if (n < 2 || nh == 0) {
#pragma unroll
                    for (int m = 0; m < 4; m++) acc[m][n] = MFMA16(fw[j][n], fa[j][m], acc[m][n]);
                }
        }
    }
    f4v* ex = (f4v*)smem + wave * (12 * 64) + lane;
#pragma unroll
    for (int m = 0; m < 4; m++)
#pragma unroll
        for (int n = 0; n < 3; n++) if (n < 2 || nh == 0) ex[(m * 3 + n) * 64] = acc[m][n];
    __syncthreads();
    float hv[3][4];
    float sum = 0.f;
#pragma unroll
    for (int n = 0; n < 3; n++) {
        const f4v* e = (const f4v*)smem + (nh * 4 * 12 + (wave & 3) * 3 + n) * 64 + lane;
        const int col = (nb + n) * 16 + 4 * g;
        if (col < 72) {
            f4v a = (e[0] + e[768]) + (e[2 * 768] + e[3 * 768]);
            unsigned p0 = pack_bf2(bf_lo(xx[n][0]) + a[0], bf_hi(xx[n][0]) + a[1]);
            unsigned p1 = pack_bf2(bf_lo(xx[n][1]) + a[2], bf_hi(xx[n][1]) + a[3]);
            *(u2v*)(h + (size_t)row * D_ + c0 + col) = u2v{p0, p1};
            {
                int cc = c0 + col;
                *(u2v*)(hT + (size_t)((row >> 4) * 18 + (cc >> 5)) * 1024 + (((cc & 31) >> 3) * 16 + (row & 15)) * 16 + (cc & 7) * 2) = u2v{p0, p1};
            }
            hv[n][0] = bf_lo(p0); hv[n][1] = bf_hi(p0); hv[n][2] = bf_lo(p1); hv[n][3] = bf_hi(p1);
            sum += hv[n][0] + hv[n][1] + hv[n][2] + hv[n][3];
        } else {
            hv[n][0] = hv[n][1] = hv[n][2] = hv[n][3] = 0.f;
        }
    }
    sum = xsum4(sum);
    const float mean = sum * (nh == 0 ? 1.f / 48.f : 1.f / 24.f);
    float m2 = 0.f;
#pragma unroll
    for (int n = 0; n < 3; n++) {
        const int col = (nb + n) * 16 + 4 * g;
        if (col < 72) {
#pragma unroll
            for (int j = 0; j < 4; j++) { float d = hv[n][j] - mean; m2 += d * d; }
        }
    }
    m2 = xsum4(m2);
    if (g == 0) stats[((size_t)row * 8 + ct) * 2 + nh] = f2v{sum, m2};
}

constexpr int W2F_TAIL = 8 * 144 * 4 * 1024;
constexpr int K3_STAGE = 8192 + 18432;
constexpr int K3_NB = 5;
constexpr int K3_PAR = K3_NB * K3_STAGE;
constexpr int K3_LDS = K3_PAR + 1024 + 2304;

__device__ __forceinline__ float gelu_tanh(float v) {
    float t = v * v;
    float z = v * (-2.302208198f * 0.044715f * t - 2.302208198f);
    float e = __builtin_amdgcn_exp2f(z);
    return v * __builtin_amdgcn_rcpf(1.f + e);
}

__device__ __forceinline__ f2v gelu_tanh2(f2v v) {
    f2v t = v * v;
    f2v z = v * (t * (-2.302208198f * 0.044715f) + (-2.302208198f));
    f2v d = f2v{__builtin_amdgcn_exp2f(z[0]), __builtin_amdgcn_exp2f(z[1])} + 1.f;
    return v * f2v{__builtin_amdgcn_rcpf(d[0]), __builtin_amdgcn_rcpf(d[1])};
}

__global__ __launch_bounds__(512, 2) void k3_up(const char* __restrict__ hT, const bf16_t* __restrict__ w1,
                                                const bf16_t* __restrict__ b1, const float* __restrict__ cs1,
                                                const f2v* __restrict__ stats, const bf16_t* __restrict__ w2,
                                                char* __restrict__ h1f, char* __restrict__ w2f) {
    extern __shared__ __attribute__((aligned(16))) char smem[];
    const int tid = threadIdx.x, lane = tid & 63;
    const int wave = __builtin_amdgcn_readfirstlane(tid >> 6);
    const int xcd = blockIdx.x & 7, idx = blockIdx.x >> 3;
    const int rt = (xcd & 1) * 8 + (idx & 7), ct = (xcd >> 1) * 4 + (idx >> 3);
    const int r0 = rt * 128, c0 = ct * 288;
    const unsigned lbase = lds_addr(smem);
    const int g = lane >> 4, q = (lane & 15) >> 2, p = lane & 3;
    const int xr = lane & 15;
    f2v* mu = (f2v*)(smem + K3_PAR);
    float* csl = (float*)(smem + K3_PAR + 1024);
    float* bbl = csl + 288;
    f4v acc[4][9];
    const int wm = (wave >> 1) & 1, wn = wave & 1;
    if (wave >= 4) {
        const int lw = wave - 4;
        unsigned bsrc[5];
        const unsigned avo = lane * 16;
        const char* ag = hT + (size_t)(rt * 8 + lw * 2) * 18 * 1024;
#pragma unroll
        for (int i = 0; i < 5; i++) {
            int bi = lw * 5 + i; if (bi > 17) bi = 17;
            int off = bi * 1024 + lane * 16;
            int k = off / 576, r = off - k * 576;
            int nt = (r >> 5) ^ ((k >> 3) & 1);
            bsrc[i] = (unsigned)((k * F_ + c0 + nt * 16 + ((r >> 4) & 1) * 8) * 2);
        }
        auto issue = [&](int st) {
            int sc = st > 17 ? 17 : st;
            unsigned lb = lbase + (st % K3_NB) * K3_STAGE;
            const char* ga = ag + sc * 1024;
            const char* gb = (const char*)w1 + (size_t)sc * 32 * F_ * 2;
            glds16s(ga, avo, lb + (lw * 2) * 1024);
            glds16s(ga + 18 * 1024, avo, lb + (lw * 2 + 1) * 1024);
#pragma unroll
            for (int i = 0; i < 5; i++) glds16s(gb, bsrc[i], lb + 8192 + (lw * 5 + i > 17 ? 17 : lw * 5 + i) * 1024);
        };
#pragma unroll
        for (int st = 0; st < K3_NB - 1; st++) issue(st);
        WAIT_V(21);
        BAR;
        for (int i = -1; i < 14; i++) {
            if (i >= 0) issue(i + K3_NB - 1);
            WAIT_V(14);
            BAR;
        }
        WAIT_V(7); BAR;
        WAIT_V(0); BAR;
#pragma unroll
        for (int uu = 0; uu < 2; uu++) {
            if (uu == 0 || (lw == 0 && blockIdx.x < 128)) {
                const int u = uu * 1024 + lw * 256 + blockIdx.x;
                const int ctp = u / 144, kst = u - ctp * 144;
#pragma unroll
                for (int j = 0; j < 5; j++) {
                    int c = j * 64 + lane; if (c > 287) c = 287;
                    int row = c / 9, pc = c - row * 9;
                    glds16s(w2, (unsigned)(((kst * 32 + row) * D_ + ctp * 72 + pc * 8) * 2), lbase + 3 * K3_STAGE + (uu * 4 + lw) * 5120 + j * 1024);
                }
            }
        }
        BAR; BAR;
    } else {
        {
            float c0v = 0.f, b0v = bf2f(b1[c0 + tid]);
            float c1v = 0.f, b1v = 0.f;
            {
                float t0[12], t1[12];
                const int c1i = c0 + 256 + (tid & 31);
#pragma unroll
                for (int i = 0; i < 12; i++) { t0[i] = cs1[i * F_ + c0 + tid]; t1[i] = cs1[i * F_ + c1i]; }
                b1v = bf2f(b1[c1i]);
#pragma unroll
                for (int i = 0; i < 12; i++) { c0v += t0[i]; c1v += t1[i]; }
            }
            if (tid < 128) {
                const f4v* sp = (const f4v*)(stats + (size_t)(r0 + tid) * 16);
                f4v v[8];
#pragma unroll
                for (int i = 0; i < 8; i++) v[i] = sp[i];
                float tot = 0.f;
#pragma unroll
                for (int i = 0; i < 8; i++) tot += v[i][0] + v[i][2];
                float mean = tot * (1.f / 576.f);
                float M2 = 0.f;
#pragma unroll
                for (int i = 0; i < 8; i++) {
                    float d0 = v[i][0] * (1.f / 48.f) - mean, d1 = v[i][2] * (1.f / 24.f) - mean;
                    M2 += v[i][1] + v[i][3] + (48.f * d0 * d0 + 24.f * d1 * d1);
                }
                float rstd = rsqrtf(M2 * (1.f / 576.f) + 1e-5f);
                mu[tid] = f2v{mean * rstd, rstd};
            }
            csl[tid] = c0v; bbl[tid] = b0v;
            if (tid < 32) { csl[256 + tid] = c1v; bbl[256 + tid] = b1v; }
        }
#pragma unroll
        for (int m = 0; m < 4; m++)
#pragma unroll
            for (int n = 0; n < 9; n++) acc[m][n] = f4v{0, 0, 0, 0};
        const int dlt = (g & 1) ? 32 : 0;
        const int wb0 = 8192 + (8 * g + q) * 576 + wn * 9 * 32 + 8 * p;
        const int wbe = wb0 + ((wn & 1) ? -dlt : dlt);
        const int wbo = wb0 + ((wn & 1) ? dlt : -dlt);
        const int xoff = wm * 4096 + lane * 16;
        s8v xa[4], xb[4], wf[9];
        BAR;
        {
            const char* base = smem;
#pragma unroll
            for (int m = 0; m < 4; m++) xa[m] = *(const s8v*)(base + xoff + m * 1024);
#pragma unroll
            for (int n = 0; n < 9; n++) {
                const char* wb = base + ((n & 1) ? wbo : wbe) + n * 32;
                wf[n] = cat8(tr_read(wb), tr_read(wb + 4 * 576));
            }
        }
        WAIT_L(0);
        BAR;
#define K3_STEP(XC, XN) { \
            const char* base = smem + ((i + 1) % K3_NB) * K3_STAGE; \
            s4v wlo, whi; \
            _Pragma("unroll") for (int n = 0; n < 9; n++) { \
                const char* wbp = base + (((n - 1) & 1) ? wbo : wbe) + (n - 1) * 32; \
                acc[0][n] = MFMA16(wf[n], XC[0], acc[0][n]); SB0; \
                if (n > 0) { wlo = tr_read(wbp); SB0; } \
                acc[1][n] = MFMA16(wf[n], XC[1], acc[1][n]); SB0; \
                if (n > 0) { whi = tr_read(wbp + 4 * 576); wf[n - 1] = cat8(wlo, whi); SB0; } \
                acc[2][n] = MFMA16(wf[n], XC[2], acc[2][n]); SB0; \
                if (n < 4) { XN[n] = *(const s8v*)(base + xoff + n * 1024); SB0; } \
                acc[3][n] = MFMA16(wf[n], XC[3], acc[3][n]); SB0; \
            } \
            { const char* wbp = base + wbe + 8 * 32; wf[8] = cat8(tr_read(wbp), tr_read(wbp + 4 * 576)); } \
            BAR; }
#pragma unroll 1
        for (int i = 0; i < 18; i += 2) {
            K3_STEP(xa, xb);
            i++; K3_STEP(xb, xa); i--;
        }
    }
    __syncthreads();
    f4v* ex = (f4v*)smem + (wave & 3) * (18 * 64) + lane;
    if (wave < 4) {
#pragma unroll
        for (int m = 0; m < 2; m++)
#pragma unroll
            for (int n = 0; n < 9; n++) ex[(m * 9 + n) * 64] = acc[m + 2][n];
    }
    __syncthreads();
    if (wave >= 4) {
        __builtin_amdgcn_s_setprio(2);
#pragma unroll
        for (int m = 0; m < 2; m++)
#pragma unroll
            for (int n = 0; n < 9; n++) acc[m][n] = ex[(m * 9 + n) * 64];
    }
    {
        const int mb = (wave >> 2) * 2;
#pragma unroll
        for (int m = 0; m < 2; m++) {
            int rl = wm * 64 + (mb + m) * 16 + xr;
            f2v ms = mu[rl];
            char* ob = h1f + (size_t)((r0 + wm * 64 + (mb + m) * 16) >> 4) * (144 * 1024) + lane * 16;
            u2v pv[9];
#pragma unroll
            for (int n = 0; n < 9; n++) {
                f4v a = acc[m][n];
                int cl = wn * 144 + n * 16 + 4 * g;
                f4v cs = *(const f4v*)(csl + cl), bb = *(const f4v*)(bbl + cl);
                f2v t01 = f2v{bb[0], bb[1]} - f2v{cs[0], cs[1]} * ms[0];
                f2v t23 = f2v{bb[2], bb[3]} - f2v{cs[2], cs[3]} * ms[0];
                f2v v01 = gelu_tanh2(f2v{a[0], a[1]} * ms[1] + t01);
                f2v v23 = gelu_tanh2(f2v{a[2], a[3]} * ms[1] + t23);
                pv[n] = u2v{pack_bf2(v01[0], v01[1]), pack_bf2(v23[0], v23[1])};
            }
            if (m == 0) {
            } else {
            }
            char* oc = ob + (c0 >> 5) * 1024;
            if (wn == 0) {
#pragma unroll
                for (int j = 0; j < 4; j++) *(u4v*)(oc + j * 1024) = u4v{pv[2 * j][0], pv[2 * j][1], pv[2 * j + 1][0], pv[2 * j + 1][1]};
                *(u2v*)(oc + 4 * 1024) = pv[8];
            } else {
                *(u2v*)(oc + 4 * 1024 + 8) = pv[0];
#pragma unroll
                for (int j = 0; j < 4; j++) *(u4v*)(oc + (5 + j) * 1024) = u4v{pv[2 * j + 1][0], pv[2 * j + 1][1], pv[2 * j + 2][0], pv[2 * j + 2][1]};
            }
        }
    }
    if (wave >= 4) {
        WAIT_V(0);
#pragma unroll
        for (int uu = 0; uu < 2; uu++) {
            if (uu == 0 || (wave == 4 && blockIdx.x < 128)) {
                const int u = uu * 1024 + (wave - 4) * 256 + blockIdx.x;
                const char* sb = smem + 3 * K3_STAGE + (uu * 4 + wave - 4) * 5120 + (4 * g + q) * 144 + 8 * p;
                char* dst = w2f + (size_t)u * 4 * 1024 + lane * 16;
#pragma unroll
                for (int nt = 0; nt < 4; nt++)
                    *(s8v*)(dst + nt * 1024) = cat8(tr_read(sb + nt * 32), tr_read(sb + 16 * 144 + nt * 32));
                const int ctp = u / 144, kst = u - ctp * 144;
                const s8v t4 = cat8(tr_read(sb + 4 * 32), tr_read(sb + 16 * 144 + 4 * 32));
                if ((lane & 15) < 8)
                    *(s8v*)(w2f + W2F_TAIL + (size_t)((ctp * 8 + (kst & 7)) * 9 + (kst >> 4)) * 1024 + (g * 16 + ((kst >> 3) & 1) * 8 + (lane & 7)) * 16) = t4;
            }
        }
    }
}

constexpr int K4_LDS = 8 * 20 * 1024;
constexpr int K4_D = 3;

__global__ __launch_bounds__(512, 2) void k4_down(const char* __restrict__ h1f, const char* __restrict__ w2f,
                                                  const bf16_t* __restrict__ h, const bf16_t* __restrict__ b2,
                                                  bf16_t* __restrict__ out) {
    extern __shared__ __attribute__((aligned(16))) char smem[];
    const int tid = threadIdx.x, lane = tid & 63;
    const int wave = __builtin_amdgcn_readfirstlane(tid >> 6);
    const int xcd = blockIdx.x & 7, idx = blockIdx.x >> 3;
    const int rt = (xcd >> 1) * 8 + (idx & 7), ct = (xcd & 1) * 4 + (idx >> 3);
    const int g = lane >> 4;
    const char* pa = h1f + ((size_t)rt * 4 * 144 + wave) * 1024 + lane * 16;
    const char* pw = w2f + ((size_t)ct * 144 + wave) * 4 * 1024 + lane * 16;
    const char* pt = w2f + W2F_TAIL + (size_t)(ct * 8 + wave) * 9 * 1024 + lane * 16;
    f4v acc[4][4], accA[4], accB[4];
#pragma unroll
    for (int m = 0; m < 4; m++) {
#pragma unroll
        for (int n = 0; n < 4; n++) acc[m][n] = f4v{0, 0, 0, 0};
        accA[m] = f4v{0, 0, 0, 0}; accB[m] = f4v{0, 0, 0, 0};
    }
    s8v fa[K4_D][4], fw[K4_D][4], ft[2];
#pragma unroll
    for (int j = 0; j < K4_D; j++) {
#pragma unroll
        for (int m = 0; m < 4; m++) fa[j][m] = *(const s8v*)(pa + m * (144 * 1024) + j * 8192);
#pragma unroll
        for (int n = 0; n < 4; n++) fw[j][n] = *(const s8v*)(pw + n * 1024 + j * 32768);
        if ((j & 1) == 0) ft[(j >> 1) & 1] = *(const s8v*)(pt + (j >> 1) * 1024);
        SB0;
    }
#pragma unroll
    for (int j = 0; j < 18; j++) {
        const int sl = j % K4_D;
        SB0;
#pragma unroll
        for (int n = 0; n < 4; n++)
#pragma unroll
            for (int m = 0; m < 4; m++) acc[m][n] = MFMA16(fw[sl][n], fa[sl][m], acc[m][n]);
#pragma unroll
        for (int m = 0; m < 4; m++) {
            if ((j & 1) == 0) accA[m] = MFMA16(ft[(j >> 1) & 1], fa[sl][m], accA[m]);
            else accB[m] = MFMA16(ft[(j >> 1) & 1], fa[sl][m], accB[m]);
        }
        SB0;
        if (j + K4_D < 18) {
            const int jn = j + K4_D;
#pragma unroll
            for (int m = 0; m < 4; m++) fa[sl][m] = *(const s8v*)(pa + m * (144 * 1024) + jn * 8192);
#pragma unroll
            for (int n = 0; n < 4; n++) fw[sl][n] = *(const s8v*)(pw + n * 1024 + jn * 32768);
            if ((jn & 1) == 0) ft[(jn >> 1) & 1] = *(const s8v*)(pt + (jn >> 1) * 1024);
        }
        SB0;
    }
    f4v acc4[4];
#pragma unroll
    for (int m = 0; m < 4; m++)
#pragma unroll
        for (int r = 0; r < 4; r++) {
            float t = accB[m][r], u = accB[m][r];
            asm volatile("s_nop 1\n\tv_permlane32_swap_b32 %0, %1" : "+v"(t), "+v"(u));
            acc4[m][r] = accA[m][r] + u;
        }
    u2v hh[3], bb[3];
#pragma unroll
    for (int j = 0; j < 3; j++) {
        int t = wave + 8 * j; if (t > 19) t = 19;
        const int m = t / 5, n = t - m * 5;
        const int row = rt * 64 + m * 16 + (lane & 15);
        int cl = n * 16 + 4 * g; if (cl > 68) cl = 68;
        hh[j] = *(const u2v*)(h + (size_t)row * D_ + ct * 72 + cl);
        bb[j] = *(const u2v*)(b2 + ct * 72 + cl);
    }
    f4v* ex = (f4v*)smem + wave * (20 * 64) + lane;
#pragma unroll
    for (int m = 0; m < 4; m++) {
#pragma unroll
        for (int n = 0; n < 4; n++) ex[(m * 5 + n) * 64] = acc[m][n];
        ex[(m * 5 + 4) * 64] = acc4[m];
    }
    __syncthreads();
#pragma unroll
    for (int j = 0; j < 3; j++) {
        const int t = wave + 8 * j;
        if (t < 20) {
            const int m = t / 5, n = t - m * 5;
            const f4v* e = (const f4v*)smem + t * 64 + lane;
            f4v a = ((e[0] + e[1280]) + (e[2 * 1280] + e[3 * 1280])) + ((e[4 * 1280] + e[5 * 1280]) + (e[6 * 1280] + e[7 * 1280]));
            const int row = rt * 64 + m * 16 + (lane & 15);
            const int cl = n * 16 + 4 * g;
            float v0 = bf_lo(hh[j][0]) + (a[0] + bf_lo(bb[j][0]));
            float v1 = bf_hi(hh[j][0]) + (a[1] + bf_hi(bb[j][0]));
            float v2 = bf_lo(hh[j][1]) + (a[2] + bf_lo(bb[j][1]));
            float v3 = bf_hi(hh[j][1]) + (a[3] + bf_hi(bb[j][1]));
            if (cl < 72) *(u2v*)(out + (size_t)row * D_ + ct * 72 + cl) = u2v{pack_bf2(v0, v1), pack_bf2(v2, v3)};
        }
    }
}

extern "C" void kernel_launch(void* const* d_in, const int* in_sizes, int n_in,
                              void* d_out, int out_size, void* d_ws, size_t ws_size,
                              hipStream_t stream) {
    const bf16_t* x = (const bf16_t*)d_in[0];
    const bf16_t* wqkv = (const bf16_t*)d_in[1];
    const bf16_t* wo = (const bf16_t*)d_in[2];
    const bf16_t* w1 = (const bf16_t*)d_in[4];
    const bf16_t* b1 = (const bf16_t*)d_in[5];
    const bf16_t* w2 = (const bf16_t*)d_in[6];
    const bf16_t* b2 = (const bf16_t*)d_in[7];
    bf16_t* out = (bf16_t*)d_out;
    char* ws = (char*)d_ws;
    char* of = ws;
    char* wof = ws + (3 << 20);
    bf16_t* h = (bf16_t*)(ws + (4 << 20));
    char* hT = ws + (12 << 20);
    f2v* stats = (f2v*)(ws + (8 << 20));
    float* cs1 = (float*)(ws + (9 << 20));
    char* h1 = (char*)(ws + (16 << 20));
    char* w2f = ws + (48 << 20);

    hipFuncSetAttribute((const void*)k1_qkv_attn, hipFuncAttributeMaxDynamicSharedMemorySize, K1_LDS);
    hipFuncSetAttribute((const void*)k2_wo, hipFuncAttributeMaxDynamicSharedMemorySize, K2_LDS);
    hipFuncSetAttribute((const void*)k3_up, hipFuncAttributeMaxDynamicSharedMemorySize, K3_LDS);
    hipFuncSetAttribute((const void*)k4_down, hipFuncAttributeMaxDynamicSharedMemorySize, K4_LDS);

    k1_qkv_attn<<<256, 512, K1_LDS, stream>>>(x, wqkv, w1, wo, of, wof, cs1);
    k2_wo<<<256, 512, K2_LDS, stream>>>(of, wof, x, h, hT, stats);
    k3_up<<<256, 512, K3_LDS, stream>>>(hT, w1, b1, cs1, stats, w2, h1, w2f);
    k4_down<<<256, 512, K4_LDS, stream>>>(h1, w2f, h, b2, out);
}
```

```cpp
#include <hip/hip_runtime.h>
#include <hip/hip_bf16.h>

typedef __attribute__((ext_vector_type(8))) short s8v;
typedef __attribute__((ext_vector_type(4))) short s4v;
typedef __attribute__((ext_vector_type(4))) short v4i16_t;
typedef __attribute__((ext_vector_type(4))) float f4v;
typedef __attribute__((ext_vector_type(2))) float f2v;
typedef __attribute__((ext_vector_type(4))) unsigned u4v;
typedef __attribute__((ext_vector_type(2))) unsigned u2v;
typedef unsigned short bf16_t;

constexpr int S_ = 2048, D_ = 576, NQKV = 1728, F_ = 4608;

__device__ __forceinline__ unsigned lds_addr(const void* l) {
    return (unsigned)(size_t)(__attribute__((address_space(3))) const char*)l;
}
__device__ __forceinline__ void glds16(const void* g, unsigned dst) {
    unsigned keep;
    asm volatile("s_mov_b32 %0, m0\n\ts_mov_b32 m0, %2\n\ts_nop 0\n\tglobal_load_lds_dwordx4 %1, off\n\ts_mov_b32 m0, %0"
                 : "=&s"(keep) : "v"(g), "s"(dst) : "memory");
}
__device__ __forceinline__ void glds16s(const void* sbase, unsigned voff, unsigned dst) {
    unsigned keep;
    asm volatile("s_mov_b32 %0, m0\n\ts_mov_b32 m0, %3\n\ts_nop 0\n\tglobal_load_lds_dwordx4 %1, %2\n\ts_mov_b32 m0, %0"
                 : "=&s"(keep) : "v"(voff), "s"(sbase), "s"(dst) : "memory");
}
__device__ __forceinline__ s4v tr_read(const char* p) {
    return __builtin_bit_cast(s4v, __builtin_amdgcn_ds_read_tr16_b64_v4i16((__attribute__((address_space(3))) v4i16_t*)p));
}
__device__ __forceinline__ s8v cat8(s4v a, s4v b) {
    return __builtin_shufflevector(a, b, 0, 1, 2, 3, 4, 5, 6, 7);
}
__device__ __forceinline__ float bf_lo(unsigned u) { return __builtin_bit_cast(float, u << 16); }
__device__ __forceinline__ float bf_hi(unsigned u) { return __builtin_bit_cast(float, u & 0xffff0000u); }
__device__ __forceinline__ float bf2f(bf16_t v) { return __builtin_bit_cast(float, (unsigned)v << 16); }
__device__ __forceinline__ unsigned pack_bf2(float a, float b) {
    unsigned r;
    asm("v_cvt_pk_bf16_f32 %0, %1, %2" : "=v"(r) : "v"(a), "v"(b));
    return r;
}
#define MFMA16(a, b, c) __builtin_amdgcn_mfma_f32_16x16x32_bf16(a, b, c, 0, 0, 0)
#define WAIT_V(n) asm volatile("s_waitcnt vmcnt(" #n ")" ::: "memory")
#define WAIT_L(n) asm volatile("s_waitcnt lgkmcnt(" #n ")" ::: "memory")

__device__ __forceinline__ float xmax4(float v) {
    float a = v, b = v;
    asm volatile("s_nop 1\n\tv_permlane16_swap_b32 %0, %1" : "+v"(a), "+v"(b));
    a = fmaxf(a, b); b = a;
    asm volatile("s_nop 1\n\tv_permlane32_swap_b32 %0, %1" : "+v"(a), "+v"(b));
    return fmaxf(a, b);
}
__device__ __forceinline__ float xsum4(float v) {
    float a = v, b = v;
    asm volatile("s_nop 1\n\tv_permlane16_swap_b32 %0, %1" : "+v"(a), "+v"(b));
    a = a + b; b = a;
    asm volatile("s_nop 1\n\tv_permlane32_swap_b32 %0, %1" : "+v"(a), "+v"(b));
    return a + b;
}
#define SB0 __builtin_amdgcn_sched_barrier(0)
#define BAR __builtin_amdgcn_s_barrier()
constexpr int K1_XN_BYTES = 9 * 80 * 128;
constexpr int K1_STAGE = 14336;
constexpr int K1_NB = 4;
constexpr int K1_DUMMY = K1_XN_BYTES + K1_NB * K1_STAGE;
constexpr int K1_RED = K1_DUMMY + 5120;
constexpr int K1_LDS = K1_RED + 2048;

__global__ __launch_bounds__(512, 2) void k1_qkv_attn(const bf16_t* __restrict__ x, const bf16_t* __restrict__ wqkv,
                                                      const bf16_t* __restrict__ w1, const bf16_t* __restrict__ wo,
                                                      char* __restrict__ of, char* __restrict__ wof,
                                                      float* __restrict__ cs1) {
    extern __shared__ __attribute__((aligned(16))) char smem[];
    const int tid = threadIdx.x, lane = tid & 63;
    const int wave = __builtin_amdgcn_readfirstlane(tid >> 6);
    const int xcd = blockIdx.x & 7, idx = blockIdx.x >> 3;
    const int hg = (xcd & 1) * 4 + (idx >> 3), rt = (xcd >> 1) * 8 + (idx & 7);
    const int s0 = rt * 64;
    const unsigned lring = lds_addr(smem) + K1_XN_BYTES;
    const int g = lane >> 4, q = (lane & 15) >> 2, p = lane & 3;

    const int lw = wave - 4;
    unsigned bsrc[4];
    const int pc0 = lw < 2 ? lw * 4 : 2 + lw * 3;
    const int rot = ((idx & 7) + 2 * (idx >> 3)) % 9;
    auto rblk = [&](int b) { int t = b + rot; return t >= 9 ? t - 9 : t; };
    auto issue = [&](int st) {
        int sc = st > 17 ? 17 : st;
        sc = rblk(sc >> 1) * 2 + (sc & 1);
        unsigned lb = lring + (st % K1_NB) * K1_STAGE;
        const char* gb = (const char*)wqkv + (size_t)sc * 32 * NQKV * 2;
#pragma unroll
        for (int i = 0; i < 3; i++) glds16s(gb, bsrc[i], lb + (pc0 + i) * 1024);
        if (lw < 2) glds16s(gb, bsrc[3], lb + (pc0 + 3) * 1024);
    };
    const unsigned dz = lds_addr(smem) + K1_DUMMY + (wave & 3) * 1024;
    float* mus = (float*)(smem + K1_RED);
    unsigned xo[3];
    auto xissue = [&](int b0) {
        const int b = rblk(b0);
#pragma unroll
        for (int j = 0; j < 3; j++)
            glds16s((const char*)x + b * 128, xo[j], lds_addr(smem) + b * (80 * 128) + (j == 2 && wave >= 2 ? wave + 4 : wave + 4 * j) * 1024);
    };
    if (wave >= 4) {
#pragma unroll
        for (int i = 0; i < 4; i++) {
            int bi = pc0 + i; if (bi > 13) bi = 13;
            int off = bi * 1024 + lane * 16;
            int k = off / 448, r = off - k * 448;
            int nt = (r >> 5) ^ ((k >> 3) & 1);
            int pc = nt * 2 + ((r >> 4) & 1); if (pc > 26) pc = 26;
            int sec = pc / 9, wi = pc - sec * 9;
            bsrc[i] = (unsigned)((k * NQKV + sec * 576 + hg * 72 + wi * 8) * 2);
        }
#pragma unroll
        for (int st = 0; st < K1_NB - 1; st++) issue(st);
    } else {
#pragma unroll
        for (int j = 0; j < 3; j++) {
            int gq = wave + 4 * j; if (gq > 9) gq = wave + 4;
            int row = gq * 8 + (lane >> 3);
            int s = s0 - 16 + row; if (s < 0) s = 0;
            xo[j] = (unsigned)(s * (D_ * 2) + (((lane & 7) ^ (lane >> 3)) << 4));
        }
#pragma unroll
        for (int b = 0; b < 3; b++) xissue(b);
    }

    f4v acc[5][4], acs[4], ssum[2], ssq[2];
    u4v cw[6];
    const int cu = blockIdx.x < 216 ? blockIdx.x : blockIdx.x - 216;
    const int cchunk = cu % 72, rthird = cu / 72;
    if (wave >= 4) {
        if (lw < 2) WAIT_V(8); else WAIT_V(6);
        __syncthreads();
        if (lw < 2) WAIT_V(4); else WAIT_V(3);
        BAR;
        for (int i = 0; i < 15; i++) {
            issue(i + K1_NB - 1);
            if (lw < 2) WAIT_V(4); else WAIT_V(3);
            BAR;
        }
        WAIT_V(0); BAR;
        BAR; BAR;
        {
            if (wave == 4 && blockIdx.x < 144) {
                const int ctp = blockIdx.x / 18, kst = blockIdx.x - ctp * 18;
#pragma unroll
                for (int j = 0; j < 5; j++) {
                    int c = j * 64 + lane; if (c > 287) c = 287;
                    int row = c / 9, pc = c - row * 9;
                    glds16s(wo, (unsigned)(((kst * 32 + row) * D_ + ctp * 72 + pc * 8) * 2), lds_addr(smem) + K1_DUMMY + j * 1024);
                }
            }
        }
    } else {
        const int nt0 = wave == 0 ? 0 : 1 + 3 * wave;
        const int nn = (wave == 0 || wave == 3) ? 4 : 3;
#pragma unroll
        for (int m = 0; m < 5; m++)
#pragma unroll
            for (int n = 0; n < 4; n++) acc[m][n] = f4v{0, 0, 0, 0};
#pragma unroll
        for (int n = 0; n < 4; n++) acs[n] = f4v{0, 0, 0, 0};
        ssum[0] = ssum[1] = ssq[0] = ssq[1] = f4v{0, 0, 0, 0};
        const s8v ones = s8v{0x3F80, 0x3F80, 0x3F80, 0x3F80, 0x3F80, 0x3F80, 0x3F80, 0x3F80};
        const int xr = lane & 15;
        int woff[4];
#pragma unroll
        for (int n = 0; n < 4; n++) woff[n] = K1_XN_BYTES + (8 * g + q) * 448 + (((nt0 + n) ^ (g & 1)) << 5) + 8 * p;
        s8v xa[5], xb[5], wf[4];
        auto rdx = [&](s8v* X, int st) {
            const char* xbp = smem + rblk(st >> 1) * (80 * 128) + xr * 128 + ((((st & 1) * 4 + g) ^ (xr & 7)) << 4);
#pragma unroll
            for (int m = 0; m < 5; m++) X[m] = *(const s8v*)(xbp + m * 2048);
        };
        WAIT_V(6);
        __syncthreads();
        rdx(xa, 0);
#pragma unroll
        for (int n = 0; n < 4; n++) wf[n] = cat8(tr_read(smem + woff[n]), tr_read(smem + woff[n] + 4 * 448));
        WAIT_L(0);
        BAR;
#define K1_STEP(W, XC, XN, PRE, POST) { \
            const char* base = smem + ((i + 1) % K1_NB) * K1_STAGE; \
            PRE; \
            rdx(XN, i + 1 > 17 ? 17 : i + 1); \
            SB0; \
            _Pragma("unroll") for (int n = 0; n < (W == 0 || W == 3 ? 4 : 3); n++) { \
                if (W != 0) acc[0][n] = MFMA16(wf[n], XC[0], acc[0][n]); \
                _Pragma("unroll") for (int m = 1; m < 5; m++) acc[m][n] = MFMA16(wf[n], XC[m], acc[m][n]); \
                acs[n] = MFMA16(wf[n], ones, acs[n]); \
                SB0; \
                wf[n] = cat8(tr_read(base + woff[n]), tr_read(base + woff[n] + 4 * 448)); \
                SB0; \
            } \
            if (W == 0) { ssum[0] = MFMA16(XC[0], ones, ssum[0]); ssq[0] = MFMA16(XC[0], XC[0], ssq[0]); } \
            if (W == 1) { ssum[0] = MFMA16(XC[1], ones, ssum[0]); ssq[0] = MFMA16(XC[1], XC[1], ssq[0]); \
                          ssum[1] = MFMA16(XC[2], ones, ssum[1]); ssq[1] = MFMA16(XC[2], XC[2], ssq[1]); } \
            if (W == 2) { ssum[0] = MFMA16(XC[3], ones, ssum[0]); ssq[0] = MFMA16(XC[3], XC[3], ssq[0]); \
                          ssum[1] = MFMA16(XC[4], ones, ssum[1]); ssq[1] = MFMA16(XC[4], XC[4], ssq[1]); } \
            SB0; \
            POST; \
            BAR; }
#define K1_LOOPS(W) { \
        _Pragma("unroll 1") for (int i = 0; i < 6; i += 2) { \
            K1_STEP(W, xa, xb, xissue(i + 3), WAIT_V(6)); \
            i++; K1_STEP(W, xb, xa, xissue(i + 3), WAIT_V(6)); i--; \
        } \
        _Pragma("unroll 1") for (int i = 6; i < 18; i += 2) { \
            K1_STEP(W, xa, xb, , WAIT_V(0)); \
            i++; K1_STEP(W, xb, xa, , WAIT_V(0)); i--; \
        } }
        if (wave == 0) K1_LOOPS(0) else if (wave == 1) K1_LOOPS(1) else if (wave == 2) K1_LOOPS(2) else K1_LOOPS(3)
        if (wave < 3 && q == g) {
#pragma unroll
            for (int t = 0; t < 2; t++) {
                if (t == 0 || wave > 0) {
                    const float mean = ssum[t][p] * (1.f / 576.f);
                    const float var = fmaxf(ssq[t][p] * (1.f / 576.f) - mean * mean, 0.f);
                    const int r = (wave == 0 ? 0 : 2 * wave - 1 + t) * 16 + (lane & 15);
                    mus[r] = mean; mus[80 + r] = rsqrtf(var + 1e-5f);
                }
            }
        }
        const u4v* pcw = (const u4v*)(w1 + (size_t)(rthird * 192 + wave * 48 + (lane >> 3)) * F_ + cchunk * 64) + (lane & 7);
#pragma unroll
        for (int j = 0; j < 6; j++) cw[j] = pcw[(size_t)j * 8 * F_ / 8];
    }
    __syncthreads();
    constexpr int RSB = 456;
    if (wave < 4) {
        const int nt0 = wave == 0 ? 0 : 1 + 3 * wave;
        const int nn = (wave == 0 || wave == 3) ? 4 : 3;
        int lco[4][2];
#pragma unroll
        for (int n = 0; n < 4; n++)
#pragma unroll
            for (int h2 = 0; h2 < 2; h2++) {
                const int c = (nt0 + n) * 16 + 4 * g + 2 * h2;
                const int hc = c - 144, hd = hc / 18;
                lco[n][h2] = (n < nn && c < 216) ? (c < 144 ? c : 144 + hd * 20 + (hc - hd * 18)) * 2 : -1;
            }
        float mean[5], rs[5];
#pragma unroll
        for (int m = 0; m < 5; m++) { mean[m] = mus[m * 16 + (lane & 15)]; rs[m] = mus[80 + m * 16 + (lane & 15)]; }
        const int dump = K1_RED + 1024 + lane * 4;
#pragma unroll
        for (int m = 0; m < 5; m++) {
            const int rowo = (m * 16 + (lane & 15)) * RSB;
            const bool mok = m > 0 || wave > 0;
#pragma unroll
            for (int n = 0; n < 4; n++) {
                const f4v v = (acc[m][n] - acs[n] * mean[m]) * rs[m];
                *(unsigned*)(smem + (mok && lco[n][0] >= 0 ? rowo + lco[n][0] : dump)) = pack_bf2(v[0], v[1]);
                *(unsigned*)(smem + (mok && lco[n][1] >= 0 ? rowo + lco[n][1] : dump)) = pack_bf2(v[2], v[3]);
            }
        }
    }
    __syncthreads();
    if (wave == 4 && blockIdx.x < 144) {
        WAIT_V(0);
        const char* sb = smem + K1_DUMMY + (8 * g + q) * 144 + 8 * p;
        char* dst = wof + (size_t)blockIdx.x * 5 * 1024 + lane * 16;
#pragma unroll
        for (int nt = 0; nt < 5; nt++)
            *(s8v*)(dst + nt * 1024) = cat8(tr_read(sb + nt * 32), tr_read(sb + 4 * 144 + nt * 32));
    }
    {
        const int head = wave >> 1, li = lane & 15;
        const unsigned qmask0 = g < 3 ? 0xffffffffu : 0u, qmask1 = g < 2 ? 0xffffffffu : 0u;
        const int t0 = (wave & 1) * 2;
        u4v qf[2], kf[3];
        s8v vf0[2], vf1[2];
#pragma unroll
        for (int uu = 0; uu < 2; uu++) {
            const char* qp = smem + (16 + 16 * (t0 + uu) + li) * RSB + (head * 18 + 8 * g) * 2;
#pragma unroll
            for (int i = 0; i < 4; i++) qf[uu][i] = *(const unsigned*)(qp + 4 * i) & (i == 0 ? qmask0 : qmask1);
        }
#pragma unroll
        for (int kt = 0; kt < 3; kt++) {
            const char* kp = smem + (16 * (t0 + kt) + li) * RSB + (72 + head * 18 + 8 * g) * 2;
#pragma unroll
            for (int i = 0; i < 4; i++) kf[kt][i] = *(const unsigned*)(kp + 4 * i);
        }
#pragma unroll
        for (int uu = 0; uu < 2; uu++) {
            const char* vp = smem + (16 * (t0 + uu) + 4 * g + q) * RSB + (144 + head * 20 + 4 * p) * 2;
            vf0[uu] = cat8(tr_read(vp), tr_read(vp + 16 * RSB));
            vf1[uu] = cat8(tr_read(vp + 32), tr_read(vp + 16 * RSB + 32));
        }
        const f4v z4 = f4v{0.f, 0.f, 0.f, 0.f};
        f4v sv[2][2];
#pragma unroll
        for (int uu = 0; uu < 2; uu++)
#pragma unroll
            for (int tau = 0; tau < 2; tau++)
                sv[uu][tau] = MFMA16(__builtin_bit_cast(s8v, kf[uu + tau]), __builtin_bit_cast(s8v, qf[uu]), z4);
        bool ok[2][8]; float mx[2], den[2]; u4v pb[2];
#pragma unroll
        for (int uu = 0; uu < 2; uu++) {
            const bool tile0_ok = !(s0 == 0 && t0 + uu == 0);
            mx[uu] = -3.0e38f;
#pragma unroll
            for (int j = 0; j < 8; j++) {
                const int dl = 16 * (j >> 2) + 4 * g + (j & 3) - li;
                ok[uu][j] = !(dl & 1) && dl >= 2 && dl <= 16 && (j >= 4 || tile0_ok);
                mx[uu] = fmaxf(mx[uu], ok[uu][j] ? sv[uu][j >> 2][j & 3] : -3.0e38f);
            }
        }
#pragma unroll
        for (int uu = 0; uu < 2; uu++) mx[uu] = xmax4(mx[uu]);
#pragma unroll
        for (int uu = 0; uu < 2; uu++) {
            float a[8];
#pragma unroll
            for (int j = 0; j < 8; j++) {
                const float e = __builtin_amdgcn_exp2f((sv[uu][j >> 2][j & 3] - mx[uu]) * (0.23570226039551584f * 1.4426950408889634f));
                a[j] = ok[uu][j] ? e : 0.f;
            }
            pb[uu] = u4v{pack_bf2(a[0], a[1]), pack_bf2(a[2], a[3]), pack_bf2(a[4], a[5]), pack_bf2(a[6], a[7])};
            den[uu] = 0.f;
#pragma unroll
            for (int j = 0; j < 4; j++) den[uu] += bf_lo(pb[uu][j]) + bf_hi(pb[uu][j]);
        }
#pragma unroll
        for (int uu = 0; uu < 2; uu++) den[uu] = 1.f / xsum4(den[uu]);
#pragma unroll
        for (int uu = 0; uu < 2; uu++) {
            const f4v o0 = MFMA16(vf0[uu], __builtin_bit_cast(s8v, pb[uu]), z4) * den[uu];
            const f4v o1 = MFMA16(vf1[uu], __builtin_bit_cast(s8v, pb[uu]), z4) * den[uu];
            char* ob = of + (size_t)((s0 >> 4) + t0 + uu) * (18 * 1024) + li * 16;
            const int c0 = hg * 72 + head * 18 + 4 * g, c1 = c0 + 2, c2 = hg * 72 + head * 18 + 16;
            *(unsigned*)(ob + (c0 >> 5) * 1024 + ((c0 & 31) >> 3) * 256 + (c0 & 7) * 2) = pack_bf2(o0[0], o0[1]);
            *(unsigned*)(ob + (c1 >> 5) * 1024 + ((c1 & 31) >> 3) * 256 + (c1 & 7) * 2) = pack_bf2(o0[2], o0[3]);
            if (g == 0) *(unsigned*)(ob + (c2 >> 5) * 1024 + ((c2 & 31) >> 3) * 256 + (c2 & 7) * 2) = pack_bf2(o1[0], o1[1]);
        }
    }
    if (wave < 4) {
        float a[8];
#pragma unroll
        for (int j = 0; j < 8; j++) a[j] = 0.f;
#pragma unroll
        for (int i = 0; i < 6; i++)
#pragma unroll
            for (int j = 0; j < 4; j++) { a[2 * j] += bf_lo(cw[i][j]); a[2 * j + 1] += bf_hi(cw[i][j]); }
#pragma unroll
        for (int j = 0; j < 8; j++) {
            float t = a[j];
            t += __shfl_xor(t, 8); t += __shfl_xor(t, 16); t += __shfl_xor(t, 32);
            a[j] = t;
        }
        if (lane < 8 && blockIdx.x < 216) {
            float* rq = cs1 + (rthird * 4 + wave) * F_ + cchunk * 64 + lane * 8;
            *(f4v*)rq = f4v{a[0], a[1], a[2], a[3]};
            *(f4v*)(rq + 4) = f4v{a[4], a[5], a[6], a[7]};
        }
    }
}

constexpr int K2_LDS = 8 * 12 * 1024;

__global__ __launch_bounds__(512, 2) void k2_wo(const char* __restrict__ of, const char* __restrict__ wof,
                                                const bf16_t* __restrict__ x,
                                                char* __restrict__ hT, f2v* __restrict__ stats) {
    extern __shared__ __attribute__((aligned(16))) char smem[];
    const int tid = threadIdx.x, lane = tid & 63;
    const int wave = __builtin_amdgcn_readfirstlane(tid >> 6);
    const int rt = (blockIdx.x & 7) * 4 + ((blockIdx.x >> 3) & 3), ct = blockIdx.x >> 5;
    const int r0 = rt * 64, c0 = ct * 72;
    const int g = lane >> 4;
    const int kq = wave & 3, nh = wave >> 2, nb = nh * 3;
    const char* pa = of + ((size_t)rt * 4 * 18 + kq) * 1024 + lane * 16;
    const char* pw = wof + (((size_t)ct * 18 + kq) * 5 + nb) * 1024 + lane * 16;
    s8v fa[5][4], fw[5][3];
#pragma unroll
    for (int j = 0; j < 5; j++) {
        if (j < 4 || kq < 2) {
#pragma unroll
            for (int m = 0; m < 4; m++) fa[j][m] = *(const s8v*)(pa + m * (18 * 1024) + j * 4096);
#pragma unroll
            for (int n = 0; n < 3; n++) if (n < 2 || nh == 0) fw[j][n] = *(const s8v*)(pw + n * 1024 + j * 20480);
        }
    }
    const int row = r0 + (wave & 3) * 16 + (lane & 15);
    u2v xx[3];
#pragma unroll
    for (int n = 0; n < 3; n++) {
        int col = (nb + n) * 16 + 4 * g; if (col > 68) col = 68;
        xx[n] = *(const u2v*)(x + (size_t)row * D_ + c0 + col);
    }
    f4v acc[4][3];
#pragma unroll
    for (int m = 0; m < 4; m++)
#pragma unroll
        for (int n = 0; n < 3; n++) acc[m][n] = f4v{0, 0, 0, 0};
#pragma unroll
    for (int j = 0; j < 5; j++) {
        if (j < 4 || kq < 2) {
#pragma unroll
            for (int n = 0; n < 3; n++)
                if (n < 2 || nh == 0) {
#pragma unroll
                    for (int m = 0; m < 4; m++) acc[m][n] = MFMA16(fw[j][n], fa[j][m], acc[m][n]);
                }
        }
    }
    f4v* ex = (f4v*)smem + wave * (12 * 64) + lane;
#pragma unroll
    for (int m = 0; m < 4; m++)
#pragma unroll
        for (int n = 0; n < 3; n++) if (n < 2 || nh == 0) ex[(m * 3 + n) * 64] = acc[m][n];
    __syncthreads();
    float hv[3][4];
    float sum = 0.f;
#pragma unroll
    for (int n = 0; n < 3; n++) {
        const f4v* e = (const f4v*)smem + (nh * 4 * 12 + (wave & 3) * 3 + n) * 64 + lane;
        const int col = (nb + n) * 16 + 4 * g;
        if (col < 72) {
            f4v a = (e[0] + e[768]) + (e[2 * 768] + e[3 * 768]);
            unsigned p0 = pack_bf2(bf_lo(xx[n][0]) + a[0], bf_hi(xx[n][0]) + a[1]);
            unsigned p1 = pack_bf2(bf_lo(xx[n][1]) + a[2], bf_hi(xx[n][1]) + a[3]);
            {
                int cc = c0 + col;
                *(u2v*)(hT + (size_t)((row >> 4) * 18 + (cc >> 5)) * 1024 + (((cc & 31) >> 3) * 16 + (row & 15)) * 16 + (cc & 7) * 2) = u2v{p0, p1};
            }
            hv[n][0] = bf_lo(p0); hv[n][1] = bf_hi(p0); hv[n][2] = bf_lo(p1); hv[n][3] = bf_hi(p1);
            sum += hv[n][0] + hv[n][1] + hv[n][2] + hv[n][3];
        } else {
            hv[n][0] = hv[n][1] = hv[n][2] = hv[n][3] = 0.f;
        }
    }
    sum = xsum4(sum);
    const float mean = sum * (nh == 0 ? 1.f / 48.f : 1.f / 24.f);
    float m2 = 0.f;
#pragma unroll
    for (int n = 0; n < 3; n++) {
        const int col = (nb + n) * 16 + 4 * g;
        if (col < 72) {
#pragma unroll
            for (int j = 0; j < 4; j++) { float d = hv[n][j] - mean; m2 += d * d; }
        }
    }
    m2 = xsum4(m2);
    if (g == 0) stats[((size_t)row * 8 + ct) * 2 + nh] = f2v{sum, m2};
}

constexpr int W2F_TAIL = 8 * 144 * 4 * 1024;
constexpr int K3_STAGE = 8192 + 18432;
constexpr int K3_NB = 5;
constexpr int K3_PAR = K3_NB * K3_STAGE;
constexpr int K3_LDS = K3_PAR + 1024 + 2304;

__device__ __forceinline__ float gelu_tanh(float v) {
    float t = v * v;
    float z = v * (-2.302208198f * 0.044715f * t - 2.302208198f);
    float e = __builtin_amdgcn_exp2f(z);
    return v * __builtin_amdgcn_rcpf(1.f + e);
}

__device__ __forceinline__ f2v gelu_tanh2(f2v v) {
    f2v t = v * v;
    f2v z = v * (t * (-2.302208198f * 0.044715f) + (-2.302208198f));
    f2v d = f2v{__builtin_amdgcn_exp2f(z[0]), __builtin_amdgcn_exp2f(z[1])} + 1.f;
    return v * f2v{__builtin_amdgcn_rcpf(d[0]), __builtin_amdgcn_rcpf(d[1])};
}

__global__ __launch_bounds__(512, 2) void k3_up(const char* __restrict__ hT, const bf16_t* __restrict__ w1,
                                                const bf16_t* __restrict__ b1, const float* __restrict__ cs1,
                                                const f2v* __restrict__ stats, const bf16_t* __restrict__ w2,
                                                char* __restrict__ h1f, char* __restrict__ w2f) {
    extern __shared__ __attribute__((aligned(16))) char smem[];
    const int tid = threadIdx.x, lane = tid & 63;
    const int wave = __builtin_amdgcn_readfirstlane(tid >> 6);
    const int xcd = blockIdx.x & 7, idx = blockIdx.x >> 3;
    const int rt = (xcd & 1) * 8 + (idx & 7), ct = (xcd >> 1) * 4 + (idx >> 3);
    const int r0 = rt * 128, c0 = ct * 288;
    const unsigned lbase = lds_addr(smem);
    const int g = lane >> 4, q = (lane & 15) >> 2, p = lane & 3;
    const int xr = lane & 15;
    f2v* mu = (f2v*)(smem + K3_PAR);
    float* csl = (float*)(smem + K3_PAR + 1024);
    float* bbl = csl + 288;
    f4v acc[4][9];
    const int wm = (wave >> 1) & 1, wn = wave & 1;
    if (wave >= 4) {
        const int lw = wave - 4;
        unsigned bsrc[5];
        const unsigned avo = lane * 16;
        const char* ag = hT + (size_t)(rt * 8 + lw * 2) * 18 * 1024;
#pragma unroll
        for (int i = 0; i < 5; i++) {
            int bi = lw * 5 + i; if (bi > 17) bi = 17;
            int off = bi * 1024 + lane * 16;
            int k = off / 576, r = off - k * 576;
            int nt = (r >> 5) ^ ((k >> 3) & 1);
            bsrc[i] = (unsigned)((k * F_ + c0 + nt * 16 + ((r >> 4) & 1) * 8) * 2);
        }
        auto issue = [&](int st) {
            int sc = st > 17 ? 17 : st;
            unsigned lb = lbase + (st % K3_NB) * K3_STAGE;
            const char* ga = ag + sc * 1024;
            const char* gb = (const char*)w1 + (size_t)sc * 32 * F_ * 2;
            glds16s(ga, avo, lb + (lw * 2) * 1024);
            glds16s(ga + 18 * 1024, avo, lb + (lw * 2 + 1) * 1024);
#pragma unroll
            for (int i = 0; i < 5; i++) glds16s(gb, bsrc[i], lb + 8192 + (lw * 5 + i > 17 ? 17 : lw * 5 + i) * 1024);
        };
#pragma unroll
        for (int st = 0; st < K3_NB - 1; st++) issue(st);
        WAIT_V(21);
        BAR;
        for (int i = -1; i < 14; i++) {
            if (i >= 0) issue(i + K3_NB - 1);
            WAIT_V(14);
            BAR;
        }
        WAIT_V(7); BAR;
        WAIT_V(0); BAR;
#pragma unroll
        for (int uu = 0; uu < 2; uu++) {
            if (uu == 0 || (lw == 0 && blockIdx.x < 128)) {
                const int u = uu * 1024 + lw * 256 + blockIdx.x;
                const int ctp = u / 144, kst = u - ctp * 144;
#pragma unroll
                for (int j = 0; j < 5; j++) {
                    int c = j * 64 + lane; if (c > 287) c = 287;
                    int row = c / 9, pc = c - row * 9;
                    glds16s(w2, (unsigned)(((kst * 32 + row) * D_ + ctp * 72 + pc * 8) * 2), lbase + 3 * K3_STAGE + (uu * 4 + lw) * 5120 + j * 1024);
                }
            }
        }
        BAR; BAR;
    } else {
        {
            float c0v = 0.f, b0v = bf2f(b1[c0 + tid]);
            float c1v = 0.f, b1v = 0.f;
            {
                float t0[12], t1[12];
                const int c1i = c0 + 256 + (tid & 31);
#pragma unroll
                for (int i = 0; i < 12; i++) { t0[i] = cs1[i * F_ + c0 + tid]; t1[i] = cs1[i * F_ + c1i]; }
                b1v = bf2f(b1[c1i]);
#pragma unroll
                for (int i = 0; i < 12; i++) { c0v += t0[i]; c1v += t1[i]; }
            }
            if (tid < 128) {
                const f4v* sp = (const f4v*)(stats + (size_t)(r0 + tid) * 16);
                f4v v[8];
#pragma unroll
                for (int i = 0; i < 8; i++) v[i] = sp[i];
                float tot = 0.f;
#pragma unroll
                for (int i = 0; i < 8; i++) tot += v[i][0] + v[i][2];
                float mean = tot * (1.f / 576.f);
                float M2 = 0.f;
#pragma unroll
                for (int i = 0; i < 8; i++) {
                    float d0 = v[i][0] * (1.f / 48.f) - mean, d1 = v[i][2] * (1.f / 24.f) - mean;
                    M2 += v[i][1] + v[i][3] + (48.f * d0 * d0 + 24.f * d1 * d1);
                }
                float rstd = rsqrtf(M2 * (1.f / 576.f) + 1e-5f);
                mu[tid] = f2v{mean * rstd, rstd};
            }
            csl[tid] = c0v; bbl[tid] = b0v;
            if (tid < 32) { csl[256 + tid] = c1v; bbl[256 + tid] = b1v; }
        }
#pragma unroll
        for (int m = 0; m < 4; m++)
#pragma unroll
            for (int n = 0; n < 9; n++) acc[m][n] = f4v{0, 0, 0, 0};
        const int dlt = (g & 1) ? 32 : 0;
        const int wb0 = 8192 + (8 * g + q) * 576 + wn * 9 * 32 + 8 * p;
        const int wbe = wb0 + ((wn & 1) ? -dlt : dlt);
        const int wbo = wb0 + ((wn & 1) ? dlt : -dlt);
        const int xoff = wm * 4096 + lane * 16;
        s8v xa[4], xb[4], wf[9];
        BAR;
        {
            const char* base = smem;
#pragma unroll
            for (int m = 0; m < 4; m++) xa[m] = *(const s8v*)(base + xoff + m * 1024);
#pragma unroll
            for (int n = 0; n < 9; n++) {
                const char* wb = base + ((n & 1) ? wbo : wbe) + n * 32;
                wf[n] = cat8(tr_read(wb), tr_read(wb + 4 * 576));
            }
        }
        WAIT_L(0);
        BAR;
#define K3_STEP(XC, XN) { \
            const char* base = smem + ((i + 1) % K3_NB) * K3_STAGE; \
            s4v wlo, whi; \
            _Pragma("unroll") for (int n = 0; n < 9; n++) { \
                const char* wbp = base + (((n - 1) & 1) ? wbo : wbe) + (n - 1) * 32; \
                acc[0][n] = MFMA16(wf[n], XC[0], acc[0][n]); SB0; \
                if (n > 0) { wlo = tr_read(wbp); SB0; } \
                acc[1][n] = MFMA16(wf[n], XC[1], acc[1][n]); SB0; \
                if (n > 0) { whi = tr_read(wbp + 4 * 576); wf[n - 1] = cat8(wlo, whi); SB0; } \
                acc[2][n] = MFMA16(wf[n], XC[2], acc[2][n]); SB0; \
                if (n < 4) { XN[n] = *(const s8v*)(base + xoff + n * 1024); SB0; } \
                acc[3][n] = MFMA16(wf[n], XC[3], acc[3][n]); SB0; \
            } \
            { const char* wbp = base + wbe + 8 * 32; wf[8] = cat8(tr_read(wbp), tr_read(wbp + 4 * 576)); } \
            BAR; }
#pragma unroll 1
        for (int i = 0; i < 18; i += 2) {
            K3_STEP(xa, xb);
            i++; K3_STEP(xb, xa); i--;
        }
    }
    __syncthreads();
    f4v* ex = (f4v*)smem + (wave & 3) * (18 * 64) + lane;
    if (wave < 4) {
#pragma unroll
        for (int m = 0; m < 2; m++)
#pragma unroll
            for (int n = 0; n < 9; n++) ex[(m * 9 + n) * 64] = acc[m + 2][n];
    }
    __syncthreads();
    if (wave >= 4) {
        __builtin_amdgcn_s_setprio(2);
#pragma unroll
        for (int m = 0; m < 2; m++)
#pragma unroll
            for (int n = 0; n < 9; n++) acc[m][n] = ex[(m * 9 + n) * 64];
    }
    {
        const int mb = (wave >> 2) * 2;
#pragma unroll
        for (int m = 0; m < 2; m++) {
            int rl = wm * 64 + (mb + m) * 16 + xr;
            f2v ms = mu[rl];
            char* ob = h1f + (size_t)((r0 + wm * 64 + (mb + m) * 16) >> 4) * (144 * 1024) + lane * 16;
            u2v pv[9];
#pragma unroll
            for (int n = 0; n < 9; n++) {
                f4v a = acc[m][n];
                int cl = wn * 144 + n * 16 + 4 * g;
                f4v cs = *(const f4v*)(csl + cl), bb = *(const f4v*)(bbl + cl);
                f2v t01 = f2v{bb[0], bb[1]} - f2v{cs[0], cs[1]} * ms[0];
                f2v t23 = f2v{bb[2], bb[3]} - f2v{cs[2], cs[3]} * ms[0];
                f2v v01 = gelu_tanh2(f2v{a[0], a[1]} * ms[1] + t01);
                f2v v23 = gelu_tanh2(f2v{a[2], a[3]} * ms[1] + t23);
                pv[n] = u2v{pack_bf2(v01[0], v01[1]), pack_bf2(v23[0], v23[1])};
            }
            if (m == 0) {
            } else {
            }
            char* oc = ob + (c0 >> 5) * 1024;
            if (wn == 0) {
#pragma unroll
                for (int j = 0; j < 4; j++) *(u4v*)(oc + j * 1024) = u4v{pv[2 * j][0], pv[2 * j][1], pv[2 * j + 1][0], pv[2 * j + 1][1]};
                *(u2v*)(oc + 4 * 1024) = pv[8];
            } else {
                *(u2v*)(oc + 4 * 1024 + 8) = pv[0];
#pragma unroll
                for (int j = 0; j < 4; j++) *(u4v*)(oc + (5 + j) * 1024) = u4v{pv[2 * j + 1][0], pv[2 * j + 1][1], pv[2 * j + 2][0], pv[2 * j + 2][1]};
            }
        }
    }
    if (wave >= 4) {
        WAIT_V(0);
#pragma unroll
        for (int uu = 0; uu < 2; uu++) {
            if (uu == 0 || (wave == 4 && blockIdx.x < 128)) {
                const int u = uu * 1024 + (wave - 4) * 256 + blockIdx.x;
                const char* sb = smem + 3 * K3_STAGE + (uu * 4 + wave - 4) * 5120 + (4 * g + q) * 144 + 8 * p;
                char* dst = w2f + (size_t)u * 4 * 1024 + lane * 16;
#pragma unroll
                for (int nt = 0; nt < 4; nt++)
                    *(s8v*)(dst + nt * 1024) = cat8(tr_read(sb + nt * 32), tr_read(sb + 16 * 144 + nt * 32));
                const int ctp = u / 144, kst = u - ctp * 144;
                const s8v t4 = cat8(tr_read(sb + 4 * 32), tr_read(sb + 16 * 144 + 4 * 32));
                if ((lane & 15) < 8)
                    *(s8v*)(w2f + W2F_TAIL + (size_t)((ctp * 8 + (kst & 7)) * 9 + (kst >> 4)) * 1024 + (g * 16 + ((kst >> 3) & 1) * 8 + (lane & 7)) * 16) = t4;
            }
        }
    }
}

constexpr int K4_LDS = 8 * 20 * 1024;
constexpr int K4_D = 3;

__global__ __launch_bounds__(512, 2) void k4_down(const char* __restrict__ h1f, const char* __restrict__ w2f,
                                                  const char* __restrict__ hT, const bf16_t* __restrict__ b2,
                                                  bf16_t* __restrict__ out) {
    extern __shared__ __attribute__((aligned(16))) char smem[];
    const int tid = threadIdx.x, lane = tid & 63;
    const int wave = __builtin_amdgcn_readfirstlane(tid >> 6);
    const int xcd = blockIdx.x & 7, idx = blockIdx.x >> 3;
    const int rt = (xcd >> 1) * 8 + (idx & 7), ct = (xcd & 1) * 4 + (idx >> 3);
    const int g = lane >> 4;
    const char* pa = h1f + ((size_t)rt * 4 * 144 + wave) * 1024 + lane * 16;
    const char* pw = w2f + ((size_t)ct * 144 + wave) * 4 * 1024 + lane * 16;
    const char* pt = w2f + W2F_TAIL + (size_t)(ct * 8 + wave) * 9 * 1024 + lane * 16;
    f4v acc[4][4], accA[4], accB[4];
#pragma unroll
    for (int m = 0; m < 4; m++) {
#pragma unroll
        for (int n = 0; n < 4; n++) acc[m][n] = f4v{0, 0, 0, 0};
        accA[m] = f4v{0, 0, 0, 0}; accB[m] = f4v{0, 0, 0, 0};
    }
    s8v fa[K4_D][4], fw[K4_D][4], ft[2];
#pragma unroll
    for (int j = 0; j < K4_D; j++) {
#pragma unroll
        for (int m = 0; m < 4; m++) fa[j][m] = *(const s8v*)(pa + m * (144 * 1024) + j * 8192);
#pragma unroll
        for (int n = 0; n < 4; n++) fw[j][n] = *(const s8v*)(pw + n * 1024 + j * 32768);
        if ((j & 1) == 0) ft[(j >> 1) & 1] = *(const s8v*)(pt + (j >> 1) * 1024);
        SB0;
    }
#pragma unroll
    for (int j = 0; j < 18; j++) {
        const int sl = j % K4_D;
        SB0;
#pragma unroll
        for (int n = 0; n < 4; n++)
#pragma unroll
            for (int m = 0; m < 4; m++) acc[m][n] = MFMA16(fw[sl][n], fa[sl][m], acc[m][n]);
#pragma unroll
        for (int m = 0; m < 4; m++) {
            if ((j & 1) == 0) accA[m] = MFMA16(ft[(j >> 1) & 1], fa[sl][m], accA[m]);
            else accB[m] = MFMA16(ft[(j >> 1) & 1], fa[sl][m], accB[m]);
        }
        SB0;
        if (j + K4_D < 18) {
            const int jn = j + K4_D;
#pragma unroll
            for (int m = 0; m < 4; m++) fa[sl][m] = *(const s8v*)(pa + m * (144 * 1024) + jn * 8192);
#pragma unroll
            for (int n = 0; n < 4; n++) fw[sl][n] = *(const s8v*)(pw + n * 1024 + jn * 32768);
            if ((jn & 1) == 0) ft[(jn >> 1) & 1] = *(const s8v*)(pt + (jn >> 1) * 1024);
        }
        SB0;
    }
    f4v acc4[4];
#pragma unroll
    for (int m = 0; m < 4; m++)
#pragma unroll
        for (int r = 0; r < 4; r++) {
            float t = accB[m][r], u = accB[m][r];
            asm volatile("s_nop 1\n\tv_permlane32_swap_b32 %0, %1" : "+v"(t), "+v"(u));
            acc4[m][r] = accA[m][r] + u;
        }
    u2v hh[3], bb[3];
#pragma unroll
    for (int j = 0; j < 3; j++) {
        int t = wave + 8 * j; if (t > 19) t = 19;
        const int m = t / 5, n = t - m * 5;
        const int row = rt * 64 + m * 16 + (lane & 15);
        int cl = n * 16 + 4 * g; if (cl > 68) cl = 68;
        const int cc = ct * 72 + cl;
        hh[j] = *(const u2v*)(hT + (size_t)((row >> 4) * 18 + (cc >> 5)) * 1024 + (((cc & 31) >> 3) * 16 + (row & 15)) * 16 + (cc & 7) * 2);
        bb[j] = *(const u2v*)(b2 + ct * 72 + cl);
    }
    f4v* ex = (f4v*)smem + wave * (20 * 64) + lane;
#pragma unroll
    for (int m = 0; m < 4; m++) {
#pragma unroll
        for (int n = 0; n < 4; n++) ex[(m * 5 + n) * 64] = acc[m][n];
        ex[(m * 5 + 4) * 64] = acc4[m];
    }
    __syncthreads();
#pragma unroll
    for (int j = 0; j < 3; j++) {
        const int t = wave + 8 * j;
        if (t < 20) {
            const int m = t / 5, n = t - m * 5;
            const f4v* e = (const f4v*)smem + t * 64 + lane;
            f4v a = ((e[0] + e[1280]) + (e[2 * 1280] + e[3 * 1280])) + ((e[4 * 1280] + e[5 * 1280]) + (e[6 * 1280] + e[7 * 1280]));
            const int row = rt * 64 + m * 16 + (lane & 15);
            const int cl = n * 16 + 4 * g;
            float v0 = bf_lo(hh[j][0]) + (a[0] + bf_lo(bb[j][0]));
            float v1 = bf_hi(hh[j][0]) + (a[1] + bf_hi(bb[j][0]));
            float v2 = bf_lo(hh[j][1]) + (a[2] + bf_lo(bb[j][1]));
            float v3 = bf_hi(hh[j][1]) + (a[3] + bf_hi(bb[j][1]));
            if (cl < 72) *(u2v*)(out + (size_t)row * D_ + ct * 72 + cl) = u2v{pack_bf2(v0, v1), pack_bf2(v2, v3)};
        }
    }
}

extern "C" void kernel_launch(void* const* d_in, const int* in_sizes, int n_in,
                              void* d_out, int out_size, void* d_ws, size_t ws_size,
                              hipStream_t stream) {
    const bf16_t* x = (const bf16_t*)d_in[0];
    const bf16_t* wqkv = (const bf16_t*)d_in[1];
    const bf16_t* wo = (const bf16_t*)d_in[2];
    const bf16_t* w1 = (const bf16_t*)d_in[4];
    const bf16_t* b1 = (const bf16_t*)d_in[5];
    const bf16_t* w2 = (const bf16_t*)d_in[6];
    const bf16_t* b2 = (const bf16_t*)d_in[7];
    bf16_t* out = (bf16_t*)d_out;
    char* ws = (char*)d_ws;
    char* of = ws;
    char* wof = ws + (3 << 20);
    char* hT = ws + (12 << 20);
    f2v* stats = (f2v*)(ws + (8 << 20));
    float* cs1 = (float*)(ws + (9 << 20));
    char* h1 = (char*)(ws + (16 << 20));
    char* w2f = ws + (48 << 20);

    hipFuncSetAttribute((const void*)k1_qkv_attn, hipFuncAttributeMaxDynamicSharedMemorySize, K1_LDS);
    hipFuncSetAttribute((const void*)k2_wo, hipFuncAttributeMaxDynamicSharedMemorySize, K2_LDS);
    hipFuncSetAttribute((const void*)k3_up, hipFuncAttributeMaxDynamicSharedMemorySize, K3_LDS);
    hipFuncSetAttribute((const void*)k4_down, hipFuncAttributeMaxDynamicSharedMemorySize, K4_LDS);

    k1_qkv_attn<<<256, 512, K1_LDS, stream>>>(x, wqkv, w1, wo, of, wof, cs1);
    k2_wo<<<256, 512, K2_LDS, stream>>>(of, wof, x, hT, stats);
    k3_up<<<256, 512, K3_LDS, stream>>>(hT, w1, b1, cs1, stats, w2, h1, w2f);
    k4_down<<<256, 512, K4_LDS, stream>>>(h1, w2f, hT, b2, out);
}
```
